```python
import torch

def _softmax(x, axis=-1):
    return torch.softmax(x, dim=axis)

def _gelu(x):
    return torch.nn.functional.gelu(x, approximate='tanh')

def _silu(x):
    return torch.nn.functional.silu(x)

def _sigmoid(x):
    return torch.sigmoid(x)

def _layernorm(x, axis=-1, eps=1e-05):
    m = x.mean(dim=axis, keepdim=True)
    v = x.var(dim=axis, keepdim=True, unbiased=False)
    return (x - m) / torch.sqrt(v + eps)

def _scatter_add(tgt, idx, src, axis=0):
    return tgt.clone().index_add_(axis, idx, src)

def _segment_sum(src, seg, n):
    out = torch.zeros((n,) + src.shape[1:], dtype=src.dtype)
    return out.index_add_(0, seg, src)

def _pad(x, pw):
    flat = tuple((v for pair in reversed(pw) for v in pair))
    return torch.nn.functional.pad(x, flat)
kernel_name = 'adaLN_cond__sliding_local_dilated__swiglu_ffn__prompt'

def setup_inputs():

    _w = lambda a: (lambda t: t.to(torch.bfloat16) if t.is_floating_point() else t)(torch.as_tensor(a))

    def _A_setup():
        B, S, D = (1, 2048, 1024)
        return {'x': _w((lambda *s: torch.randn(*s, dtype=torch.bfloat16) * 0.05)(B, S, D)), 'wqkv': _w((lambda *s: torch.randn(*s, dtype=torch.bfloat16) * 0.05)(D, 3 * D)), 'wo': _w((lambda *s: torch.randn(*s, dtype=torch.bfloat16) * 0.05)(D, D))}
    _A_in = _A_setup()

    def _B_setup():
        B, S, D, F = (1, 2048, 1024, 3584)
        return {'x': _w((lambda *s: torch.randn(*s, dtype=torch.bfloat16) * 0.05)(B, S, D)), 'w_gate': _w((lambda *s: torch.randn(*s, dtype=torch.bfloat16) * 0.05)(D, F)), 'w_up': _w((lambda *s: torch.randn(*s, dtype=torch.bfloat16) * 0.05)(D, F)), 'w_down': _w((lambda *s: torch.randn(*s, dtype=torch.bfloat16) * 0.05)(F, D))}
    _B_in = _B_setup()
    return {'A_x': _A_in['x'], 'A_wqkv': _A_in['wqkv'], 'A_wo': _A_in['wo'], 'B_x': _B_in['x'], 'B_w_gate': _B_in['w_gate'], 'B_w_up': _B_in['w_up'], 'B_w_down': _B_in['w_down'], 'c': _w((lambda *s: torch.randn(*s, dtype=torch.bfloat16) * 0.1)(1, 1024) * 8.0), 'w_mod': _w((lambda *s: torch.randn(*s, dtype=torch.bfloat16) * 0.1)(1024, 6 * 1024) * 1024 ** (-0.5) * 8.0)}

def reference(A_x, A_wqkv, A_wo, B_x, B_w_gate, B_w_up, B_w_down, c, w_mod):

    def _A_ref(x, wqkv, wo):
        B, S, D = x.shape
        H = 32
        Dh = D // H
        W, dil = (8, 2)
        qkv = (lambda x, *d: x.permute(*d))((x @ wqkv).reshape(B, S, 3, H, Dh), 2, 0, 3, 1, 4)
        q, k, v = (qkv[0], qkv[1], qkv[2])
        scale = Dh ** (-0.5)
        pos = torch.arange(S)
        off = torch.arange(W) * dil
        idx = pos[:, None] - off[None, :]
        mask = idx >= 0
        idx_c = torch.where(mask, idx, 0).reshape(-1)
        kg = k[:, :, idx_c, :].reshape(B, H, S, W, Dh)
        vg = v[:, :, idx_c, :].reshape(B, H, S, W, Dh)
        logits = torch.einsum('bhsd,bhswd->bhsw', q, kg) * scale
        logits = torch.where(mask[None, None, :, :], logits, -1000000000.0)
        attn = _softmax(logits, axis=-1)
        o = torch.einsum('bhsw,bhswd->bhsd', attn, vg)
        return (lambda x, *d: x.permute(*d))(o, 0, 2, 1, 3).reshape(B, S, D) @ wo

    def _B_ref(x, w_gate, w_up, w_down):
        g = _silu(x @ w_gate)
        u = x @ w_up
        return g * u @ w_down
    x = A_x
    mod = (_silu(c) @ w_mod).reshape(c.shape[0], 6, 1, -1)
    sa, ca, ga, sb, cb, gb = (mod[:, 0].clone(), mod[:, 1].clone(), mod[:, 2].clone(), mod[:, 3].clone(), mod[:, 4].clone(), mod[:, 5].clone())
    h = _layernorm(x, axis=-1) * (1 + ca) + sa
    x = x + ga * _A_ref(h, A_wqkv, A_wo)
    h = _layernorm(x, axis=-1) * (1 + cb) + sb
    x = x + gb * _B_ref(h, B_w_gate, B_w_up, B_w_down)
    return x
```

```cpp
#include <hip/hip_runtime.h>
#include <hip/hip_bf16.h>
#include <stdint.h>

typedef __hip_bfloat16 bf16;
typedef __attribute__((ext_vector_type(8))) short bf16x8;
typedef __attribute__((ext_vector_type(4))) float f32x4;
typedef __attribute__((ext_vector_type(2))) float f32x2;
typedef __attribute__((ext_vector_type(4))) unsigned u32x4;
typedef __attribute__((ext_vector_type(2))) unsigned u32x2;
typedef __attribute__((ext_vector_type(2))) __bf16 bf16x2_t;
typedef __amdgpu_buffer_rsrc_t rsrc_t;

#define DI __device__ __forceinline__
#define QBAR 2
#define QRN 17
#define TQ 16
#define TST 3
#define TPU 6
#define TG 4
#define QK0 0
#define QK0W "6"
#define CODEPF 55

#define STAMP(k, s) do { } while (0)
#define STAMPT(k, s, T) do { } while (0)
#define STAMPNW(k, s)
#define STAMPFLUSH(k, s)
#define STAMPDECL

constexpr int S_LEN = 2048;
constexpr int D_DIM = 1024;
constexpr int F_DIM = 3584;

template <int V> struct ic { static constexpr int value = V; };
template <int N, int I = 0, class F>
DI void static_for(F&& f) {
  if constexpr (I < N) { f(ic<I>{}); static_for<N, I + 1>(f); }
}

DI float bf_lo(unsigned u) { return __uint_as_float(u << 16); }
DI float bf_hi(unsigned u) { return __uint_as_float(u & 0xffff0000u); }
DI unsigned pk_bf16(float lo, float hi) {
  const f32x2 v = {lo, hi};
  return __builtin_bit_cast(unsigned, __builtin_convertvector(v, bf16x2_t));
}
DI rsrc_t mk_rsrc(const void* p, unsigned bytes) { return __builtin_amdgcn_make_buffer_rsrc((void*)p, (short)0, (int)bytes, 0x00020000); }

template <int N>
DI void wait_vm() { asm volatile("s_waitcnt vmcnt(%0)" ::"n"(N > 63 ? 63 : N) : "memory"); }
#define WAIT_LGKM0() do { asm volatile("s_waitcnt lgkmcnt(0)" ::: "memory"); __builtin_amdgcn_sched_barrier(0); } while (0)
#define SCHED0() __builtin_amdgcn_sched_barrier(0)

#define MFMA_A(C, B, A) asm volatile("v_mfma_f32_16x16x32_bf16 %0, %1, %2, %0" : "+a"(C) : "v"(B), "a"(A))
#define MFMA_AV(C, B, A) asm volatile("v_mfma_f32_16x16x32_bf16 %0, %1, %2, %0" : "+v"(C) : "v"(B), "v"(A))
#define PERM_LO(D, X, Y) asm volatile("v_perm_b32 %0, %1, %2, %3" : "=v"(D) : "v"(Y), "v"(X), "s"(0x05040100u))
#define PERM_HI(D, X, Y) asm volatile("v_perm_b32 %0, %1, %2, %3" : "=v"(D) : "v"(Y), "v"(X), "s"(0x07060302u))
template <int OFF>
DI void bload_v(u32x4& d, unsigned voff, rsrc_t rs, unsigned soff) {
  asm volatile("buffer_load_dwordx4 %0, %1, %2, %3 offen offset:%4" : "+v"(d) : "v"(voff), "s"(rs), "s"(soff), "i"(OFF) : "memory");
}
template <int OFF>
DI void bload_a(u32x4& d, unsigned voff, rsrc_t rs, unsigned soff) {
  asm volatile("buffer_load_dwordx4 %0, %1, %2, %3 offen offset:%4" : "+a"(d) : "v"(voff), "s"(rs), "s"(soff), "i"(OFF) : "memory");
}
template <int OFF>
DI void bload2_v(u32x2& d, unsigned voff, rsrc_t rs, unsigned soff) {
  asm volatile("buffer_load_dwordx2 %0, %1, %2, %3 offen offset:%4" : "+v"(d) : "v"(voff), "s"(rs), "s"(soff), "i"(OFF) : "memory");
}
template <int OFF>
DI void bload1_v(unsigned& d, unsigned voff, rsrc_t rs, unsigned soff) {
  asm volatile("buffer_load_dword %0, %1, %2, %3 offen offset:%4" : "+v"(d) : "v"(voff), "s"(rs), "s"(soff), "i"(OFF) : "memory");
}
template <int OFF>
DI void bload_lds(unsigned m0v, unsigned voff, rsrc_t rs, unsigned soff) {
  asm volatile("s_mov_b32 m0, %0\n\ts_nop 0\n\tbuffer_load_dwordx4 %1, %2, %3 offen offset:%4 lds" ::"s"(m0v), "v"(voff), "s"(rs), "s"(soff), "i"(OFF) : "memory");
}
template <int OFF>
DI void dsr128(u32x4& d, unsigned addr) { asm volatile("ds_read_b128 %0, %1 offset:%2" : "=v"(d) : "v"(addr), "i"(OFF) : "memory"); }
template <int OFF>
DI void dsr64(u32x2& d, unsigned addr) { asm volatile("ds_read_b64 %0, %1 offset:%2" : "=v"(d) : "v"(addr), "i"(OFF) : "memory"); }
template <int OFF>
DI void dsr_tr(u32x2& d, unsigned addr) { asm volatile("ds_read_b64_tr_b16 %0, %1 offset:%2" : "=v"(d) : "v"(addr), "i"(OFF) : "memory"); }
template <int OFF>
DI void dsw128(unsigned addr, u32x4 d) { asm volatile("ds_write_b128 %0, %1 offset:%2" ::"v"(addr), "v"(d), "i"(OFF) : "memory"); }
template <int OFF>
DI void dsw128a(unsigned addr, const f32x4& d) { asm volatile("ds_write_b128 %0, %1 offset:%2" ::"v"(addr), "a"(d), "i"(OFF) : "memory"); }
template <int OFF>
DI void dsw32(unsigned addr, unsigned d) { asm volatile("ds_write_b32 %0, %1 offset:%2" ::"v"(addr), "v"(d), "i"(OFF) : "memory"); }

DI void gst128(void* p, u32x4 v) {
  asm volatile("global_store_dwordx4 %0, %1, off sc1\n\ts_nop 1" ::"v"(p), "v"(v) : "memory");
}
DI f32x2 lo2(f32x4 v) { return __builtin_shufflevector(v, v, 0, 1); }
DI f32x2 hi2(f32x4 v) { return __builtin_shufflevector(v, v, 2, 3); }
DI float rowsum4(float v) {
  const unsigned u = __float_as_uint(v);
  const auto a = __builtin_amdgcn_permlane16_swap(u, u, false, false);
  const unsigned t = __float_as_uint(__uint_as_float(a[0]) + __uint_as_float(a[1]));
  const auto b = __builtin_amdgcn_permlane32_swap(t, t, false, false);
  return __uint_as_float(b[0]) + __uint_as_float(b[1]);
}
DI void split4(const f32x4 v, u32x2& hi, u32x2& lo) {
  hi[0] = pk_bf16(v[0], v[1]); hi[1] = pk_bf16(v[2], v[3]);
  lo[0] = pk_bf16(v[0] - bf_lo(hi[0]), v[1] - bf_hi(hi[0]));
  lo[1] = pk_bf16(v[2] - bf_lo(hi[1]), v[3] - bf_hi(hi[1]));
}
DI float silu_f(float v) { return v / (1.0f + __expf(-v)); }

__global__ __launch_bounds__(512) void pre_kernel(const bf16* __restrict__ x, const bf16* __restrict__ c,
                                                  const bf16* __restrict__ wmod, float* __restrict__ modp,
                                                  float* __restrict__ rstat1, const bf16* __restrict__ Wq) {
  __shared__ __attribute__((aligned(16))) float red[8 * 2 * 128 + 256];
  STAMP(0, 0);
  const int tid = threadIdx.x, lane = tid & 63, w = tid >> 6;
  if (blockIdx.x < 192) {
    const int fr = lane & 15, fq = lane >> 4;
    const int cg = blockIdx.x >> 2, kq = blockIdx.x & 3;
    const int col0 = cg * 128;
    const int k0 = kq * 256 + w * 32 + fq * 8;
    u32x4 raw[8];
#pragma unroll
    for (int j = 0; j < 8; ++j) {
      const u32x4* p_ = reinterpret_cast<const u32x4*>(wmod + (size_t)(k0 + j) * 6144 + col0 + fr * 8);
      raw[j] = __builtin_nontemporal_load(p_);
    }
    const u32x4 cv = *reinterpret_cast<const u32x4*>(c + k0);
    u32x4 cf;
#pragma unroll
    for (int e = 0; e < 4; ++e) {
      const float a = silu_f(bf_lo(cv[e])), b = silu_f(bf_hi(cv[e]));
      const unsigned hi = pk_bf16(a, b);
      const unsigned lo = pk_bf16(a - bf_lo(hi), b - bf_hi(hi));
      cf[e] = (fr == 0) ? hi : ((fr == 1) ? lo : 0u);
    }
    f32x4 acc[8];
#pragma unroll
    for (int p = 0; p < 4; ++p) {
      u32x4 lo, hi;
#pragma unroll
      for (int t = 0; t < 4; ++t) {
        lo[t] = __builtin_amdgcn_perm(raw[2 * t + 1][p], raw[2 * t][p], 0x05040100u);
        hi[t] = __builtin_amdgcn_perm(raw[2 * t + 1][p], raw[2 * t][p], 0x07060302u);
      }
      acc[2 * p] = __builtin_amdgcn_mfma_f32_16x16x32_bf16(__builtin_bit_cast(bf16x8, lo), __builtin_bit_cast(bf16x8, cf), f32x4{0, 0, 0, 0}, 0, 0, 0);
      acc[2 * p + 1] = __builtin_amdgcn_mfma_f32_16x16x32_bf16(__builtin_bit_cast(bf16x8, hi), __builtin_bit_cast(bf16x8, cf), f32x4{0, 0, 0, 0}, 0, 0, 0);
    }
    if (fr < 2) {
#pragma unroll
      for (int r = 0; r < 4; ++r) {
        float* d = red + (w * 2 + fr) * 128 + fq * 32 + r * 8;
        *reinterpret_cast<f32x4*>(d) = f32x4{acc[0][r], acc[1][r], acc[2][r], acc[3][r]};
        *reinterpret_cast<f32x4*>(d + 4) = f32x4{acc[4][r], acc[5][r], acc[6][r], acc[7][r]};
      }
    }
    __syncthreads();
    if (tid < 128) {
      float s = 0;
#pragma unroll
      for (int g = 0; g < 16; ++g) s += red[g * 128 + tid];
      modp[kq * 6144 + col0 + tid] = s;
    }
    STAMP(0, 1);
    return;
  }
  const int row0 = (blockIdx.x - 192) * 16 + w * 2;
  u32x4 a[2][2];
#pragma unroll
  for (int i = 0; i < 2; ++i) {
    a[i][0] = *reinterpret_cast<const u32x4*>(x + (size_t)(row0 + i) * D_DIM + lane * 8);
    a[i][1] = *reinterpret_cast<const u32x4*>(x + (size_t)(row0 + i) * D_DIM + 512 + lane * 8);
  }
  if (blockIdx.x < 192 + 8) {
    unsigned long long pc;
    asm volatile("s_getpc_b64 %0" : "=s"(pc));
    const rsrc_t rsC = mk_rsrc((const void*)(uintptr_t)(pc & ~255ull), CODEPF * 1024);
#pragma unroll
    for (int i = 0; i < (CODEPF + 7) / 8; ++i) bload_lds<0>((unsigned)(uintptr_t)(red + 2048), tid * 16, rsC, i * 8192);
  }
#pragma unroll
  for (int i = 0; i < 2; ++i) {
    float v[16];
#pragma unroll
    for (int j = 0; j < 8; ++j) { v[2 * j] = bf_lo(a[i][j >> 2][j & 3]); v[2 * j + 1] = bf_hi(a[i][j >> 2][j & 3]); }
    float s = 0;
#pragma unroll
    for (int j = 0; j < 16; ++j) s += v[j];
#pragma unroll
    for (int o = 32; o > 0; o >>= 1) s += __shfl_xor(s, o);
    const float mean = s * (1.0f / D_DIM);
    float q = 0;
#pragma unroll
    for (int j = 0; j < 16; ++j) { const float d = v[j] - mean; q += d * d; }
#pragma unroll
    for (int o = 32; o > 0; o >>= 1) q += __shfl_xor(q, o);
    const float rstd = rsqrtf(q * (1.0f / D_DIM) + 1e-5f);
    if (lane == 0) *reinterpret_cast<f32x2*>(rstat1 + (row0 + i) * 2) = f32x2{rstd, -mean * rstd};
  }
  STAMP(0, 2);
}

__global__ __launch_bounds__(256) void ln_apply_kernel(const bf16* __restrict__ x, const float* __restrict__ modp,
                                                       const float* __restrict__ rstat1, bf16* __restrict__ H,
                                                       const bf16* __restrict__ Wq, unsigned* __restrict__ flags) {
  __shared__ __attribute__((aligned(16))) float tab[4][64];
  __shared__ __attribute__((aligned(16))) char sink[1024];
  STAMP(1, 0);
  const int lane = threadIdx.x & 63, w = threadIdx.x >> 6;
  const int fr = lane & 15, fq = lane >> 4;
  const int kb = (blockIdx.x & 7) * 4 + w, mb0 = (blockIdx.x >> 3) * 4;
  u32x4 mr, xv[4];
  u32x2 rr[4];
  asm volatile("" : "=v"(mr));
  {
    bload_v<0>(mr, (unsigned)((fq * 6144 + ((lane >> 3) & 1) * 1024 + kb * 32 + (lane & 7) * 4) * 4), mk_rsrc(modp, 4 * 6144 * 4), 0);
    const rsrc_t rsX = mk_rsrc(x, S_LEN * D_DIM * 2), rsS = mk_rsrc(rstat1, S_LEN * 8);
    const unsigned vx = (unsigned)(((mb0 * 16 + fr) * D_DIM + kb * 32 + fq * 8) * 2), vs = (unsigned)((mb0 * 16 + fr) * 8);
#pragma unroll
    for (int i = 0; i < 4; ++i) {
      asm volatile("" : "=v"(xv[i]), "=v"(rr[i]));
      bload_v<0>(xv[i], vx, rsX, i * 32768);
      bload2_v<0>(rr[i], vs, rsS, i * 128);
    }
    const rsrc_t rsP = mk_rsrc((const char*)Wq + (size_t)blockIdx.x * 24576, 24576);
    const unsigned vo = threadIdx.x * 16;
#pragma unroll
    for (int i = 0; i < 6; ++i) bload_lds<0>((unsigned)(uintptr_t)sink, vo, rsP, i * 4096);
  }
  asm volatile("s_waitcnt vmcnt(" QK0W ")" : "+v"(mr), "+v"(xv[0]), "+v"(xv[1]), "+v"(xv[2]), "+v"(xv[3]), "+v"(rr[0]), "+v"(rr[1]), "+v"(rr[2]), "+v"(rr[3])::"memory");
  f32x4 mv = __builtin_bit_cast(f32x4, mr);
  f32x2 rs[4];
#pragma unroll
  for (int i = 0; i < 4; ++i) rs[i] = __builtin_bit_cast(f32x2, rr[i]);
#pragma unroll
  for (int e = 0; e < 4; ++e) { mv[e] += __shfl_xor(mv[e], 16); mv[e] += __shfl_xor(mv[e], 32); }
  if (lane < 16) *reinterpret_cast<f32x4*>(&tab[w][lane * 4]) = mv;
  __builtin_amdgcn_wave_barrier();
  asm volatile("s_waitcnt lgkmcnt(0)" ::: "memory");
  const f32x4 s0 = *reinterpret_cast<const f32x4*>(&tab[w][fq * 8]), s1 = *reinterpret_cast<const f32x4*>(&tab[w][fq * 8 + 4]);
  const f32x4 g0 = *reinterpret_cast<const f32x4*>(&tab[w][32 + fq * 8]) + 1.0f, g1 = *reinterpret_cast<const f32x4*>(&tab[w][32 + fq * 8 + 4]) + 1.0f;
#pragma unroll
  for (int i = 0; i < 4; ++i) {
    u32x4 ho;
#pragma unroll
    for (int d = 0; d < 4; ++d) {
      const f32x4 gg = d < 2 ? g0 : g1, ss = d < 2 ? s0 : s1;
      const float a = (bf_lo(xv[i][d]) * rs[i][0] + rs[i][1]) * gg[(d & 1) * 2] + ss[(d & 1) * 2];
      const float b = (bf_hi(xv[i][d]) * rs[i][0] + rs[i][1]) * gg[(d & 1) * 2 + 1] + ss[(d & 1) * 2 + 1];
      ho[d] = pk_bf16(a, b);
    }
    gst128((char*)H + ((size_t)(mb0 + i) * 32 + kb) * 1024 + lane * 16, ho);
  }
  if (threadIdx.x < 4) flags[blockIdx.x * 4 + threadIdx.x] = 0u;
  STAMP(1, 1);
}

constexpr int Q_SLOT = 21504, Q_NSL = 7, QH_OFF = Q_SLOT * Q_NSL;
constexpr int QX_OFF = QH_OFF + 1024;
constexpr int Q_LDS = QX_OFF + 12288;

DI void qkv_loader(unsigned smem_base, int lane, int sw, int m0, int n0, const bf16* __restrict__ H, const bf16* __restrict__ W,
                   const bf16* __restrict__ wg, const bf16* __restrict__ wu, const bf16* __restrict__ wo) {
  const rsrc_t rsH = mk_rsrc(H, S_LEN * D_DIM * 2);
  const rsrc_t rsW = mk_rsrc(W, D_DIM * 3 * D_DIM * 2);
  int fb0 = (m0 >> 4) - 1 + 3 * sw; fb0 = fb0 < 0 ? 0 : fb0;
  const unsigned voffH0 = (unsigned)(fb0 * 32768 + lane * 16);
  const unsigned voffH1 = (unsigned)(((m0 >> 4) + 3 * sw) * 32768 + lane * 16);
  unsigned voffW[2];
#pragma unroll
  for (int i = 0; i < 2; ++i) {
    const int r = i * 8 + (lane >> 3), ch = lane & 7;
    const int g = ((r >> 1) & 1) | (((r >> 3) & 1) << 1);
    voffW[i] = (unsigned)(r * 6144 + sw * 2048 + (n0 + (((ch >> 1) ^ g) * 16) + (ch & 1) * 8) * 2);
  }
  const unsigned m0a = smem_base + sw * 3072, m0w = smem_base + 9216 + sw * 4096;
#define Q_LOAD(SOFF, U)                                                                                          \
  do {                                                                                                           \
    const unsigned so_ = (unsigned)(U) * 1024u;                                                                  \
    bload_lds<0>(m0a + (SOFF), voffH0, rsH, so_);                                                                \
    bload_lds<0>(m0a + (SOFF) + 1024, voffH1, rsH, so_);                                                         \
    bload_lds<0>(m0a + (SOFF) + 2048, voffH1, rsH, so_ + 32768u);                                                \
    _Pragma("unroll") for (int i = 0; i < 4; ++i)                                                                \
      bload_lds<0>(m0w + (SOFF) + i * 1024, voffW[i & 1], rsW, (unsigned)(U) * 196608u + (i >> 1) * 98304u);     \
  } while (0)
#pragma unroll
  for (int u = 0; u < Q_NSL - 1; ++u) Q_LOAD(u * Q_SLOT, u);
  wait_vm<(Q_NSL - 2) * 7>();
  __builtin_amdgcn_s_barrier();
  unsigned sb = (Q_NSL - 1) * Q_SLOT;
  for (int u = 0; u < 33 - Q_NSL; ++u) {
    Q_LOAD(sb, u + Q_NSL - 1);
    sb = (sb == (Q_NSL - 1) * Q_SLOT) ? 0u : sb + Q_SLOT;
    wait_vm<(Q_NSL - 2) * 7>();
    __builtin_amdgcn_s_barrier();
  }
  const unsigned tsz = sw == 0 ? 8192u : TQ * 1024u;
  const rsrc_t rsT = mk_rsrc((const char*)(sw == 0 ? wo : (sw == 1 ? wg : wu)) + (size_t)blockIdx.x * tsz, tsz);
  static_for<Q_NSL>([&](auto U) {
    constexpr int u = U.value, left = Q_NSL - 3 - u;
    constexpr int t0 = u < TST ? 0 : (u - TST) * TPU, t1 = u < TST ? 0 : (u == Q_NSL - 1 ? 16 : t0 + TPU);
    (void)&rsT;
#pragma unroll
    for (int i = t0; i < t1 && i < 16; ++i) bload_lds<0>(smem_base + QH_OFF, lane * 16u, rsT, i * 1024u);
    if constexpr (left >= 0) wait_vm<left * 7 + (t1 < 16 ? t1 : 16)>();
    __builtin_amdgcn_s_barrier();
  });
}

DI void qkv_mfma(char* smem, unsigned smem_base, int tid, int wid, int m0, int n0, bf16* __restrict__ O) {
  const int lane = tid & 63;
  const int wm = wid & 1, wn = wid >> 1;
  const int fr = lane & 15, fq = lane >> 4;
  unsigned brd[6];
  const unsigned hrd = smem_base + lane * 16, ard = hrd + 1024 + wm * 4096;
  {
    const int q = (lane >> 2) & 3, p = lane & 3;
    const int r = 8 * fq + q;
    const int g = ((r >> 1) & 1) | (((r >> 3) & 1) << 1);
#pragma unroll
    for (int jj = 0; jj < 6; ++jj) {
      brd[jj] = smem_base + 9216 + (jj >> 1) * 4096 + r * 128 + (((wn * 2 + ((jj & 1) ^ wm)) ^ g) * 32) + 8 * p;
    }
  }
  f32x4 acc[4][6], acch[2];
#pragma unroll
  for (int i = 0; i < 4; ++i)
#pragma unroll
    for (int j = 0; j < 6; ++j) { acc[i][j] = f32x4{0, 0, 0, 0}; asm volatile("" : "+v"(acc[i][j])); }
#pragma unroll
  for (int j = 0; j < 2; ++j) { acch[j] = f32x4{0, 0, 0, 0}; asm volatile("" : "+v"(acch[j])); }
  __builtin_amdgcn_s_setprio(2);
  __builtin_amdgcn_s_barrier();
  STAMPDECL;
  STAMPNW(2, 1);
  u32x4 aX[5], aY[5];
  u32x2 bXl[6], bXh[6], bYl[6], bYh[6];
#define Q_READ(AR, BL, BH, SO, R)                                                                                \
  do {                                                                                                           \
    if ((R) < 12) {                                                                                              \
      constexpr int jb = ((R) < 12 ? (R) : 0) >> 1;                                                              \
      if ((R) & 1) dsr_tr<512>(BH[jb], brd[jb] + (SO)); else dsr_tr<0>(BL[jb], brd[jb] + (SO));                  \
    } else if ((R) < 16) {                                                                                       \
      constexpr int ia = ((R) >= 12 && (R) < 16) ? (R) - 12 : 0;                                                 \
      dsr128<ia * 1024>(AR[ia], ard + (SO));                                                                     \
    } else {                                                                                                     \
      dsr128<0>(AR[4], hrd + (SO));                                                                              \
    }                                                                                                            \
  } while (0)
#define Q_MFMA(AR, BB, N)                                                                                        \
  do {                                                                                                           \
                                     \
    if ((N) < 16) MFMA_AV(acc[(N) & 3][((N) < 24 ? (N) : 0) >> 2], BB[((N) < 24 ? (N) : 0) >> 2], AR[(N) & 3]);  \
    else if ((N) < 24) MFMA_AV(acc[(N) & 3][((N) < 24 ? (N) : 0) >> 2], AR[(N) & 3], BB[((N) < 24 ? (N) : 0) >> 2]);  \
    else if ((N) == 24) MFMA_AV(acch[0], BB[2], AR[4]);                                                          \
    else MFMA_AV(acch[1], AR[4], BB[4]);                                                                         \
  } while (0)
#define Q_UNIT(AR, BL, BH, NAR, NBL, NBH)                                                                        \
  do {                                                                                                           \
    so = (so == (Q_NSL - 1) * Q_SLOT) ? 0u : so + Q_SLOT;                                                        \
    WAIT_LGKM0();                                                                                                \
    u32x4 bb[6];                                                                                                 \
    _Pragma("unroll") for (int j = 0; j < 6; ++j) bb[j] = u32x4{BL[j][0], BL[j][1], BH[j][0], BH[j][1]};         \
    SCHED0();                                                                                                    \
    asm volatile("s_nop 1");                                                                                     \
    static_for<26>([&](auto N) {                                                                                 \
      constexpr int n = N.value;                                                                                 \
      if constexpr (n == QBAR) __builtin_amdgcn_s_barrier();                                                   \
      Q_MFMA(AR, bb, n);                                                                                         \
      if constexpr (n >= QBAR && n < QBAR + QRN) {                                                               \
        constexpr int r0 = ((n - QBAR) * 17) / QRN, r1 = ((n - QBAR + 1) * 17) / QRN;                            \
        Q_READ(NAR, NBL, NBH, so, r0);                                                                           \
        if constexpr (r0 + 1 < r1) Q_READ(NAR, NBL, NBH, so, (r0 + 1 < 17 ? r0 + 1 : 0));                        \
      }                                                                                                          \
    });                                                                                                          \
    SCHED0();                                                                                                    \
  } while (0)
  unsigned so = 0;
  static_for<17>([&](auto R) { Q_READ(aX, bXl, bXh, 0u, R.value); });
  for (int u = 0; u < 32; u += 2) {
    Q_UNIT(aX, bXl, bXh, aY, bYl, bYh);
    Q_UNIT(aY, bYl, bYh, aX, bXl, bXh);
  }
  asm volatile("s_waitcnt vmcnt(0) lgkmcnt(0)\n\ts_nop 15" ::: "memory");
#pragma unroll
  for (int i = 0; i < 5; ++i) asm volatile("" : "+v"(aX[i]));
#pragma unroll
  for (int j = 0; j < 6; ++j) asm volatile("" : "+v"(bXl[j]), "+v"(bXh[j]));
  SCHED0();
  __builtin_amdgcn_s_setprio(0);
  STAMP(2, 2); STAMPFLUSH(2, 1);
#pragma unroll
  for (int i = 0; i < 4; ++i)
#pragma unroll
    for (int j = 0; j < 6; ++j) asm volatile("" : "+v"(acc[i][j]));
  asm volatile("" : "+v"(acch[0]), "+v"(acch[1]));
  auto pk2 = [](const f32x4 v) { return u32x2{pk_bf16(v[0], v[1]), pk_bf16(v[2], v[3])}; };
  auto cat = [](const u32x2 a, const u32x2 b) { return __builtin_bit_cast(bf16x8, u32x4{a[0], a[1], b[0], b[1]}); };
  u32x2 kp[5][2], vp[5][2];
  {
    char* xb = smem + QX_OFF + wn * 6144 + lane * 8;
#pragma unroll
    for (int f = 0; f < 2; ++f) { kp[4][f] = pk2(acc[3][2 + f]); vp[4][f] = pk2(acc[3][4 + f]); }
    *reinterpret_cast<u32x2*>(xb + wm * 512) = pk2(acch[0]);
    *reinterpret_cast<u32x2*>(xb + 1024 + wm * 512) = pk2(acch[1]);
#pragma unroll
    for (int f = 0; f < 2; ++f) {
      *reinterpret_cast<u32x2*>(xb + (wm + 1) * 2048 + f * 512) = kp[4][f];
      *reinterpret_cast<u32x2*>(xb + (wm + 1) * 2048 + 1024 + f * 512) = vp[4][f];
    }
    asm volatile("s_waitcnt lgkmcnt(0)" ::: "memory");
    __builtin_amdgcn_s_barrier();
    asm volatile("" ::: "memory");
#pragma unroll
    for (int f = 0; f < 2; ++f) {
      kp[0][f] = *reinterpret_cast<const u32x2*>(xb + wm * 2048 + (f ^ wm) * 512);
      vp[0][f] = *reinterpret_cast<const u32x2*>(xb + wm * 2048 + 1024 + (f ^ wm) * 512);
    }
  }
  STAMP(2, 3);
#pragma unroll
  for (int i = 0; i < 3; ++i)
#pragma unroll
    for (int f = 0; f < 2; ++f) { kp[1 + i][f] = pk2(acc[i][2 + f]); vp[1 + i][f] = pk2(acc[i][4 + f]); }
  const bool par = fr & 1;
  const bool cura = fq * 4 + (fr & 1) <= fr, curb = fq * 4 + 2 + (fr & 1) <= fr;
  const unsigned mh = par ? 0xffff0000u : 0x0000ffffu;
  const unsigned mca = cura ? mh : 0u, mpa = cura ? 0u : mh, mcb = curb ? mh : 0u, mpb = curb ? 0u : mh;
  const float cs = 0.17677669529663687f * 1.4426950408889634f;
  char* op = (char*)O + ((size_t)((m0 >> 6) + wm) * (D_DIM / 32) + (n0 >> 5) + wn) * 4096 + lane * 16;
  const f32x4 z4 = {0, 0, 0, 0};
#pragma unroll
  for (int ii = 0; ii < 4; ++ii) {
    const int i = 3 - ii;
    const bf16x8 qf = cat(pk2(acc[i][0]), pk2(acc[i][1]));
    const f32x4 sc = __builtin_amdgcn_mfma_f32_16x16x32_bf16(cat(kp[i + 1][0], kp[i + 1][1]), qf, z4, 0, 0, 0);
    f32x4 sp = __builtin_amdgcn_mfma_f32_16x16x32_bf16(cat(kp[i][0], kp[i][1]), qf, z4, 0, 0, 0);
    if (i == 0 && m0 == 0 && wm == 0) sp = f32x4{-5.656854249e9f, -5.656854249e9f, -5.656854249e9f, -5.656854249e9f};
    const float la = cura ? (par ? sc[1] : sc[0]) : (par ? sp[1] : sp[0]);
    const float lb = curb ? (par ? sc[3] : sc[2]) : (par ? sp[3] : sp[2]);
    float mx = fmaxf(la, lb);
    {
      const auto a = __builtin_amdgcn_permlane16_swap(__float_as_uint(mx), __float_as_uint(mx), false, false);
      mx = fmaxf(__uint_as_float(a[0]), __uint_as_float(a[1]));
      const auto b = __builtin_amdgcn_permlane32_swap(__float_as_uint(mx), __float_as_uint(mx), false, false);
      mx = fmaxf(__uint_as_float(b[0]), __uint_as_float(b[1]));
    }
    const float ea = __builtin_amdgcn_exp2f((la - mx) * cs), eb = __builtin_amdgcn_exp2f((lb - mx) * cs);
    const float inv = __builtin_amdgcn_rcpf(rowsum4(ea + eb));
    const unsigned wa = pk_bf16(ea, ea), wb = pk_bf16(eb, eb);
    const bf16x8 pf = __builtin_bit_cast(bf16x8, u32x4{wa & mpa, wb & mpb, wa & mca, wb & mcb});
    const f32x4 o0 = __builtin_amdgcn_mfma_f32_16x16x32_bf16(cat(vp[i][0], vp[i + 1][0]), pf, z4, 0, 0, 0) * inv;
    const f32x4 o1 = __builtin_amdgcn_mfma_f32_16x16x32_bf16(cat(vp[i][1], vp[i + 1][1]), pf, z4, 0, 0, 0) * inv;
    const u32x2 u0 = pk2(o0), u1 = pk2(o1);
    gst128(op + i * 1024, wm ? u32x4{u1[0], u1[1], u0[0], u0[1]} : u32x4{u0[0], u0[1], u1[0], u1[1]});
  }
}

__global__ __launch_bounds__(512, 2) void qkv_attn_kernel(const bf16* __restrict__ H, const bf16* __restrict__ W,
                                                          bf16* __restrict__ O, const bf16* __restrict__ wg,
                                                          const bf16* __restrict__ wu, const bf16* __restrict__ wo) {
  __shared__ __attribute__((aligned(16))) char smem[Q_LDS];
  STAMP(2, 0);
  const int tid = threadIdx.x;
  const int wid = __builtin_amdgcn_readfirstlane(tid >> 6);
  const unsigned smem_base = (unsigned)(uintptr_t)smem;
  const int bid = (blockIdx.x & 7) * 32 + (blockIdx.x >> 3);
  const int grp = bid >> 7, ing = bid & 127;
  const int bm = grp * 8 + (ing & 7), bn = ing >> 3;
  const int m0 = bm * 128, n0 = bn * 64;
  if (wid < 4) qkv_mfma(smem, smem_base, tid, wid, m0, n0, O);
  else qkv_loader(smem_base, tid & 63, wid - 4, m0, n0, H, W, wg, wu, wo);
  STAMP(2, 4);
  STAMP(2, 5);
  STAMPT(2, 6, 320);
}

template <int NSTEP, int R, int EPI>
__global__ __launch_bounds__(256, 1) void gemm_direct(const bf16* __restrict__ At, const bf16* __restrict__ W,
                                                      const bf16* __restrict__ Xres,
                                                      const float* __restrict__ modp, bf16* __restrict__ C,
                                                      bf16* __restrict__ Y, float* __restrict__ stat,
                                                      const bf16* __restrict__ wg, const bf16* __restrict__ wu) {
  constexpr int KB = NSTEP * 4;
  __shared__ __attribute__((aligned(16))) char smem[96 * 1024 + 1024 + 1024];
  constexpr int KID = (EPI == 0) ? 3 : 6;
  STAMPDECL;
  STAMP(KID, 0);
  const int tid = threadIdx.x, lane = tid & 63;
  const int w = __builtin_amdgcn_readfirstlane(tid >> 6);
  const int xcd = blockIdx.x & 7, idx = blockIdx.x >> 3;
  const int bm = (xcd >> 1) * 8 + (idx & 7), bn = (xcd & 1) * 4 + (idx >> 3);
  const int m0 = bm * 64;
  const int fr = lane & 15, fq = lane >> 4;
  const int cA = bn * 128, cB = cA + 64;
  const int colq = (fq < 2 ? cA : cB - 64) + fq * 32;
  const int colf = (fr < 8 ? cA : cB - 64) + fr * 8;

  const rsrc_t rsA = mk_rsrc((const char*)At + (size_t)bm * KB * 4096, KB * 4096);
  const rsrc_t rsB = mk_rsrc(W, KB * 65536);
  const unsigned voffA = lane * 16;
  unsigned voffR[4];
#pragma unroll
  for (int i = 0; i < 4; ++i) voffR[i] = voffA + ((i + w) & 3) * 1024;
  const unsigned voffB = fq * (EPI == 0 ? 4 : 8) * 2048 + colf * 2;

  u32x4 braw[R][8], afr[R][4];
  u32x4 T[2][2];
  u32x4 xv[4];
  unsigned gv[4] = {0, 0, 0, 0};
  f32x4 acc[4][8];
#pragma unroll
  for (int r = 0; r < R; ++r) {
#pragma unroll
    for (int j = 0; j < 8; ++j) asm volatile("" : "=v"(braw[r][j]));
#pragma unroll
    for (int i = 0; i < 4; ++i) asm volatile("" : "=a"(afr[r][i]));
  }
#pragma unroll
  for (int r = 0; r < 4; ++r) asm volatile("" : "=v"(xv[r]));
#define DLOAD(SL, L, KBV)                                                                          \
  do {                                                                                             \
    if ((L) < 8) {                                                                                 \
      const unsigned so = (unsigned)(KBV) * 65536u + (EPI == 0 ? (((L) >> 1) & 1) * 4096u + (((L) >> 2) & 1) * 32768u : ((L) >> 1) * 4096u); \
      if ((L) & 1) bload_v<2048>(braw[SL][(L) & 7], voffB, rsB, so);                               \
      else bload_v<0>(braw[SL][(L) & 7], voffB, rsB, so);                                          \
    } else {                                                                                       \
      bload_a<0>(afr[SL][(L) & 3], voffR[(L) & 3], rsA, (unsigned)(KBV) * 4096u);                  \
    }                                                                                              \
  } while (0)
#define DPERM(TB, Q, SL, PP)                                                                       \
  do {                                                                                             \
    if ((Q) & 1) PERM_HI(T[TB][1][(Q) >> 1], braw[SL][((Q) >> 1) * 2][PP], braw[SL][((Q) >> 1) * 2 + 1][PP]); \
    else PERM_LO(T[TB][0][(Q) >> 1], braw[SL][((Q) >> 1) * 2][PP], braw[SL][((Q) >> 1) * 2 + 1][PP]);         \
  } while (0)

#pragma unroll
  for (int l = 0; l < 12; ++l) DLOAD(0, l, w);
  {
    const rsrc_t rsG = mk_rsrc(modp + (w >= 2 ? 4096 : (EPI == 0 ? 2048 : 5120)), 4 * 6144 * 4);
    const unsigned vo = (unsigned)((((tid & 64) ? cB : cA) + (tid & 63)) * 4);
    bload1_v<0>(gv[0], vo, rsG, 0); bload1_v<0>(gv[1], vo, rsG, 24576);
    bload1_v<0>(gv[2], vo, rsG, 49152); bload1_v<0>(gv[3], vo, rsG, 73728);
    const rsrc_t rsX = mk_rsrc(Xres, S_LEN * D_DIM * 2);
    if (EPI == 0) {
      const unsigned vx = (unsigned)(((m0 + w * 16 + fr) * D_DIM + colq) * 2);
      bload_v<0>(xv[0], vx, rsX, 0); bload_v<16>(xv[1], vx, rsX, 0); bload_v<32>(xv[2], vx, rsX, 0); bload_v<48>(xv[3], vx, rsX, 0);
    } else {
      const unsigned vx = (unsigned)(((bm >> 2) * 32 + (colq >> 5)) * 16384 + ((bm & 3) * 4 + w) * 1024 + fr * 16);
      bload_v<0>(xv[0], vx, rsX, 0); bload_v<256>(xv[1], vx, rsX, 0); bload_v<512>(xv[2], vx, rsX, 0); bload_v<768>(xv[3], vx, rsX, 0);
    }
  }
#pragma unroll
  for (int s = 1; s < R - 1; ++s)
#pragma unroll
    for (int l = 0; l < 12; ++l) DLOAD(s, l, s * 4 + w);
#pragma unroll
  for (int i = 0; i < 4; ++i)
#pragma unroll
    for (int f = 0; f < 8; ++f) { acc[i][f] = f32x4{0, 0, 0, 0}; asm volatile("" : "+a"(acc[i][f])); }
  unsigned wlo[3];
#pragma unroll
  for (int i = 1; i < 4; ++i) wlo[i - 1] = (unsigned)(uintptr_t)smem + voffA + (((w + i) & 3) * 3 + (i - 1)) * 8192;
  wait_vm<(R - 2) * 12 + 8>();
  STAMPNW(KID, 1);
#pragma unroll
  for (int q = 0; q < 8; ++q) DPERM(0, q, 0, 0);
  asm volatile("s_nop 1");
  int kbl = (R - 1) * 4 + w;
  auto step = [&](auto SLc, auto REMc) {
    constexpr int sl = SLc.value, rem = REMc.value, ns = (sl + 1) % R, ls = (sl + R - 1) % R;
    static_for<4>([&](auto P) {
      constexpr int p = P.value;
      if constexpr (p == 3 && rem > 0) wait_vm<(rem - 1 < R - 2 ? rem - 1 : R - 2) * 12>();
      static_for<8>([&](auto Q) {
        constexpr int q = Q.value;
        MFMA_A(acc[q & 3][2 * p + (q >> 2)], T[p & 1][q >> 2], afr[sl][q & 3]);
        if constexpr (p < 3) DPERM((p + 1) & 1, q, sl, p + 1);
        else if constexpr (rem > 0) DPERM(0, q, ns, 0);
        if constexpr (rem >= R - 1 && p < 3 && (q & 1)) DLOAD(ls, p * 4 + (q >> 1), kbl);
        if constexpr (rem == 0 && p > 0 && q >= 2) {
          constexpr int e = q - 2, i = 1 + e % 3, f = 2 * (p - 1) + e / 3;
          dsw128a<f * 1024>(wlo[i - 1], acc[i][f]);
        }
      });
    });
    kbl += 4;
  };
  for (int it = 0; it < NSTEP / R - 1; ++it) static_for<R>([&](auto S) { step(S, ic<R>{}); });
  static_for<R>([&](auto S) { step(S, ic<R - 1 - S.value>{}); });
  asm volatile("s_nop 15" ::: "memory");
#pragma unroll
  for (int f = 0; f < 8; ++f) asm volatile("" : "+a"(acc[0][f]));
  static_for<6>([&](auto E) { constexpr int i = 1 + E.value % 3, f = 6 + E.value / 3; dsw128a<f * 1024>(wlo[i - 1], acc[i][f]); });
#pragma unroll
  for (int r = 0; r < 4; ++r) asm volatile("" : "+v"(xv[r]), "+v"(gv[r]));
  STAMP(KID, 2); STAMPFLUSH(KID, 1);
  if constexpr (EPI == 0 && TG > 0) {
    const unsigned vo = tid * 16, sink = (unsigned)(uintptr_t)smem + 96 * 1024 + 1024;
    const rsrc_t rsG = mk_rsrc((const char*)wg + 256 * TQ * 1024 + (size_t)blockIdx.x * (TG * 1024), TG * 1024);
    const rsrc_t rsU = mk_rsrc((const char*)wu + 256 * TQ * 1024 + (size_t)blockIdx.x * (TG * 1024), TG * 1024);
#pragma unroll
    for (int i = 0; i < TG / 4; ++i) { bload_lds<0>(sink, vo, rsG, i * 4096); bload_lds<0>(sink, vo, rsU, i * 4096); }
  }
  float* gtab = reinterpret_cast<float*>(smem + 96 * 1024);
  {
    float sum = (__uint_as_float(gv[0]) + __uint_as_float(gv[1])) + (__uint_as_float(gv[2]) + __uint_as_float(gv[3]));
    if (w >= 2) {
      sum += 1.0f;
      if (__builtin_fabsf(sum) < 8.67361738e-19f) sum = 8.67361738e-19f;
      if (EPI == 1) sum = 1.0f / sum;
    }
    const int col = tid & 127;
    gtab[(tid & 128) + (col & 96) + (col & 7) * 4 + ((col >> 3) & 3)] = sum;
  }
  asm volatile("s_waitcnt lgkmcnt(0)" ::: "memory");
  __builtin_amdgcn_s_barrier();
  u32x4 pr[3][8], gt[8], ct[8];
  f32x4 fin[8];
  {
    const unsigned src = (unsigned)(uintptr_t)smem + w * 24576 + lane * 16;
    const unsigned gsrc = (unsigned)(uintptr_t)smem + 96 * 1024 + fq * 128;
    static_for<24>([&](auto E) { dsr128<E.value * 1024>(pr[E.value >> 3][E.value & 7], src); });
    static_for<8>([&](auto F) { dsr128<F.value * 16>(gt[F.value], gsrc); });
    static_for<8>([&](auto F) { dsr128<512 + F.value * 16>(ct[F.value], gsrc); });
#pragma unroll
    for (int f = 0; f < 8; ++f) { fin[f] = acc[0][f]; asm volatile("" : "+v"(fin[f])); }
    asm volatile("s_waitcnt lgkmcnt(0)" ::: "memory");
#pragma unroll
    for (int f = 0; f < 8; ++f) {
      asm volatile("" : "+v"(pr[0][f]), "+v"(pr[1][f]), "+v"(pr[2][f]), "+v"(gt[f]), "+v"(ct[f]));
    }
  }
#pragma unroll
  for (int f = 0; f < 8; ++f) {
    fin[f] = (fin[f] + __builtin_bit_cast(f32x4, pr[0][f])) + (__builtin_bit_cast(f32x4, pr[1][f]) + __builtin_bit_cast(f32x4, pr[2][f]));
    f32x4 xf;
#pragma unroll
    for (int r = 0; r < 4; ++r) xf[r] = (f & 1) ? bf_hi(xv[r][f >> 1]) : bf_lo(xv[r][f >> 1]);
    if constexpr (EPI == 1) xf *= __builtin_bit_cast(f32x4, ct[f]);
    fin[f] = xf + __builtin_bit_cast(f32x4, gt[f]) * fin[f];
  }
  if constexpr (EPI == 0) {
    f32x4 s4 = (fin[0] + fin[1]) + (fin[2] + fin[3]) + ((fin[4] + fin[5]) + (fin[6] + fin[7]));
    const float s = rowsum4((s4[0] + s4[1]) + (s4[2] + s4[3]));
    const float mb = s * (1.0f / 128.0f);
    f32x4 q4 = {0, 0, 0, 0};
    f32x4 yv[8];
#pragma unroll
    for (int f = 0; f < 8; ++f) {
      yv[f] = fin[f] * __builtin_bit_cast(f32x4, ct[f]);
      const f32x4 d = fin[f] - mb;
      q4 += d * d;
    }
    char* yb = (char*)Y + ((size_t)(bm >> 2) * 32 + (colq >> 5)) * 16384 + ((bm & 3) * 4 + w) * 1024 + fr * 16;
#pragma unroll
    for (int r = 0; r < 4; ++r) {
      const u32x4 yo = {pk_bf16(yv[0][r], yv[1][r]), pk_bf16(yv[2][r], yv[3][r]), pk_bf16(yv[4][r], yv[5][r]), pk_bf16(yv[6][r], yv[7][r])};
      gst128(yb + r * 256, yo);
    }
    const float m2 = rowsum4((q4[0] + q4[1]) + (q4[2] + q4[3]));
    if (fq == 0) *reinterpret_cast<f32x2*>(stat + ((size_t)(m0 + w * 16 + fr) * 8 + bn) * 2) = f32x2{s, m2};
  } else {
    char* tb = smem + (w * 3 * 8) * 1024;
#pragma unroll
    for (int r = 0; r < 4; ++r) {
      const u32x4 ov = {pk_bf16(fin[0][r], fin[1][r]), pk_bf16(fin[2][r], fin[3][r]), pk_bf16(fin[4][r], fin[5][r]), pk_bf16(fin[6][r], fin[7][r])};
      *reinterpret_cast<u32x4*>(tb + fr * 256 + (((fq * 4 + r) ^ (fr & 7)) * 16)) = ov;
    }
#pragma unroll
    for (int j = 0; j < 4; ++j) {
      const int row = j * 4 + fq;
      const u32x4 ov = *reinterpret_cast<const u32x4*>(tb + row * 256 + ((fr ^ (row & 7)) * 16));
      gst128(C + (size_t)(m0 + w * 16 + row) * D_DIM + colf, ov);
    }
  }
  STAMP(KID, 3);
  STAMP(KID, 4);
}

#define NBR 0
#define TPOS 6
#define NBC 1
#define G3_TW "12"
#define G3_DUMMY() do { } while (0)
constexpr int G3_CT = 131072 + 4096 + 1024;
#define G3_STAGE(SLOT, P, U)                                                                               \
  do {                                                                                                     \
    if ((P) == 0) bload_lds<0>(m0w + (SLOT) * 32768, voffA, rsA, (unsigned)(U) * 16384u);                  \
    else if ((P) == 1) bload_lds<1024>(m0w + (SLOT) * 32768, voffA, rsA, (unsigned)(U) * 16384u);          \
    else if ((P) == 2) bload_lds<0>(m0w + (SLOT) * 32768 + 16384, voffB0, rsB, (unsigned)(U) * 229376u);   \
    else bload_lds<0>(m0w + (SLOT) * 32768 + 16384 + 1024, voffB1, rsB, (unsigned)(U) * 229376u);          \
  } while (0)

template <int FN>
DI void g3_body(char* smem, unsigned smem_base, int wm, int wn, int wid, int lane, int bm, int bn, rsrc_t rsA, rsrc_t rsB,
                rsrc_t rsP, unsigned voffA, unsigned voffB0, unsigned voffB1, bf16* __restrict__ act) {
  constexpr int NB = 2 * FN, NM = 4 * NB, RD = 5 + 2 * NB;
  const int fr = lane & 15, fq = lane >> 4;
  f32x4 acc[4][NB], accC[2];
  accC[0] = f32x4{0, 0, 0, 0}; accC[1] = f32x4{0, 0, 0, 0};
  asm volatile("" : "+v"(accC[0]), "+v"(accC[1]));
  const int rot = wm % FN;
  unsigned cad = smem_base + G3_CT + (fr < 4 ? fr * 64 : 256) + fq * 16;
#pragma unroll
  for (int i = 0; i < 4; ++i)
#pragma unroll
    for (int j = 0; j < NB; ++j) { acc[i][j] = f32x4{0, 0, 0, 0}; asm volatile("" : "+v"(acc[i][j])); }
  unsigned aad[2], bad[2][FN];
  aad[0] = smem_base + wm * 4096 + lane * 16; aad[1] = aad[0] + 65536;
  {
    const int q = (lane >> 2) & 3, p = lane & 3;
    const int r = 8 * fq + q;
    const int sw = (r & 3) | (((r >> 3) & 1) << 2);
#pragma unroll
    for (int j = 0; j < FN; ++j) {
      const int gi = wn * 4 + (j + rot) % FN + (bn & 1);
      bad[0][j] = smem_base + 16384 + r * 256 + ((gi ^ sw) * 32) + 8 * p;
      bad[1][j] = bad[0][j] + 65536;
    }
  }
  u32x4 aX[5], aY[5];
  u32x2 bXl[NB], bXh[NB], bYl[NB], bYh[NB];
  const unsigned m0w = smem_base + wid * 2048;
  const unsigned voffP = lane * 16;
#define G3_READ(AR, BL, BH, SLOT, R)                                                                       \
  do {                                                                                                     \
    if ((R) < 2 * NB) {                                                                                    \
      constexpr int jb = ((R) < 2 * NB ? (R) : 0) >> 1;                                                    \
      constexpr int off = ((SLOT) & 1) * 32768 + (jb / FN) * 8192 + ((R) & 1) * 1024;                      \
      if ((R) & 1) dsr_tr<off>(BH[jb], bad[(SLOT) >> 1][jb % FN]);                                         \
      else dsr_tr<off>(BL[jb], bad[(SLOT) >> 1][jb % FN]);                                                 \
    } else if ((R) < 2 * NB + 4) {                                                                         \
      constexpr int ia = ((R) >= 2 * NB && (R) < 2 * NB + 4) ? (R) - 2 * NB : 0;                           \
      dsr128<((SLOT) & 1) * 32768 + ia * 1024>(AR[ia], aad[(SLOT) >> 1]);                                  \
    } else {                                                                                               \
      if ((SLOT) == 0) cad += 1280;                                      \
      dsr128<(SLOT) * 320>(AR[4], cad);                                                                    \
    }                                                                                                      \
  } while (0)
  auto rd = [&](auto SL, auto RR, u32x4 (&AR)[5], u32x2 (&BL)[NB], u32x2 (&BH)[NB]) { G3_READ(AR, BL, BH, SL.value, RR.value); };
#define G3_UNIT(AR, BL, BH, NAR, NBL, NBH, SLOT, U)                                                        \
  do {                                                                                                     \
    WAIT_LGKM0();                                                                                          \
    u32x4 bb[NB];                                                                                          \
    _Pragma("unroll") for (int j = 0; j < NB; ++j) bb[j] = u32x4{BL[j][0], BL[j][1], BH[j][0], BH[j][1]};  \
    SCHED0();                                                                                              \
    asm volatile("s_nop 1");                                                                               \
    static_for<NM>([&](auto N) {                                                                           \
      constexpr int n = N.value;                                                                           \
      if constexpr (n == NM / 2) {                                                                         \
        wait_vm<NBR ? 10 : 8>(); __builtin_amdgcn_s_barrier();                                             \
      }                                                                                                    \
      if constexpr (n == NM / 2 + TPOS) {                                                                  \
                      \
        if (NBR) bload_lds<0>(smem_base + 131072 + 4096, voffP, rsP, (((U) & 7) == wid) ? (unsigned)(U) * 1024u : 0x40000000u); \
        else if (((U) & 7) == wid) bload_lds<0>(smem_base + 131072 + 4096, voffP, rsP, (unsigned)(U) * 1024u);  \
      }                                                                                                    \
      MFMA_AV(acc[n & 3][n >> 2], bb[n >> 2], AR[n & 3]);                                                  \
      if constexpr (n < NM / 2) {                                                                          \
        if constexpr (n % 3 == 1 && n / 3 < 4) G3_STAGE(((SLOT) + 3) & 3, n / 3, (U) + 3);                 \
      } else {                                                                                             \
        constexpr int r0 = (n - NM / 2) * 2;                                                               \
        if constexpr (r0 < RD) rd(ic<((SLOT) + 1) & 3>{}, ic<(r0 < RD ? r0 : 0)>{}, NAR, NBL, NBH);        \
        if constexpr (r0 + 1 < RD) rd(ic<((SLOT) + 1) & 3>{}, ic<(r0 + 1 < RD ? r0 + 1 : 0)>{}, NAR, NBL, NBH); \
      }                                                                                                    \
    });                                                                                                    \
    if (NBC || FN == 4 || wm < 3) {                                                                        \
      MFMA_AV(accC[0], bb[0], AR[4]);                                                                      \
      MFMA_AV(accC[1], bb[FN], AR[4]);                                                                     \
    }                                                                                                      \
    SCHED0();                                                                                              \
  } while (0)

  if (NBR) asm volatile("s_waitcnt vmcnt(10) lgkmcnt(0)" ::: "memory");
  else asm volatile("s_waitcnt vmcnt(8) lgkmcnt(0)" ::: "memory");
  __builtin_amdgcn_s_barrier();
  STAMPDECL;
  STAMPNW(5, 1);
  cad -= 1280;
  static_for<RD>([&](auto RR) { rd(ic<0>{}, RR, aX, bXl, bXh); });
  for (int u = 0; u < 32; u += 4) {
    G3_UNIT(aX, bXl, bXh, aY, bYl, bYh, 0, u);
    G3_UNIT(aY, bYl, bYh, aX, bXl, bXh, 1, u + 1);
    G3_UNIT(aX, bXl, bXh, aY, bYl, bYh, 2, u + 2);
    G3_UNIT(aY, bYl, bYh, aX, bXl, bXh, 3, u + 3);
  }
  asm volatile("s_waitcnt lgkmcnt(0)\n\ts_nop 15" ::: "memory");
#pragma unroll
  for (int i = 0; i < 5; ++i) asm volatile("" : "+v"(aX[i]));
#pragma unroll
  for (int j = 0; j < NB; ++j) asm volatile("" : "+v"(bXl[j]), "+v"(bXh[j]));
  SCHED0();
  STAMP(5, 2);
  STAMPFLUSH(5, 1);
  const f32x2* rstat = reinterpret_cast<const f32x2*>(smem + 131072);
  float* colc = reinterpret_cast<float*>(smem + 131072 + 2048);
#pragma unroll
  for (int m = 0; m < 2; ++m) {
    f32x4 v;
#pragma unroll
    for (int r = 0; r < 4; ++r) v[r] = accC[m][r] + __shfl_xor(accC[m][r], 1);
    if ((fr == 0 || fr == 2) && wm < FN) *reinterpret_cast<f32x4*>(colc + (m * 2 + (fr >> 1)) * 128 + (wn * 4 + rot) * 16 + fq * 4) = v;
  }
  asm volatile("s_waitcnt lgkmcnt(0)" ::: "memory");
  __builtin_amdgcn_s_barrier();
  asm volatile("" ::: "memory");
  f32x2 rs[4];
#pragma unroll
  for (int i = 0; i < 4; ++i) rs[i] = rstat[wm * 64 + i * 16 + fr];
#pragma unroll
  for (int j = 0; j < FN; ++j) {
    const int jg = (j + rot) % FN;
    const int cl = (wn * 4 + jg) * 16 + fq * 4;
    const f32x4 csg = *reinterpret_cast<const f32x4*>(colc + cl), bg = *reinterpret_cast<const f32x4*>(colc + 128 + cl);
    const f32x4 csu = *reinterpret_cast<const f32x4*>(colc + 256 + cl), bu = *reinterpret_cast<const f32x4*>(colc + 384 + cl);
    u32x2 pk[4];
#pragma unroll
    for (int i = 0; i < 4; ++i) {
#pragma unroll
      for (int hh = 0; hh < 2; ++hh) {
        const f32x2 ag = hh ? hi2(acc[i][j]) : lo2(acc[i][j]), au = hh ? hi2(acc[i][FN + j]) : lo2(acc[i][FN + j]);
        const f32x2 g = rs[i][0] * ag + (rs[i][1] * (hh ? hi2(csg) : lo2(csg)) + (hh ? hi2(bg) : lo2(bg)));
        const f32x2 uu = rs[i][0] * au + (rs[i][1] * (hh ? hi2(csu) : lo2(csu)) + (hh ? hi2(bu) : lo2(bu)));
        const f32x2 ea = g * -1.4426950408889634f;
        f32x2 e = {__builtin_amdgcn_exp2f(ea[0]), __builtin_amdgcn_exp2f(ea[1])};
        e = e + 1.0f;
        const f32x2 rc = {__builtin_amdgcn_rcpf(e[0]), __builtin_amdgcn_rcpf(e[1])};
        const f32x2 o = (g * uu) * rc;
        pk[i][hh] = pk_bf16(o[0], o[1]);
      }
    }
    const int J = bn * 7 + wn * 4 + jg;
    char* dst = (char*)act + ((size_t)((bm * 4 + wm) * (F_DIM / 32) + (J >> 1)) * 4 + (fq & 1)) * 1024 + (((J & 1) * 2 + (fq >> 1)) * 16 + fr) * 16;
#pragma unroll
    for (int i = 0; i < 4; i += 2) {
      const auto s0 = __builtin_amdgcn_permlane16_swap(pk[i][0], pk[i + 1][0], false, false);
      const auto s1 = __builtin_amdgcn_permlane16_swap(pk[i][1], pk[i + 1][1], false, false);
      gst128(dst + i * 1024, u32x4{s0[0], s1[0], s0[1], s1[1]});
    }
  }
}

__global__ __launch_bounds__(512, 2) void gemm3_kernel(const bf16* __restrict__ Y, const bf16* __restrict__ Wg,
                                                       const bf16* __restrict__ Wu, bf16* __restrict__ act,
                                                       const float* __restrict__ stat, const float* __restrict__ modp,
                                                       const bf16* __restrict__ Wd) {
  __shared__ __attribute__((aligned(16))) char smem[G3_CT + 33 * 320];
  STAMP(5, 0);
  const int tid = threadIdx.x, lane = tid & 63;
  const int wid = __builtin_amdgcn_readfirstlane(tid >> 6);
  const int wm = wid & 3, wn = wid >> 2;
  const int xcd = blockIdx.x & 7, idx = blockIdx.x >> 3;
  const int bm = idx & 7, bn = xcd * 4 + (idx >> 3);
  const int n0 = bn * 112, n0a = n0 & ~31;
  const unsigned smem_base = (unsigned)(uintptr_t)smem;
  const rsrc_t rsA = mk_rsrc((const char*)Y + (size_t)bm * 32 * 16384, 32 * 16384);
  const rsrc_t rsB = mk_rsrc(wn ? Wu : Wg, D_DIM * F_DIM * 2);
  const rsrc_t rsP = mk_rsrc((const char*)Wd + (size_t)blockIdx.x * 28672, 28672);
  const unsigned voffA = wid * 2048 + lane * 16;
  unsigned voffB[2];
#pragma unroll
  for (int i = 0; i < 2; ++i) {
    const int r = (wm * 2 + i) * 4 + (lane >> 4);
    const int sw = (r & 3) | (((r >> 3) & 1) << 2);
    const int c16 = lane & 15;
    voffB[i] = (unsigned)(r * (F_DIM * 2) + (n0a + (((c16 >> 1) ^ sw) * 16) + (c16 & 1) * 8) * 2);
  }
  const unsigned m0w = smem_base + wid * 2048;
  const unsigned voffB0 = voffB[0], voffB1 = voffB[1];
  if (wn == 0) {
    u32x4 tv[4];
#pragma unroll
    for (int i = 0; i < 4; ++i) asm volatile("" : "=v"(tv[i]));
    const rsrc_t rsS = mk_rsrc(stat, S_LEN * 64);
    const unsigned vo = (unsigned)((bm * 256 + tid) * 64);
    bload_v<0>(tv[0], vo, rsS, 0); bload_v<16>(tv[1], vo, rsS, 0); bload_v<32>(tv[2], vo, rsS, 0); bload_v<48>(tv[3], vo, rsS, 0);
    G3_STAGE(0, 0, 0); G3_STAGE(0, 1, 0); G3_STAGE(0, 2, 0); G3_STAGE(0, 3, 0);
    G3_STAGE(1, 0, 1); G3_STAGE(1, 1, 1); G3_STAGE(1, 2, 1); G3_STAGE(1, 3, 1);
    G3_DUMMY();
    G3_STAGE(2, 0, 2); G3_STAGE(2, 1, 2); G3_STAGE(2, 2, 2); G3_STAGE(2, 3, 2);
    G3_DUMMY();
    asm volatile("s_waitcnt vmcnt(" G3_TW ")" : "+v"(tv[0]), "+v"(tv[1]), "+v"(tv[2]), "+v"(tv[3])::"memory");
    float s = 0;
#pragma unroll
    for (int i = 0; i < 4; ++i) s += __uint_as_float(tv[i][0]) + __uint_as_float(tv[i][2]);
    const float mean = s * (1.0f / D_DIM);
    float m2 = 0;
#pragma unroll
    for (int i = 0; i < 4; ++i) {
      const float d0 = __uint_as_float(tv[i][0]) * (1.0f / 128.0f) - mean, d1 = __uint_as_float(tv[i][2]) * (1.0f / 128.0f) - mean;
      m2 += __uint_as_float(tv[i][1]) + __uint_as_float(tv[i][3]) + 128.0f * (d0 * d0 + d1 * d1);
    }
    const float rstd = rsqrtf(m2 * (1.0f / D_DIM) + 1e-5f);
    reinterpret_cast<f32x2*>(smem + 131072)[tid] = f32x2{rstd, -mean * rstd};
    g3_body<4>(smem, smem_base, wm, wn, wid, lane, bm, bn, rsA, rsB, rsP, voffA, voffB0, voffB1, act);
  } else {
    const rsrc_t rsC = mk_rsrc(modp, 4 * 6144 * 4);
    const int t = tid - 256;
    const unsigned vo = (unsigned)((3072 + t * 4) * 4);
    u32x4 tc[8];
#pragma unroll
    for (int i = 0; i < 8; ++i) { asm volatile("" : "=v"(tc[i])); bload_v<0>(tc[i], vo, rsC, (i >> 1) * 24576 + (i & 1) * 4096); }
    G3_STAGE(0, 0, 0); G3_STAGE(0, 1, 0); G3_STAGE(0, 2, 0); G3_STAGE(0, 3, 0);
    G3_STAGE(1, 0, 1); G3_STAGE(1, 1, 1); G3_STAGE(1, 2, 1); G3_STAGE(1, 3, 1);
    G3_DUMMY();
    G3_STAGE(2, 0, 2); G3_STAGE(2, 1, 2); G3_STAGE(2, 2, 2); G3_STAGE(2, 3, 2);
    G3_DUMMY();
    asm volatile("s_waitcnt vmcnt(" G3_TW ")" : "+v"(tc[0]), "+v"(tc[1]), "+v"(tc[2]), "+v"(tc[3]), "+v"(tc[4]), "+v"(tc[5]), "+v"(tc[6]), "+v"(tc[7])::"memory");
    const f32x4 sv = (__builtin_bit_cast(f32x4, tc[0]) + __builtin_bit_cast(f32x4, tc[2])) + (__builtin_bit_cast(f32x4, tc[4]) + __builtin_bit_cast(f32x4, tc[6]));
    const f32x4 gw = (__builtin_bit_cast(f32x4, tc[1]) + __builtin_bit_cast(f32x4, tc[3])) + (__builtin_bit_cast(f32x4, tc[5]) + __builtin_bit_cast(f32x4, tc[7])) + 1.0f;
    u32x2 gh, gl, sh, sl;
    split4(gw, gh, gl); split4(sv, sh, sl);
    char* d = smem + G3_CT + (t >> 3) * 320 + ((t >> 1) & 3) * 16 + (t & 1) * 8;
    *reinterpret_cast<u32x2*>(d) = gh; *reinterpret_cast<u32x2*>(d + 64) = gl;
    *reinterpret_cast<u32x2*>(d + 128) = sh; *reinterpret_cast<u32x2*>(d + 192) = sl;
    *reinterpret_cast<u32x2*>(d + 256) = u32x2{0, 0};
    g3_body<3>(smem, smem_base, wm, wn, wid, lane, bm, bn, rsA, rsB, rsP, voffA, voffB0, voffB1, act);
  }
  STAMP(5, 3);
  STAMP(5, 4);
}

#define XST 0
#define G4NF 11
#define HFS 3
#define HFL 4
#define HCK 5
#define XMAP 1
template <int OFF>
DI void bstore_sc1(u32x4 d, unsigned voff, rsrc_t rs, unsigned soff) {
  asm volatile("buffer_store_dwordx4 %0, %1, %2, %3 offen offset:%4 sc1\n\ts_nop 1" ::"v"(d), "v"(voff), "s"(rs), "s"(soff), "i"(OFF) : "memory");
}
template <int OFF>
DI void bload_sc1(u32x4& d, unsigned voff, rsrc_t rs, unsigned soff) {
  asm volatile("buffer_load_dwordx4 %0, %1, %2, %3 offen offset:%4 sc1" : "+v"(d) : "v"(voff), "s"(rs), "s"(soff), "i"(OFF) : "memory");
}
DI void bload1_sc1(unsigned& d, unsigned voff, rsrc_t rs) {
  asm volatile("buffer_load_dword %0, %1, %2, 0 offen sc1" : "+v"(d) : "v"(voff), "s"(rs) : "memory");
}
namespace g4 {
constexpr int NF = G4NF, NH = 28 - 2 * NF, NS = NF + NH, HEP = NH - 3;
constexpr int nslot(int g) { return g < NF ? 64 : 32; }
constexpr int wslot(int g) { return g < NF - 1 ? 48 : (g == NF - 1 ? 56 : 24); }
constexpr int ring(int g, int n) {
  if (g < NF - 1) {
    if (n >= 48 || n % 3 != 1) return -1;
    const int l = n / 3;
    if (g + 2 < NF) return (g + 2) * 16 + l;
    return l < 12 ? (g + 2) * 16 + (l < 8 ? l : l + 4) : -1;
  }
  if (g == NF - 1) { if (n >= 48 || n % 4 != 1) return -1; const int l = n / 4; return (g + 2) * 16 + (l < 8 ? l : l + 4); }
  if (g == NF) { if (n >= 24) return -1; const int l = n % 12; return (g + 2 + n / 12) * 16 + (l < 8 ? l : l + 4); }
  if (g + 3 >= NS || n >= 24 || (n & 1)) return -1;
  const int l = n >> 1;
  return (g + 3) * 16 + (l < 8 ? l : l + 4);
}
constexpr int pwrite(int g, int n) {
  const int m = g == NF - 1 ? n : 64 + n;
  for (int e = 0; e < 24; ++e) if (12 + (13 * e) / 4 == m) return e;
  return -1;
}
constexpr int red0(int n) { return n < 8 ? 0 : (64 * (n - 8)) / 24; }
#define SPS 4
constexpr int sslot(int f) {
  const int ready = (f >> 2) * 32 + 8 + (((f & 3) + 1) * 16 * 24 + 63) / 64;
  const int paced = (f / SPS) * 32 + 15 + (f % SPS) * (32 / SPS);
  int m = ready > paced ? ready : paced;
  if ((m & 1) == 0) ++m;
  return m;
}
constexpr int dstore_f(int g, int n) {
  for (int f = 0; f < 8; ++f) if (sslot(f) == (g - NF) * 32 + n) return f;
  return -1;
}
constexpr bool dstore(int g, int n) { return g >= NF && dstore_f(g, n) >= 0; }
constexpr int vmops(int g, int n) {
  int c = (ring(g, n) >= 0 ? 1 : 0) + (dstore(g, n) ? 1 : 0);
  const int h = g - NF;
  if (h == HFS && n == 24) c += 1;
  if (h == HFL && n == 25) c += 1;
  if (h == HCK && n >= 24) c += 1;
  if (h == HEP && (n == 1 || n == 3)) c += 4;
  return c;
}
constexpr int between(int g0, int n0, int g1, int n1) {
  int c = 0;
  for (int g = g0; g <= g1; ++g)
    for (int n = (g == g0 ? n0 + 1 : 0); n < (g == g1 ? n1 : nslot(g)); ++n) c += vmops(g, n);
  return c;
}
constexpr int imin(int a, int b) { return a < b ? a : b; }
constexpr int waitcnt(int g) {
  int c = 63;
  if (g + 1 < NS && g + 1 >= 2) {
    for (int s = 0; s <= g; ++s)
      for (int n = 0; n < nslot(s); ++n)
        if (ring(s, n) == (g + 1) * 16 + 15) c = imin(c, between(s, n, g, wslot(g)) + (dstore(s, n) ? 1 : 0));
  }
  if (g == 0) c = 16;
  if (g - NF == HFS) c = imin(c, between(NF + sslot(7) / 32, sslot(7) % 32, g, 24));
  if (g - NF == HCK) c = imin(c, between(NF + HFL, 25, g, 24));
  return c;
}
constexpr int bslot(int b) { return b < NF + 3 ? b % 3 : ((b - NF) % 4 == 3 ? 3 : (NF + (b - NF) % 4) % 3); }
}
__global__ __launch_bounds__(256, 1) void gemm4_kernel(const bf16* __restrict__ At, const bf16* __restrict__ W,
                                                       const bf16* __restrict__ Yt, const float* __restrict__ modp,
                                                       bf16* __restrict__ C, float* xbuf, unsigned* flags) {
  using namespace g4;
  constexpr int KB = F_DIM / 32;
  constexpr int GT = 96 * 1024;
  __shared__ __attribute__((aligned(16))) char smem[GT + 1024];
  STAMPDECL;
  STAMP(6, 0);
  const int tid = threadIdx.x, lane = tid & 63;
  const int w = __builtin_amdgcn_readfirstlane(tid >> 6);
  const int xcd = blockIdx.x & 7, idx = blockIdx.x >> 3;
  const int kh = xcd & 1, bm = ((xcd >> 1) & 1) * 8 + (idx & 7), bn = (xcd >> 2) * 4 + (idx >> 3);
  const unsigned partner = blockIdx.x ^ 1;
  const int n0 = bn * 128, bm64 = bm * 2 + kh;
  const int fr = lane & 15, fq = lane >> 4;
  const rsrc_t rsA0 = mk_rsrc((const char*)At + (size_t)(bm64 ^ 1) * KB * 4096, KB * 4096);
  const rsrc_t rsA1 = mk_rsrc((const char*)At + (size_t)bm64 * KB * 4096, KB * 4096);
  const rsrc_t rsB = mk_rsrc((const char*)W + n0 * 2, KB * 65536 - n0 * 2);
  const unsigned voffL = lane * 16;
  unsigned voffA[4];
#pragma unroll
  for (int i = 0; i < 4; ++i) voffA[i] = voffL + ((i + w) & 3) * 1024;
  const unsigned voffB = fq * 8 * 2048 + fr * 16;
  u32x4 braw[4][8], afr[3][8], afa[4][4];
  u32x4 T[2][2];
  u32x4 xv[4], pv[8];
  unsigned gv[4], flg;
  f32x4 acc[8][8];
#pragma unroll
  for (int r = 0; r < 3; ++r)
#pragma unroll
    for (int j = 0; j < 8; ++j) asm volatile("" : "=v"(braw[r][j]), "=v"(afr[r][j]));
  const int kbF = (kh ? KB - 4 * NF : 0) + w, kbH = w;
#define ELOAD(B, L)                                                                                \
  do {                                                                                             \
    const unsigned kbv = (unsigned)(((B) < NF ? kbF : kbH) + (B) * 4);                             \
    if constexpr ((L) < 8) {                                                                       \
      const unsigned so = kbv * 65536u + ((L) >> 1) * 4096u;                                       \
      if constexpr (bslot(B) == 3 || (B) >= NF + 3) asm volatile("" : "=v"(braw[bslot(B)][(L) & 7]));        \
      if constexpr ((L) & 1) bload_v<2048>(braw[bslot(B)][(L) & 7], voffB, rsB, so);               \
      else bload_v<0>(braw[bslot(B)][(L) & 7], voffB, rsB, so);                                    \
    } else if constexpr ((B) >= NF + 2) {                                                          \
      asm volatile("" : "=a"(afa[((B) - NF) & 3][(L) & 3]));                                       \
      bload_a<0>(afa[((B) - NF) & 3][(L) & 3], voffA[(L) & 3], rsA1, kbv * 4096u);                 \
    } else {                                                                                       \
      bload_v<0>(afr[(B) % 3][(L) & 7], voffA[(L) & 3], ((L) & 4) ? rsA1 : rsA0, kbv * 4096u);      \
    }                                                                                              \
  } while (0)
#define MFMA_E(C, B, A) asm volatile("v_mfma_f32_16x16x32_bf16 %0, %1, %2, %0" : "+a"(C) : "v"(B), "v"(A))
  static_for<16>([&](auto L) { ELOAD(0, L.value); });
  static_for<16>([&](auto L) { ELOAD(1, L.value); });
#pragma unroll
  for (int i = 0; i < 8; ++i)
#pragma unroll
    for (int f = 0; f < 8; ++f) { acc[i][f] = f32x4{0, 0, 0, 0}; asm volatile("" : "+a"(acc[i][f])); }
  const unsigned sbase = (unsigned)(uintptr_t)smem + voffL;
  unsigned wlo[3];
#pragma unroll
  for (int i = 1; i < 4; ++i) wlo[i - 1] = sbase + (((w + i) & 3) * 3 + (i - 1)) * 8192;
  const unsigned rlo = sbase + w * 24576;
  const rsrc_t rsEx = mk_rsrc(xbuf, 256 * 32768);
  const unsigned exo = (unsigned)(blockIdx.x * 32768 + w * 8192);
  const rsrc_t rsFo = mk_rsrc(flags + blockIdx.x * 4 + w, 4), rsFi = mk_rsrc(flags + partner * 4 + w, 4);
  wait_vm<16>();
  STAMPNW(6, 1);
#pragma unroll
  for (int q = 0; q < 8; ++q) DPERM(0, q, 0, 0);
  asm volatile("s_nop 1");
  u32x4 pr[3][4];
  f32x4 tt[8], uu;
  static_for<NS>([&](auto G) {
    constexpr int g = G.value, h = g - NF, sl = bslot(g), ns = bslot(g + 1);
    constexpr bool full = g < NF - 1, lastf = g == NF - 1;
    constexpr bool red = (h == 0 || h == 1);
    constexpr int fb = h == 1 ? 4 : 0;
    if constexpr (h == 0 || h == 1) { WAIT_LGKM0(); __builtin_amdgcn_s_barrier(); }
    if constexpr (h == 3) __builtin_amdgcn_s_barrier();
    static_for<nslot(g)>([&](auto N) {
      constexpr int n = N.value;
      if constexpr (n == wslot(g)) {
        if constexpr (waitcnt(g) < 63) {
          if constexpr (h == HCK) asm volatile("s_waitcnt vmcnt(%1)" : "+v"(flg) : "n"(waitcnt(g)) : "memory");
          else wait_vm<waitcnt(g)>();
        }
        if constexpr (h == HFS) asm volatile("buffer_store_dword %0, %1, %2, 0 offen sc1" ::"v"(1u), "v"(voffL >> 2), "s"(rsFo) : "memory");
        if constexpr (h == HCK) {
          unsigned spins = 0;
          while (__builtin_amdgcn_readfirstlane(flg) == 0u && spins < (1u << 20)) {
            __builtin_amdgcn_s_sleep(1);
            bload1_sc1(flg, 0u, rsFi);
            asm volatile("s_waitcnt vmcnt(0)" : "+v"(flg)::"memory");
            ++spins;
          }
        }
      }
      if constexpr (red && n == 8) WAIT_LGKM0();
      if constexpr (full) {
        constexpr int p = n >> 4, q = n & 15;
        MFMA_E(acc[q & 7][2 * p + (q >> 3)], T[p & 1][q >> 3], afr[g % 3][q & 7]);
        if constexpr ((q & 1) == 0) {
          if constexpr (p < 3) DPERM((p + 1) & 1, q >> 1, sl, p + 1);
          else DPERM(0, q >> 1, ns, 0);
        }
      } else {
        constexpr int p = (n >> 3) & 3, q = n & 7, ib = (lastf && n < 32) ? 0 : 4;
        if constexpr (h >= 2) MFMA_A(acc[ib + (q & 3)][2 * p + (q >> 2)], T[p & 1][q >> 2], afa[h & 3][q & 3]);
        else MFMA_E(acc[ib + (q & 3)][2 * p + (q >> 2)], T[p & 1][q >> 2], afr[g % 3][ib + (q & 3)]);
        if constexpr (p < 3) DPERM((p + 1) & 1, q, sl, p + 1);
        else if constexpr (ib == 0) DPERM(0, q, sl, 0);
        else if constexpr (g + 1 < NS) DPERM(0, q, ns, 0);
      }
      if constexpr (ring(g, n) >= 0) ELOAD((ring(g, n) >> 4), (ring(g, n) & 15));
      if constexpr ((lastf || h == 0) && pwrite(g, n) >= 0) {
        constexpr int e = pwrite(g, n), i = 1 + e % 3, f = e / 3;
        dsw128a<f * 1024>(wlo[i - 1], acc[i][f]);
      }
      if constexpr (red) {
        if constexpr (n < 6) dsr128<(n % 3) * 8192 + (fb + n / 3) * 1024>(pr[n % 3][n / 3], rlo);
        if constexpr (n == 6 || n == 7) {
          static_for<3>([&](auto E) { dsr128<E.value * 8192 + (fb + n - 4) * 1024>(pr[E.value][n - 4], rlo); });
        }
        if constexpr (n >= 8) {
          constexpr int o0 = red0(n), o1 = red0(n + 1);
          static_for<o1 - o0>([&](auto O) {
            constexpr int o = o0 + O.value, fl = o / 16, k = o % 16, r = k & 3;
            (void)&uu; (void)&tt; (void)&pr;
            if constexpr (k < 4) asm volatile("v_accvgpr_read_b32 %0, %1" : "=v"(tt[fb + fl][r]) : "a"(acc[0][fb + fl][r]));
            else if constexpr (k < 8) asm volatile("v_add_f32 %0, %0, %1" : "+v"(tt[fb + fl][r]) : "v"(pr[0][fl][r]));
            else if constexpr (k < 12) asm volatile("v_add_f32 %0, %1, %2" : "=v"(uu[r]) : "v"(pr[1][fl][r]), "v"(pr[2][fl][r]));
            else asm volatile("v_add_f32 %0, %0, %1" : "+v"(tt[fb + fl][r]) : "v"(uu[r]));
          });
        }
      }
      if constexpr (h >= 0 && dstore(g, n)) {
        constexpr int f = dstore_f(g, n);
        bstore_sc1<(f & 3) * 1024>(__builtin_bit_cast(u32x4, tt[f]), voffL, rsEx, exo + (f >> 2) * 4096);
      }
      if constexpr (h == HFL && n == 25) {
        asm volatile("" : "=v"(flg));
        bload1_sc1(flg, 0u, rsFi);
      }
      if constexpr (h == HCK && n >= 24) {
        constexpr int f = n - 24;
        asm volatile("" : "=v"(pv[f]));
        bload_sc1<(f & 3) * 1024>(pv[f], voffL, rsEx, partner * 32768u + w * 8192u + (f >> 2) * 4096u);
      }
      if constexpr (h == HEP && n == 1) {
        const rsrc_t rsG = mk_rsrc(modp + (w >= 2 ? 4096 : 5120), 4 * 6144 * 4);
        const unsigned vo = (unsigned)((n0 + (tid & 127)) * 4);
#pragma unroll
        for (int r = 0; r < 4; ++r) { asm volatile("" : "=v"(gv[r])); bload1_v<0>(gv[r], vo, rsG, r * 24576); }
      }
      if constexpr (h == HEP && n == 3) {
        const rsrc_t rsX = mk_rsrc(Yt, S_LEN * D_DIM * 2);
        const unsigned vx = (unsigned)(((bm64 >> 2) * 32 + bn * 4 + fq) * 16384 + ((bm64 & 3) * 4 + w) * 1024 + fr * 16);
#pragma unroll
        for (int r = 0; r < 4; ++r) asm volatile("" : "=v"(xv[r]));
        bload_v<0>(xv[0], vx, rsX, 0); bload_v<256>(xv[1], vx, rsX, 0); bload_v<512>(xv[2], vx, rsX, 0); bload_v<768>(xv[3], vx, rsX, 0);
      }
      if constexpr (g == NS - 1 && n >= 8 && (n & 7) >= 2) {
        constexpr int e = (n & 7) - 2, i = 1 + e % 3, f = 2 * ((n >> 3) - 1) + e / 3;
        dsw128a<f * 1024>(wlo[i - 1], acc[4 + i][f]);
      }
    });
  });
  asm volatile("s_nop 15" ::: "memory");
#pragma unroll
  for (int f = 0; f < 8; ++f) asm volatile("" : "+a"(acc[4][f]));
  static_for<6>([&](auto E) { constexpr int i = 1 + E.value % 3, f = 6 + E.value / 3; dsw128a<f * 1024>(wlo[i - 1], acc[4 + i][f]); });
  asm volatile("s_waitcnt vmcnt(%8)" : "+v"(xv[0]), "+v"(xv[1]), "+v"(xv[2]), "+v"(xv[3]), "+v"(gv[0]), "+v"(gv[1]), "+v"(gv[2]), "+v"(gv[3]) : "n"(between(NF + HEP, 3, NS - 1, 32)) : "memory");
  STAMP(6, 2); STAMPFLUSH(6, 1);
  float* gtab = reinterpret_cast<float*>(smem + GT);
  {
    float sum = (__uint_as_float(gv[0]) + __uint_as_float(gv[1])) + (__uint_as_float(gv[2]) + __uint_as_float(gv[3]));
    if (w >= 2) {
      sum += 1.0f;
      if (__builtin_fabsf(sum) < 8.67361738e-19f) sum = 8.67361738e-19f;
      sum = 1.0f / sum;
    }
    const int col = tid & 127;
    gtab[(tid & 128) + (col & 96) + (col & 7) * 4 + ((col >> 3) & 3)] = sum;
  }
  asm volatile("s_waitcnt lgkmcnt(0)" ::: "memory");
  __builtin_amdgcn_s_barrier();
  u32x4 pq[3][8], gt[8], ct[8];
  f32x4 fin[8];
  {
    const unsigned gsrc = (unsigned)(uintptr_t)smem + GT + fq * 128;
    static_for<24>([&](auto E) { dsr128<E.value * 1024>(pq[E.value >> 3][E.value & 7], rlo); });
    static_for<8>([&](auto F) { dsr128<F.value * 16>(gt[F.value], gsrc); });
    static_for<8>([&](auto F) { dsr128<512 + F.value * 16>(ct[F.value], gsrc); });
#pragma unroll
    for (int f = 0; f < 8; ++f) { fin[f] = acc[4][f]; asm volatile("" : "+v"(fin[f])); }
    asm volatile("s_waitcnt lgkmcnt(0)" ::: "memory");
#pragma unroll
    for (int f = 0; f < 8; ++f) asm volatile("" : "+v"(pq[0][f]), "+v"(pq[1][f]), "+v"(pq[2][f]), "+v"(gt[f]), "+v"(ct[f]));
  }
#pragma unroll
  for (int f = 0; f < 8; ++f) {
    fin[f] = (fin[f] + __builtin_bit_cast(f32x4, pq[0][f])) + (__builtin_bit_cast(f32x4, pq[1][f]) + __builtin_bit_cast(f32x4, pq[2][f]));
    asm volatile("" : "+v"(fin[f]));
  }
  asm volatile("s_waitcnt vmcnt(0)" : "+v"(pv[0]), "+v"(pv[1]), "+v"(pv[2]), "+v"(pv[3]), "+v"(pv[4]), "+v"(pv[5]), "+v"(pv[6]), "+v"(pv[7])::"memory");
  STAMP(6, 6);
#pragma unroll
  for (int f = 0; f < 8; ++f) {
    fin[f] += __builtin_bit_cast(f32x4, pv[f]);
    f32x4 xf;
#pragma unroll
    for (int r = 0; r < 4; ++r) xf[r] = (f & 1) ? bf_hi(xv[r][f >> 1]) : bf_lo(xv[r][f >> 1]);
    xf *= __builtin_bit_cast(f32x4, ct[f]);
    fin[f] = xf + __builtin_bit_cast(f32x4, gt[f]) * fin[f];
  }
  {
    char* tb = smem + w * 24576;
#pragma unroll
    for (int r = 0; r < 4; ++r) {
      const u32x4 ov = {pk_bf16(fin[0][r], fin[1][r]), pk_bf16(fin[2][r], fin[3][r]), pk_bf16(fin[4][r], fin[5][r]), pk_bf16(fin[6][r], fin[7][r])};
      *reinterpret_cast<u32x4*>(tb + fr * 256 + (((fq * 4 + r) ^ (fr & 7)) * 16)) = ov;
    }
#pragma unroll
    for (int j = 0; j < 4; ++j) {
      const int row = j * 4 + fq;
      const u32x4 ov = *reinterpret_cast<const u32x4*>(tb + row * 256 + ((fr ^ (row & 7)) * 16));
      gst128(C + (size_t)(bm64 * 64 + w * 16 + row) * D_DIM + n0 + fr * 8, ov);
    }
  }
  STAMP(6, 3);
  STAMP(6, 4);
}

extern "C" void kernel_launch(void* const* d_in, const int* in_sizes, int n_in, void* d_out, int out_size,
                              void* d_ws, size_t ws_size, hipStream_t stream) {
  const bf16* x = (const bf16*)d_in[0];
  const bf16* wqkv = (const bf16*)d_in[1];
  const bf16* wo = (const bf16*)d_in[2];
  const bf16* wg = (const bf16*)d_in[4];
  const bf16* wu = (const bf16*)d_in[5];
  const bf16* wd = (const bf16*)d_in[6];
  const bf16* c = (const bf16*)d_in[7];
  const bf16* wmod = (const bf16*)d_in[8];
  bf16* out = (bf16*)d_out;

  char* ws = (char*)d_ws;
  float* modp = (float*)ws;
  float* rstat1 = (float*)(ws + 114688);
  float* stat = (float*)(ws + 131072);
  bf16* ot = (bf16*)(ws + (1 << 20));
  bf16* yt = (bf16*)(ws + (9 << 20));
  bf16* ht = (bf16*)(ws + (13 << 20));
  bf16* act = (bf16*)(ws + (17 << 20));
  float* xbuf = (float*)(ws + (32 << 20));
  unsigned* flags = (unsigned*)(ws + 262144);

  pre_kernel<<<320, 512, 0, stream>>>(x, c, wmod, modp, rstat1, wqkv);
  ln_apply_kernel<<<256, 256, 0, stream>>>(x, modp, rstat1, ht, wqkv, flags);
  qkv_attn_kernel<<<256, 448, 0, stream>>>(ht, wqkv, ot, wg, wu, wo);
  gemm_direct<8, 4, 0><<<256, 256, 0, stream>>>(ot, wo, x, modp, nullptr, yt, stat, wg, wu);
  gemm3_kernel<<<256, 512, 0, stream>>>(yt, wg, wu, act, stat, modp, wd);
  gemm4_kernel<<<256, 256, 0, stream>>>(act, wd, yt, modp, out, xbuf, flags);
}
```

```cpp
#include <hip/hip_runtime.h>
#include <hip/hip_bf16.h>
#include <stdint.h>

typedef __hip_bfloat16 bf16;
typedef __attribute__((ext_vector_type(8))) short bf16x8;
typedef __attribute__((ext_vector_type(4))) float f32x4;
typedef __attribute__((ext_vector_type(2))) float f32x2;
typedef __attribute__((ext_vector_type(4))) unsigned u32x4;
typedef __attribute__((ext_vector_type(2))) unsigned u32x2;
typedef __attribute__((ext_vector_type(2))) __bf16 bf16x2_t;
typedef __amdgpu_buffer_rsrc_t rsrc_t;

#define DI __device__ __forceinline__
#define QBAR 2
#define QRN 17
#define TQ 28
#define DWO 1
#define DW0 4
#define DWS 6
#define CODEPF 53

#define STAMP(k, s) do { } while (0)
#define STAMPT(k, s, T) do { } while (0)
#define STAMPNW(k, s)
#define STAMPFLUSH(k, s)
#define STAMPDECL

constexpr int S_LEN = 2048;
constexpr int D_DIM = 1024;
constexpr int F_DIM = 3584;

template <int V> struct ic { static constexpr int value = V; };
template <int N, int I = 0, class F>
DI void static_for(F&& f) {
  if constexpr (I < N) { f(ic<I>{}); static_for<N, I + 1>(f); }
}

DI float bf_lo(unsigned u) { return __uint_as_float(u << 16); }
DI float bf_hi(unsigned u) { return __uint_as_float(u & 0xffff0000u); }
DI unsigned pk_bf16(float lo, float hi) {
  const f32x2 v = {lo, hi};
  return __builtin_bit_cast(unsigned, __builtin_convertvector(v, bf16x2_t));
}
DI rsrc_t mk_rsrc(const void* p, unsigned bytes) { return __builtin_amdgcn_make_buffer_rsrc((void*)p, (short)0, (int)bytes, 0x00020000); }

template <int N>
DI void wait_vm() { asm volatile("s_waitcnt vmcnt(%0)" ::"n"(N > 63 ? 63 : N) : "memory"); }
#define WAIT_LGKM0() do { asm volatile("s_waitcnt lgkmcnt(0)" ::: "memory"); __builtin_amdgcn_sched_barrier(0); } while (0)
#define SCHED0() __builtin_amdgcn_sched_barrier(0)

#define MFMA_A(C, B, A) asm volatile("v_mfma_f32_16x16x32_bf16 %0, %1, %2, %0" : "+a"(C) : "v"(B), "a"(A))
#define MFMA_AV(C, B, A) asm volatile("v_mfma_f32_16x16x32_bf16 %0, %1, %2, %0" : "+v"(C) : "v"(B), "v"(A))
#define PERM_LO(D, X, Y) asm volatile("v_perm_b32 %0, %1, %2, %3" : "=v"(D) : "v"(Y), "v"(X), "s"(0x05040100u))
#define PERM_HI(D, X, Y) asm volatile("v_perm_b32 %0, %1, %2, %3" : "=v"(D) : "v"(Y), "v"(X), "s"(0x07060302u))
template <int OFF>
DI void bload_v(u32x4& d, unsigned voff, rsrc_t rs, unsigned soff) {
  asm volatile("buffer_load_dwordx4 %0, %1, %2, %3 offen offset:%4" : "+v"(d) : "v"(voff), "s"(rs), "s"(soff), "i"(OFF) : "memory");
}
template <int OFF>
DI void bload_a(u32x4& d, unsigned voff, rsrc_t rs, unsigned soff) {
  asm volatile("buffer_load_dwordx4 %0, %1, %2, %3 offen offset:%4" : "+a"(d) : "v"(voff), "s"(rs), "s"(soff), "i"(OFF) : "memory");
}
template <int OFF>
DI void bload2_v(u32x2& d, unsigned voff, rsrc_t rs, unsigned soff) {
  asm volatile("buffer_load_dwordx2 %0, %1, %2, %3 offen offset:%4" : "+v"(d) : "v"(voff), "s"(rs), "s"(soff), "i"(OFF) : "memory");
}
template <int OFF>
DI void bload1_v(unsigned& d, unsigned voff, rsrc_t rs, unsigned soff) {
  asm volatile("buffer_load_dword %0, %1, %2, %3 offen offset:%4" : "+v"(d) : "v"(voff), "s"(rs), "s"(soff), "i"(OFF) : "memory");
}
template <int OFF>
DI void bload_lds(unsigned m0v, unsigned voff, rsrc_t rs, unsigned soff) {
  asm volatile("s_mov_b32 m0, %0\n\ts_nop 0\n\tbuffer_load_dwordx4 %1, %2, %3 offen offset:%4 lds" ::"s"(m0v), "v"(voff), "s"(rs), "s"(soff), "i"(OFF) : "memory");
}
DI void touch8k(unsigned m0v, unsigned voff, rsrc_t rs, unsigned soff) {
  asm volatile("s_mov_b32 m0, %0\n\ts_nop 0\n\tbuffer_load_dword %1, %2, %3 offen lds" ::"s"(m0v), "v"(voff), "s"(rs), "s"(soff) : "memory");
}
template <int OFF>
DI void dsr128(u32x4& d, unsigned addr) { asm volatile("ds_read_b128 %0, %1 offset:%2" : "=v"(d) : "v"(addr), "i"(OFF) : "memory"); }
template <int OFF>
DI void dsr64(u32x2& d, unsigned addr) { asm volatile("ds_read_b64 %0, %1 offset:%2" : "=v"(d) : "v"(addr), "i"(OFF) : "memory"); }
template <int OFF>
DI void dsr_tr(u32x2& d, unsigned addr) { asm volatile("ds_read_b64_tr_b16 %0, %1 offset:%2" : "=v"(d) : "v"(addr), "i"(OFF) : "memory"); }
template <int OFF>
DI void dsw128(unsigned addr, u32x4 d) { asm volatile("ds_write_b128 %0, %1 offset:%2" ::"v"(addr), "v"(d), "i"(OFF) : "memory"); }
template <int OFF>
DI void dsw128a(unsigned addr, const f32x4& d) { asm volatile("ds_write_b128 %0, %1 offset:%2" ::"v"(addr), "a"(d), "i"(OFF) : "memory"); }
template <int OFF>
DI void dsw32(unsigned addr, unsigned d) { asm volatile("ds_write_b32 %0, %1 offset:%2" ::"v"(addr), "v"(d), "i"(OFF) : "memory"); }

DI void gst128(void* p, u32x4 v) {
  asm volatile("global_store_dwordx4 %0, %1, off sc1\n\ts_nop 1" ::"v"(p), "v"(v) : "memory");
}
DI f32x2 lo2(f32x4 v) { return __builtin_shufflevector(v, v, 0, 1); }
DI f32x2 hi2(f32x4 v) { return __builtin_shufflevector(v, v, 2, 3); }
DI float rowsum4(float v) {
  const unsigned u = __float_as_uint(v);
  const auto a = __builtin_amdgcn_permlane16_swap(u, u, false, false);
  const unsigned t = __float_as_uint(__uint_as_float(a[0]) + __uint_as_float(a[1]));
  const auto b = __builtin_amdgcn_permlane32_swap(t, t, false, false);
  return __uint_as_float(b[0]) + __uint_as_float(b[1]);
}
DI void split4(const f32x4 v, u32x2& hi, u32x2& lo) {
  hi[0] = pk_bf16(v[0], v[1]); hi[1] = pk_bf16(v[2], v[3]);
  lo[0] = pk_bf16(v[0] - bf_lo(hi[0]), v[1] - bf_hi(hi[0]));
  lo[1] = pk_bf16(v[2] - bf_lo(hi[1]), v[3] - bf_hi(hi[1]));
}
DI int slow_col(const void* p) { return (int)((3u - ((unsigned)(uintptr_t)p >> 7)) & 7u); }
DI float silu_f(float v) { return v / (1.0f + __expf(-v)); }

__global__ __launch_bounds__(512) void pre_kernel(const bf16* __restrict__ x, const bf16* __restrict__ c,
                                                  const bf16* __restrict__ wmod, float* __restrict__ modp,
                                                  float* __restrict__ rstat1) {
  __shared__ __attribute__((aligned(16))) float red[8 * 2 * 128 + 256];
  STAMP(0, 0);
  const int tid = threadIdx.x, lane = tid & 63, w = tid >> 6;
  if (blockIdx.x < 192) {
    const int fr = lane & 15, fq = lane >> 4;
    const int cg = blockIdx.x >> 2, kq = blockIdx.x & 3;
    const int col0 = cg * 128;
    const int k0 = kq * 256 + w * 32 + fq * 8;
    u32x4 raw[8];
#pragma unroll
    for (int j = 0; j < 8; ++j) {
      const u32x4* p_ = reinterpret_cast<const u32x4*>(wmod + (size_t)(k0 + j) * 6144 + col0 + fr * 8);
      raw[j] = __builtin_nontemporal_load(p_);
    }
    const u32x4 cv = *reinterpret_cast<const u32x4*>(c + k0);
    u32x4 cf;
#pragma unroll
    for (int e = 0; e < 4; ++e) {
      const float a = silu_f(bf_lo(cv[e])), b = silu_f(bf_hi(cv[e]));
      const unsigned hi = pk_bf16(a, b);
      const unsigned lo = pk_bf16(a - bf_lo(hi), b - bf_hi(hi));
      cf[e] = (fr == 0) ? hi : ((fr == 1) ? lo : 0u);
    }
    f32x4 acc[8];
#pragma unroll
    for (int p = 0; p < 4; ++p) {
      u32x4 lo, hi;
#pragma unroll
      for (int t = 0; t < 4; ++t) {
        lo[t] = __builtin_amdgcn_perm(raw[2 * t + 1][p], raw[2 * t][p], 0x05040100u);
        hi[t] = __builtin_amdgcn_perm(raw[2 * t + 1][p], raw[2 * t][p], 0x07060302u);
      }
      acc[2 * p] = __builtin_amdgcn_mfma_f32_16x16x32_bf16(__builtin_bit_cast(bf16x8, lo), __builtin_bit_cast(bf16x8, cf), f32x4{0, 0, 0, 0}, 0, 0, 0);
      acc[2 * p + 1] = __builtin_amdgcn_mfma_f32_16x16x32_bf16(__builtin_bit_cast(bf16x8, hi), __builtin_bit_cast(bf16x8, cf), f32x4{0, 0, 0, 0}, 0, 0, 0);
    }
    if (fr < 2) {
#pragma unroll
      for (int r = 0; r < 4; ++r) {
        float* d = red + (w * 2 + fr) * 128 + fq * 32 + r * 8;
        *reinterpret_cast<f32x4*>(d) = f32x4{acc[0][r], acc[1][r], acc[2][r], acc[3][r]};
        *reinterpret_cast<f32x4*>(d + 4) = f32x4{acc[4][r], acc[5][r], acc[6][r], acc[7][r]};
      }
    }
    __syncthreads();
    if (tid < 128) {
      float s = 0;
#pragma unroll
      for (int g = 0; g < 16; ++g) s += red[g * 128 + tid];
      modp[kq * 6144 + col0 + tid] = s;
    }
    STAMP(0, 1);
    return;
  }
  const int row0 = (blockIdx.x - 192) * 16 + w * 2;
  u32x4 a[2][2];
#pragma unroll
  for (int i = 0; i < 2; ++i) {
    a[i][0] = *reinterpret_cast<const u32x4*>(x + (size_t)(row0 + i) * D_DIM + lane * 8);
    a[i][1] = *reinterpret_cast<const u32x4*>(x + (size_t)(row0 + i) * D_DIM + 512 + lane * 8);
  }
  if (blockIdx.x < 192 + 8) {
    unsigned long long pc;
    asm volatile("s_getpc_b64 %0" : "=s"(pc));
    const rsrc_t rsC = mk_rsrc((const void*)(uintptr_t)(pc & ~255ull), CODEPF * 1024);
#pragma unroll
    for (int i = 0; i < (CODEPF + 7) / 8; ++i) bload_lds<0>((unsigned)(uintptr_t)(red + 2048), tid * 16, rsC, i * 8192);
  }
#pragma unroll
  for (int i = 0; i < 2; ++i) {
    float v[16];
#pragma unroll
    for (int j = 0; j < 8; ++j) { v[2 * j] = bf_lo(a[i][j >> 2][j & 3]); v[2 * j + 1] = bf_hi(a[i][j >> 2][j & 3]); }
    float s = 0;
#pragma unroll
    for (int j = 0; j < 16; ++j) s += v[j];
#pragma unroll
    for (int o = 32; o > 0; o >>= 1) s += __shfl_xor(s, o);
    const float mean = s * (1.0f / D_DIM);
    float q = 0;
#pragma unroll
    for (int j = 0; j < 16; ++j) { const float d = v[j] - mean; q += d * d; }
#pragma unroll
    for (int o = 32; o > 0; o >>= 1) q += __shfl_xor(q, o);
    const float rstd = rsqrtf(q * (1.0f / D_DIM) + 1e-5f);
    if (lane == 0) *reinterpret_cast<f32x2*>(rstat1 + (row0 + i) * 2) = f32x2{rstd, -mean * rstd};
  }
  STAMP(0, 2);
}

__global__ __launch_bounds__(256) void ln_apply_kernel(const bf16* __restrict__ x, const float* __restrict__ modp,
                                                       const float* __restrict__ rstat1, bf16* __restrict__ H,
                                                       const bf16* __restrict__ Wq, const bf16* __restrict__ Wo,
                                                       unsigned* __restrict__ flags, char* __restrict__ wc) {
  __shared__ __attribute__((aligned(16))) float tab[4][64];
  __shared__ __attribute__((aligned(16))) char sink[1024];
  STAMP(1, 0);
  const int lane = threadIdx.x & 63, w = threadIdx.x >> 6;
  const int fr = lane & 15, fq = lane >> 4;
  const int kb = (blockIdx.x & 7) * 4 + w, mb0 = (blockIdx.x >> 3) * 4;
  u32x4 mr, xv[4], cp;
  u32x2 rr[4];
  asm volatile("" : "=v"(mr), "=v"(cp));
  const int wu_ = __builtin_amdgcn_readfirstlane(w);
  const int crow = (wu_ < 3 ? blockIdx.x * 12 + wu_ * 4 : blockIdx.x * 4) + ((lane >> 3) & 3);
  {
    bload_v<0>(mr, (unsigned)((fq * 6144 + ((lane >> 3) & 1) * 1024 + kb * 32 + (lane & 7) * 4) * 4), mk_rsrc(modp, 4 * 6144 * 4), 0);
    const rsrc_t rsX = mk_rsrc(x, S_LEN * D_DIM * 2), rsS = mk_rsrc(rstat1, S_LEN * 8);
    const unsigned vx = (unsigned)(((mb0 * 16 + fr) * D_DIM + kb * 32 + fq * 8) * 2), vs = (unsigned)((mb0 * 16 + fr) * 8);
#pragma unroll
    for (int i = 0; i < 4; ++i) {
      asm volatile("" : "=v"(xv[i]), "=v"(rr[i]));
      bload_v<0>(xv[i], vx, rsX, i * 32768);
      bload2_v<0>(rr[i], vs, rsS, i * 128);
    }
    const bf16* cs = wu_ < 3 ? Wq : Wo;
    bload_v<0>(cp, (unsigned)(crow * 2048 + (slow_col(cs) + (lane >> 5) * 8) * 128 + (lane & 7) * 16), mk_rsrc(cs, wu_ < 3 ? 3 * D_DIM * D_DIM * 2 : D_DIM * D_DIM * 2), 0);
    const rsrc_t rsP = mk_rsrc((const char*)Wq + (size_t)blockIdx.x * 24576, 24576);
    const unsigned vo = threadIdx.x * 16;
#pragma unroll
    for (int i = 0; i < 6; ++i) bload_lds<0>((unsigned)(uintptr_t)sink, vo, rsP, i * 4096);
#define K1T 6
  }
  asm volatile("s_waitcnt vmcnt(%9)" : "+v"(mr), "+v"(xv[0]), "+v"(xv[1]), "+v"(xv[2]), "+v"(xv[3]), "+v"(rr[0]), "+v"(rr[1]), "+v"(rr[2]), "+v"(rr[3]) : "n"(1 + K1T) : "memory");
  f32x4 mv = __builtin_bit_cast(f32x4, mr);
  f32x2 rs[4];
#pragma unroll
  for (int i = 0; i < 4; ++i) rs[i] = __builtin_bit_cast(f32x2, rr[i]);
#pragma unroll
  for (int e = 0; e < 4; ++e) { mv[e] += __shfl_xor(mv[e], 16); mv[e] += __shfl_xor(mv[e], 32); }
  if (lane < 16) *reinterpret_cast<f32x4*>(&tab[w][lane * 4]) = mv;
  __builtin_amdgcn_wave_barrier();
  asm volatile("s_waitcnt lgkmcnt(0)" ::: "memory");
  const f32x4 s0 = *reinterpret_cast<const f32x4*>(&tab[w][fq * 8]), s1 = *reinterpret_cast<const f32x4*>(&tab[w][fq * 8 + 4]);
  const f32x4 g0 = *reinterpret_cast<const f32x4*>(&tab[w][32 + fq * 8]) + 1.0f, g1 = *reinterpret_cast<const f32x4*>(&tab[w][32 + fq * 8 + 4]) + 1.0f;
#pragma unroll
  for (int i = 0; i < 4; ++i) {
    u32x4 ho;
#pragma unroll
    for (int d = 0; d < 4; ++d) {
      const f32x4 gg = d < 2 ? g0 : g1, ss = d < 2 ? s0 : s1;
      const float a = (bf_lo(xv[i][d]) * rs[i][0] + rs[i][1]) * gg[(d & 1) * 2] + ss[(d & 1) * 2];
      const float b = (bf_hi(xv[i][d]) * rs[i][0] + rs[i][1]) * gg[(d & 1) * 2 + 1] + ss[(d & 1) * 2 + 1];
      ho[d] = pk_bf16(a, b);
    }
    gst128((char*)H + ((size_t)(mb0 + i) * 32 + kb) * 1024 + lane * 16, ho);
  }
  asm volatile("s_waitcnt vmcnt(%1)" : "+v"(cp) : "n"(4 + K1T) : "memory");
  gst128(wc + (wu_ < 3 ? (lane >> 5) * 393216 : 786432 + (lane >> 5) * 131072) + crow * 128 + (lane & 7) * 16, cp);
  if (threadIdx.x < 4) {
    flags[blockIdx.x * 4 + threadIdx.x] = 0u;
    flags[1024 + (blockIdx.x * 4 + threadIdx.x) * 4] = 0u;
  }
  STAMP(1, 1);
}

constexpr int Q_SLOT = 21504, Q_NSL = 7;
constexpr int WO_H0 = 4 * Q_SLOT;
constexpr int QX_OFF = Q_SLOT * Q_NSL + 1024;
constexpr int QH_OFF = QX_OFF + 8192;
constexpr int QT_OFF = QH_OFF + 1024;
constexpr int Q_LDS = QT_OFF + 512;
#define C3Q 1
#define C3O 1
#define OMAP 2
#define POLLW 0
#define OD 3
#define OMB 6
template <int OFF>
DI void bload_sc1(u32x4& d, unsigned voff, rsrc_t rs, unsigned soff) {
  asm volatile("buffer_load_dwordx4 %0, %1, %2, %3 offen offset:%4 sc1" : "+v"(d) : "v"(voff), "s"(rs), "s"(soff), "i"(OFF) : "memory");
}
DI void bload1_sc1(unsigned& d, unsigned voff, rsrc_t rs) {
  asm volatile("buffer_load_dword %0, %1, %2, 0 offen sc1" : "+v"(d) : "v"(voff), "s"(rs) : "memory");
}
DI unsigned wo_voff(int lane, int odd, int sh) {
  const int q = (lane >> 3) & 3, h = lane >> 5, ch = lane & 7, g = (q >> 1) | (odd << 1);
  return (unsigned)(((h * 16 + q) << sh) + ((((ch >> 1) ^ g) * 16) + (ch & 1) * 8) * 2);
}
DI rsrc_t wo_rsrc(const bf16* wo, const char* wc, int cn) {
  const bool c3 = C3O && (cn & 7) == slow_col(wo);
  return mk_rsrc(c3 ? wc + 786432 + (cn >> 3) * 131072 : (const char*)wo + cn * 128, c3 ? 131072 : D_DIM * D_DIM * 2 - cn * 128);
}

DI void qkv_loader(unsigned smem_base, int lane, int sw, int m0, int n0, int cn, const bf16* __restrict__ H, const bf16* __restrict__ W,
                   const bf16* __restrict__ wo, const char* __restrict__ wc) {
  const rsrc_t rsH = mk_rsrc(H, S_LEN * D_DIM * 2);
  const bool c3 = C3Q && ((n0 >> 6) & 7) == slow_col(W);
  const rsrc_t rsW = mk_rsrc(c3 ? wc + (n0 >> 9) * 393216 : (const char*)W + n0 * 2, c3 ? 393216 : D_DIM * 3 * D_DIM * 2 - n0 * 2);
  const unsigned sec = c3 ? 128u : 2048u, p16 = sec * 48u;
  int fb0 = (m0 >> 4) - 1 + 3 * sw; fb0 = fb0 < 0 ? 0 : fb0;
  const unsigned voffH0 = (unsigned)(fb0 * 32768 + lane * 16);
  const unsigned voffH1 = (unsigned)(((m0 >> 4) + 3 * sw) * 32768 + lane * 16);
  unsigned voffW[2];
#pragma unroll
  for (int i = 0; i < 2; ++i) {
    const int r = i * 8 + (lane >> 3), ch = lane & 7;
    const int g = ((r >> 1) & 1) | (((r >> 3) & 1) << 1);
    voffW[i] = (unsigned)((r * 3 + sw) * sec + ((((ch >> 1) ^ g) * 16) + (ch & 1) * 8) * 2);
  }
  const unsigned m0a = smem_base + sw * 3072, m0w = smem_base + 9216 + sw * 4096;
#define Q_LOAD(SOFF, U)                                                                                          \
  do {                                                                                                           \
    const unsigned so_ = (unsigned)(U) * 1024u;                                                                  \
    bload_lds<0>(m0a + (SOFF), voffH0, rsH, so_);                                                                \
    bload_lds<0>(m0a + (SOFF) + 1024, voffH1, rsH, so_);                                                         \
    bload_lds<0>(m0a + (SOFF) + 2048, voffH1, rsH, so_ + 32768u);                                                \
    _Pragma("unroll") for (int i = 0; i < 4; ++i)                                                                \
      bload_lds<0>(m0w + (SOFF) + i * 1024, voffW[i & 1], rsW, ((unsigned)(U) * 2u + (i >> 1)) * p16);           \
  } while (0)
#pragma unroll
  for (int u = 0; u < Q_NSL - 1; ++u) Q_LOAD(u * Q_SLOT, u);
  wait_vm<(Q_NSL - 2) * 7>();
  __builtin_amdgcn_s_barrier();
  unsigned sb = (Q_NSL - 1) * Q_SLOT;
  for (int u = 0; u < 33 - Q_NSL; ++u) {
    Q_LOAD(sb, u + Q_NSL - 1);
    sb = (sb == (Q_NSL - 1) * Q_SLOT) ? 0u : sb + Q_SLOT;
    wait_vm<(Q_NSL - 2) * 7>();
    __builtin_amdgcn_s_barrier();
  }
  const rsrc_t rsO = wo_rsrc(wo, wc, cn);
  const int sh = (C3O && (cn & 7) == slow_col(wo)) ? 7 : 11;
  const unsigned voffO[2] = {wo_voff(lane, sw & 1, sh), wo_voff(lane, (sw & 1) ^ 1, sh)};
  static_for<Q_NSL>([&](auto U) {
    constexpr int u = U.value, left = Q_NSL - 3 - u;
    (void)&rsO; (void)&voffO;
    if constexpr (u < 6) {
#pragma unroll
      for (int j = 0; j < 7; ++j) {
        const int pc = (u < 3 ? u * 21 : 64 + (u - 3) * 21) + 3 * j;
        const unsigned p = pc + sw;
        bload_lds<0>(smem_base + (u < 3 ? WO_H0 : -65536) + p * 1024u, voffO[pc & 1], rsO, ((p >> 2) * 32u + (p & 3) * 4u) << sh);
      }
    }
    if constexpr (left >= 0) wait_vm<left * 7 + (u + 1) * 7>();
    __builtin_amdgcn_s_barrier();
  });
  wait_vm<0>();
}

DI void qkv_spare(char* smem, unsigned smem_base, int lane, int cn, const float* __restrict__ modp, const bf16* __restrict__ wo,
                  const bf16* __restrict__ wg, const bf16* __restrict__ wu, const char* __restrict__ wc) {
  const rsrc_t rsO = wo_rsrc(wo, wc, cn);
  const int sh = (C3O && (cn & 7) == slow_col(wo)) ? 7 : 11;
  const unsigned voffO = wo_voff(lane, 1, sh);
  {
    const float* mp = modp + cn * 64 + lane;
    const float ga = (mp[2048] + mp[6144 + 2048]) + (mp[12288 + 2048] + mp[18432 + 2048]);
    float cb = (mp[4096] + mp[6144 + 4096]) + (mp[12288 + 4096] + mp[18432 + 4096]) + 1.0f;
    if (__builtin_fabsf(cb) < 8.67361738e-19f) cb = 8.67361738e-19f;
    float* tab = reinterpret_cast<float*>(smem + QT_OFF);
    tab[lane] = ga; tab[64 + lane] = cb;
  }
  asm volatile("" ::: "memory");
  __builtin_amdgcn_s_barrier();
  const rsrc_t rsP = mk_rsrc((const char*)wo + (size_t)blockIdx.x * 8192, 8192);
  const rsrc_t rsG = mk_rsrc((const char*)wg + (size_t)blockIdx.x * (TQ * 1024), TQ * 1024);
  const rsrc_t rsU = mk_rsrc((const char*)wu + (size_t)blockIdx.x * (TQ * 1024), TQ * 1024);
  static_for<33>([&](auto U) {
    constexpr int u = U.value;
    (void)&rsG; (void)&rsU; (void)&rsP;
    if constexpr (u == 26) bload_lds<0>(smem_base + WO_H0 + 63 * 1024, voffO, rsO, (15 * 32u + 12u) << sh);
    if constexpr (u == 32) bload_lds<0>(smem_base + 63 * 1024, voffO, rsO, (31 * 32u + 12u) << sh);
    if constexpr (u == DWO) touch8k(smem_base + QH_OFF, lane * 128u, rsP, 0);
#pragma unroll
    for (int j = 0; j < 2 * ((TQ + 7) / 8); ++j)
      if (DW0 + (j * DWS) / 2 == u) touch8k(smem_base + QH_OFF, lane * 128u, (j & 1) ? rsU : rsG, (j >> 1) * 8192u);
    __builtin_amdgcn_s_barrier();
  });
  asm volatile("s_waitcnt vmcnt(0) lgkmcnt(0)" ::: "memory");
}

DI void qkv_mfma(char* smem, unsigned smem_base, int tid, int wid, int m0, int n0, bf16* O, unsigned* oflags) {
  const int lane = tid & 63;
  const int wm = wid & 1, wn = wid >> 1;
  const int fr = lane & 15, fq = lane >> 4;
  unsigned brd[6];
  const unsigned hrd = smem_base + lane * 16, ard = hrd + 1024 + wm * 4096;
  {
    const int q = (lane >> 2) & 3, p = lane & 3;
    const int r = 8 * fq + q;
    const int g = ((r >> 1) & 1) | (((r >> 3) & 1) << 1);
#pragma unroll
    for (int jj = 0; jj < 6; ++jj) {
      brd[jj] = smem_base + 9216 + (jj >> 1) * 4096 + r * 128 + (((wn * 2 + ((jj & 1) ^ wm)) ^ g) * 32) + 8 * p;
    }
  }
  f32x4 acc[4][6], acch[2];
#pragma unroll
  for (int i = 0; i < 4; ++i)
#pragma unroll
    for (int j = 0; j < 6; ++j) { acc[i][j] = f32x4{0, 0, 0, 0}; asm volatile("" : "+v"(acc[i][j])); }
#pragma unroll
  for (int j = 0; j < 2; ++j) { acch[j] = f32x4{0, 0, 0, 0}; asm volatile("" : "+v"(acch[j])); }
  __builtin_amdgcn_s_setprio(2);
  __builtin_amdgcn_s_barrier();
  STAMPDECL;
  STAMPNW(2, 1);
  u32x4 aX[5], aY[5];
  u32x2 bXl[6], bXh[6], bYl[6], bYh[6];
#define Q_READ(AR, BL, BH, SO, R)                                                                                \
  do {                                                                                                           \
    if ((R) < 12) {                                                                                              \
      constexpr int jb = ((R) < 12 ? (R) : 0) >> 1;                                                              \
      if ((R) & 1) dsr_tr<512>(BH[jb], brd[jb] + (SO)); else dsr_tr<0>(BL[jb], brd[jb] + (SO));                  \
    } else if ((R) < 16) {                                                                                       \
      constexpr int ia = ((R) >= 12 && (R) < 16) ? (R) - 12 : 0;                                                 \
      dsr128<ia * 1024>(AR[ia], ard + (SO));                                                                     \
    } else {                                                                                                     \
      dsr128<0>(AR[4], hrd + (SO));                                                                              \
    }                                                                                                            \
  } while (0)
#define Q_MFMA(AR, BB, N)                                                                                        \
  do {                                                                                                           \
                                     \
    if ((N) < 16) MFMA_AV(acc[(N) & 3][((N) < 24 ? (N) : 0) >> 2], BB[((N) < 24 ? (N) : 0) >> 2], AR[(N) & 3]);  \
    else if ((N) < 24) MFMA_AV(acc[(N) & 3][((N) < 24 ? (N) : 0) >> 2], AR[(N) & 3], BB[((N) < 24 ? (N) : 0) >> 2]);  \
    else if ((N) == 24) MFMA_AV(acch[0], BB[2], AR[4]);                                                          \
    else MFMA_AV(acch[1], AR[4], BB[4]);                                                                         \
  } while (0)
#define Q_UNIT(AR, BL, BH, NAR, NBL, NBH)                                                                        \
  do {                                                                                                           \
    so = (so == (Q_NSL - 1) * Q_SLOT) ? 0u : so + Q_SLOT;                                                        \
    WAIT_LGKM0();                                                                                                \
    u32x4 bb[6];                                                                                                 \
    _Pragma("unroll") for (int j = 0; j < 6; ++j) bb[j] = u32x4{BL[j][0], BL[j][1], BH[j][0], BH[j][1]};         \
    SCHED0();                                                                                                    \
    asm volatile("s_nop 1");                                                                                     \
    static_for<26>([&](auto N) {                                                                                 \
      constexpr int n = N.value;                                                                                 \
      if constexpr (n == QBAR) __builtin_amdgcn_s_barrier();                                                   \
      Q_MFMA(AR, bb, n);                                                                                         \
      if constexpr (n >= QBAR && n < QBAR + QRN) {                                                               \
        constexpr int r0 = ((n - QBAR) * 17) / QRN, r1 = ((n - QBAR + 1) * 17) / QRN;                            \
        Q_READ(NAR, NBL, NBH, so, r0);                                                                           \
        if constexpr (r0 + 1 < r1) Q_READ(NAR, NBL, NBH, so, (r0 + 1 < 17 ? r0 + 1 : 0));                        \
      }                                                                                                          \
    });                                                                                                          \
    SCHED0();                                                                                                    \
  } while (0)
  unsigned so = 0;
  static_for<17>([&](auto R) { Q_READ(aX, bXl, bXh, 0u, R.value); });
  for (int u = 0; u < 32; u += 2) {
    Q_UNIT(aX, bXl, bXh, aY, bYl, bYh);
    Q_UNIT(aY, bYl, bYh, aX, bXl, bXh);
  }
  asm volatile("s_waitcnt vmcnt(0) lgkmcnt(0)\n\ts_nop 15" ::: "memory");
#pragma unroll
  for (int i = 0; i < 5; ++i) asm volatile("" : "+v"(aX[i]));
#pragma unroll
  for (int j = 0; j < 6; ++j) asm volatile("" : "+v"(bXl[j]), "+v"(bXh[j]));
  SCHED0();
  __builtin_amdgcn_s_setprio(0);
  STAMP(2, 2); STAMPFLUSH(2, 1);
#pragma unroll
  for (int i = 0; i < 4; ++i)
#pragma unroll
    for (int j = 0; j < 6; ++j) asm volatile("" : "+v"(acc[i][j]));
  asm volatile("" : "+v"(acch[0]), "+v"(acch[1]));
  auto pk2 = [](const f32x4 v) { return u32x2{pk_bf16(v[0], v[1]), pk_bf16(v[2], v[3])}; };
  auto cat = [](const u32x2 a, const u32x2 b) { return __builtin_bit_cast(bf16x8, u32x4{a[0], a[1], b[0], b[1]}); };
  u32x2 kp[5][2], vp[5][2];
  {
    char* xb = smem + QX_OFF + wn * 4096 + lane * 8;
#pragma unroll
    for (int f = 0; f < 2; ++f) { kp[4][f] = pk2(acc[3][2 + f]); vp[4][f] = pk2(acc[3][4 + f]); }
    *reinterpret_cast<u32x2*>(xb + wm * 512) = pk2(acch[0]);
    *reinterpret_cast<u32x2*>(xb + 1024 + wm * 512) = pk2(acch[1]);
    if (wm == 0) {
#pragma unroll
      for (int f = 0; f < 2; ++f) {
        *reinterpret_cast<u32x2*>(xb + 2048 + f * 512) = kp[4][f];
        *reinterpret_cast<u32x2*>(xb + 2048 + 1024 + f * 512) = vp[4][f];
      }
    }
    asm volatile("s_waitcnt lgkmcnt(0)" ::: "memory");
    __builtin_amdgcn_s_barrier();
    asm volatile("" ::: "memory");
#pragma unroll
    for (int f = 0; f < 2; ++f) {
      kp[0][f] = *reinterpret_cast<const u32x2*>(xb + wm * 2048 + (f ^ wm) * 512);
      vp[0][f] = *reinterpret_cast<const u32x2*>(xb + wm * 2048 + 1024 + (f ^ wm) * 512);
    }
  }
  STAMP(2, 3);
#pragma unroll
  for (int i = 0; i < 3; ++i)
#pragma unroll
    for (int f = 0; f < 2; ++f) { kp[1 + i][f] = pk2(acc[i][2 + f]); vp[1 + i][f] = pk2(acc[i][4 + f]); }
  const bool par = fr & 1;
  const bool cura = fq * 4 + (fr & 1) <= fr, curb = fq * 4 + 2 + (fr & 1) <= fr;
  const unsigned mh = par ? 0xffff0000u : 0x0000ffffu;
  const unsigned mca = cura ? mh : 0u, mpa = cura ? 0u : mh, mcb = curb ? mh : 0u, mpb = curb ? 0u : mh;
  const float cs = 0.17677669529663687f * 1.4426950408889634f;
  char* op = (char*)O + ((size_t)((m0 >> 6) + wm) * (D_DIM / 32) + (n0 >> 5) + wn) * 4096 + lane * 16;
  const f32x4 z4 = {0, 0, 0, 0};
#pragma unroll
  for (int ii = 0; ii < 4; ++ii) {
    const int i = 3 - ii;
    const bf16x8 qf = cat(pk2(acc[i][0]), pk2(acc[i][1]));
    const f32x4 sc = __builtin_amdgcn_mfma_f32_16x16x32_bf16(cat(kp[i + 1][0], kp[i + 1][1]), qf, z4, 0, 0, 0);
    f32x4 sp = __builtin_amdgcn_mfma_f32_16x16x32_bf16(cat(kp[i][0], kp[i][1]), qf, z4, 0, 0, 0);
    if (i == 0 && m0 == 0 && wm == 0) sp = f32x4{-5.656854249e9f, -5.656854249e9f, -5.656854249e9f, -5.656854249e9f};
    const float la = cura ? (par ? sc[1] : sc[0]) : (par ? sp[1] : sp[0]);
    const float lb = curb ? (par ? sc[3] : sc[2]) : (par ? sp[3] : sp[2]);
    float mx = fmaxf(la, lb);
    {
      const auto a = __builtin_amdgcn_permlane16_swap(__float_as_uint(mx), __float_as_uint(mx), false, false);
      mx = fmaxf(__uint_as_float(a[0]), __uint_as_float(a[1]));
      const auto b = __builtin_amdgcn_permlane32_swap(__float_as_uint(mx), __float_as_uint(mx), false, false);
      mx = fmaxf(__uint_as_float(b[0]), __uint_as_float(b[1]));
    }
    const float ea = __builtin_amdgcn_exp2f((la - mx) * cs), eb = __builtin_amdgcn_exp2f((lb - mx) * cs);
    const float inv = __builtin_amdgcn_rcpf(rowsum4(ea + eb));
    const unsigned wa = pk_bf16(ea, ea), wb = pk_bf16(eb, eb);
    const bf16x8 pf = __builtin_bit_cast(bf16x8, u32x4{wa & mpa, wb & mpb, wa & mca, wb & mcb});
    const f32x4 o0 = __builtin_amdgcn_mfma_f32_16x16x32_bf16(cat(vp[i][0], vp[i + 1][0]), pf, z4, 0, 0, 0) * inv;
    const f32x4 o1 = __builtin_amdgcn_mfma_f32_16x16x32_bf16(cat(vp[i][1], vp[i + 1][1]), pf, z4, 0, 0, 0) * inv;
    const u32x2 u0 = pk2(o0), u1 = pk2(o1);
    gst128(op + i * 1024, wm ? u32x4{u1[0], u1[1], u0[0], u0[1]} : u32x4{u0[0], u0[1], u1[0], u1[1]});
  }
  STAMP(2, 4);
  wait_vm<0>();
  asm volatile("buffer_store_dword %0, %1, %2, 0 offen sc1" ::"v"(1u), "v"(lane * 4), "s"(mk_rsrc(oflags + ((m0 >> 1) + wm * 32 + (n0 >> 5) + wn) * 4, 4)) : "memory");
  STAMP(2, 5);
}

DI void o_phase(char* smem, unsigned smem_base, int lane, int w, int cm, int cn, const bf16* O, const unsigned* oflags,
                const bf16* __restrict__ x, bf16* __restrict__ Y, float* __restrict__ stat,
                const bf16* __restrict__ wg, const bf16* __restrict__ wu) {
  const int wm = w & 3, kh = w >> 2;
  const int fr = lane & 15, fq = lane >> 4;
  const int R = cm * 8 + wm * 2 + kh;
  u32x2 xv[4];
  {
    const rsrc_t rsX = mk_rsrc(x, S_LEN * D_DIM * 2);
    const unsigned vx = (unsigned)(((R * 16 + fr) * D_DIM + cn * 64 + fq * 4) * 2);
#pragma unroll
    for (int f = 0; f < 4; ++f) asm volatile("" : "=v"(xv[f]));
    bload2_v<0>(xv[0], vx, rsX, 0); bload2_v<32>(xv[1], vx, rsX, 0); bload2_v<64>(xv[2], vx, rsX, 0); bload2_v<96>(xv[3], vx, rsX, 0);
  }
  const unsigned hb = smem_base + (kh ? 0 : WO_H0);
  unsigned brd[4];
  {
    const int q = (lane >> 2) & 3, p = lane & 3;
    const int r = 8 * fq + q;
    const int g = ((r >> 1) & 1) | (((r >> 3) & 1) << 1);
#pragma unroll
    for (int f = 0; f < 4; ++f) brd[f] = hb + r * 128 + ((f ^ g) * 32) + 8 * p;
  }
  const rsrc_t rsA = mk_rsrc((const char*)O + (size_t)(cm * 2 + (wm >> 1)) * 131072 + kh * 65536, 65536);
  unsigned va0 = lane * 16 + ((wm & 1) * 2 + kh) * 1024, va1 = va0 ^ 1024;
  unsigned tsrc = smem_base + QT_OFF + fq * 16, xw = hb + lane * 16 + wm * 4096, xr = smem_base + (kh ? WO_H0 : 0) + lane * 16 + wm * 4096;
  char* yb = (char*)Y + ((size_t)(R >> 4) * 32 + cn * 2 + (fq & 1)) * 16384 + (R & 15) * 1024 + ((fq >> 1) * 16 + fr) * 16;
  asm volatile("" : "+v"(tsrc), "+v"(xw), "+v"(xr), "+v"(yb), "+v"(va0), "+v"(va1), "+v"(brd[0]), "+v"(brd[1]), "+v"(brd[2]), "+v"(brd[3]));
  f32x4 acc[2][4];
#pragma unroll
  for (int i = 0; i < 2; ++i)
#pragma unroll
    for (int f = 0; f < 4; ++f) { acc[i][f] = f32x4{0, 0, 0, 0}; asm volatile("" : "+v"(acc[i][f])); }
  if (w == POLLW) {
    const rsrc_t rsF = mk_rsrc(oflags + cm * 256, 1024);
    unsigned spins = 0, f;
    do {
      asm volatile("buffer_load_dword %0, %1, %2, 0 offen sc1\n\ts_waitcnt vmcnt(0)" : "=v"(f) : "v"(lane * 16), "s"(rsF) : "memory");
    } while (!__all(f != 0u) && ++spins < (1u << 18));
  }
  __builtin_amdgcn_s_barrier();
  STAMPDECL;
  STAMPNW(3, 1);
  u32x4 a[16][2], gt[4], ct[4];
  u32x2 bl[2][4], bh[2][4];
  auto rdb = [&](auto S) {
    constexpr int s = S.value;
#pragma unroll
    for (int f = 0; f < 4; ++f) { dsr_tr<s * 4096>(bl[s & 1][f], brd[f]); dsr_tr<s * 4096 + 512>(bh[s & 1][f], brd[f]); }
  };
  auto lda = [&](auto S) {
    constexpr int s = S.value;
    asm volatile("" : "=v"(a[s][0]), "=v"(a[s][1]));
    bload_sc1<0>(a[s][0], va0, rsA, s * 4096);
    bload_sc1<0>(a[s][1], va1, rsA, s * 4096);
  };
  static_for<OD>([&](auto S) {
    lda(S);
    if constexpr (S.value == 0) rdb(ic<0>{});
    if constexpr (S.value == 1) rdb(ic<1>{});
  });
  static_for<16>([&](auto S) {
    constexpr int s = S.value;
    asm volatile("s_waitcnt vmcnt(%0) lgkmcnt(8)" ::"n"(2 * ((s + OD - 1 < 15 ? s + OD - 1 : 15) - s)) : "memory");
    SCHED0();
    u32x4 bb[4];
#pragma unroll
    for (int f = 0; f < 4; ++f) bb[f] = u32x4{bl[s & 1][f][0], bl[s & 1][f][1], bh[s & 1][f][0], bh[s & 1][f][1]};
    SCHED0();
    if constexpr (s + OD < 16) lda(ic<(s + OD < 16 ? s + OD : 0)>{});
    if constexpr (s == OMB) __builtin_amdgcn_s_barrier();
#pragma unroll
    for (int n = 0; n < 8; ++n) MFMA_AV(acc[1 - (n >> 2)][n & 3], bb[n & 3], a[s][1 - (n >> 2)]);
    if constexpr (s < 14) rdb(ic<(s < 14 ? s + 2 : 0)>{});
    if constexpr (s == 14) {
      static_for<4>([&](auto F) { dsr128<F.value * 64>(gt[F.value], tsrc); dsr128<256 + F.value * 64>(ct[F.value], tsrc); });
    }
    SCHED0();
  });
  STAMP(3, 2); STAMPFLUSH(3, 1);
  asm volatile("s_nop 7" ::: "memory");
  static_for<4>([&](auto F) { dsw128<F.value * 1024>(xw, __builtin_bit_cast(u32x4, acc[1][F.value])); });
  asm volatile("s_waitcnt lgkmcnt(0)" ::: "memory");
  __builtin_amdgcn_s_barrier();
  u32x4 pr[4];
  static_for<4>([&](auto F) { dsr128<F.value * 1024>(pr[F.value], xr); });
#pragma unroll
  for (int f = 0; f < 4; ++f) asm volatile("" : "+v"(acc[0][f]), "+v"(xv[f]), "+v"(gt[f]), "+v"(ct[f]));
  asm volatile("s_waitcnt lgkmcnt(0)" : "+v"(pr[0]), "+v"(pr[1]), "+v"(pr[2]), "+v"(pr[3])::"memory");
  f32x4 fin[4];
  u32x2 pk[4];
#pragma unroll
  for (int f = 0; f < 4; ++f) {
    const f32x4 xf = {bf_lo(xv[f][0]), bf_hi(xv[f][0]), bf_lo(xv[f][1]), bf_hi(xv[f][1])};
    fin[f] = xf + __builtin_bit_cast(f32x4, gt[f]) * (acc[0][f] + __builtin_bit_cast(f32x4, pr[f]));
    const f32x4 yv = fin[f] * __builtin_bit_cast(f32x4, ct[f]);
    pk[f] = u32x2{pk_bf16(yv[0], yv[1]), pk_bf16(yv[2], yv[3])};
  }
#pragma unroll
  for (int j = 0; j < 2; ++j) {
    const auto s0 = __builtin_amdgcn_permlane16_swap(pk[j][0], pk[j + 2][0], false, false);
    const auto s1 = __builtin_amdgcn_permlane16_swap(pk[j][1], pk[j + 2][1], false, false);
    gst128(yb + j * 512, u32x4{s0[0], s1[0], s0[1], s1[1]});
  }
  const f32x4 s4 = (fin[0] + fin[1]) + (fin[2] + fin[3]);
  const float sm = rowsum4((s4[0] + s4[1]) + (s4[2] + s4[3]));
  const float mb = sm * (1.0f / 64.0f);
  f32x4 q4 = {0, 0, 0, 0};
#pragma unroll
  for (int f = 0; f < 4; ++f) { const f32x4 d = fin[f] - mb; q4 += d * d; }
  const float m2 = rowsum4((q4[0] + q4[1]) + (q4[2] + q4[3]));
  if (fq == 0) *reinterpret_cast<f32x2*>(stat + ((cn >> 1) * S_LEN + R * 16 + fr) * 4 + (cn & 1) * 2) = f32x2{sm, m2};
}

__global__ __launch_bounds__(512, 2) void qkv_attn_kernel(const bf16* __restrict__ H, const bf16* __restrict__ W,
                                                          bf16* O, const bf16* __restrict__ wg,
                                                          const bf16* __restrict__ wu, const bf16* __restrict__ wo,
                                                          const bf16* __restrict__ x, const float* __restrict__ modp,
                                                          bf16* __restrict__ Y, float* __restrict__ stat, unsigned* oflags,
                                                          const char* __restrict__ wc) {
  __shared__ __attribute__((aligned(16))) char smem[Q_LDS];
  STAMP(2, 0);
  const int tid = threadIdx.x;
  const int wid = __builtin_amdgcn_readfirstlane(tid >> 6);
  const unsigned smem_base = (unsigned)(uintptr_t)smem;
  const int xcd = blockIdx.x & 7, idx = blockIdx.x >> 3;
  const int bm = (xcd >> 2) * 8 + (idx & 7), bn = (xcd & 3) * 4 + (idx >> 3);
  const int m0 = bm * 128, n0 = bn * 64;
  const int cm = (xcd >> 1) * 4 + (idx & 3), cn = __builtin_amdgcn_readfirstlane((xcd & 1) * 8 + (idx >> 2));
  if (wid < 4) qkv_mfma(smem, smem_base, tid, wid, m0, n0, O, oflags);
  else if (wid < 7) qkv_loader(smem_base, tid & 63, wid - 4, m0, n0, cn, H, W, wo, wc);
  else qkv_spare(smem, smem_base, tid & 63, cn, modp, wo, wg, wu, wc);
  STAMPT(2, 6, 320);
  STAMPT(3, 0, 448);
  o_phase(smem, smem_base, tid & 63, wid, cm, cn, O, oflags, x, Y, stat, wg, wu);
  STAMP(3, 3);
  STAMP(3, 4);
}

#define NBR 0
#define TPOS 6
#define G3B 30
#define G3S 0
#define NBC 1
#define G3_TW "12"
#define G3_DUMMY() do { } while (0)
constexpr int G3_CT = 131072 + 4096 + 1024;
#define G3_STAGE(SLOT, P, U)                                                                               \
  do {                                                                                                     \
    if ((P) == 0) bload_lds<0>(m0w + (SLOT) * 32768, voffA, rsA, (unsigned)(U) * 16384u);                  \
    else if ((P) == 1) bload_lds<1024>(m0w + (SLOT) * 32768, voffA, rsA, (unsigned)(U) * 16384u);          \
    else if ((P) == 2) bload_lds<0>(m0w + (SLOT) * 32768 + 16384, voffB0, rsB, (unsigned)(U) * 229376u);   \
    else bload_lds<0>(m0w + (SLOT) * 32768 + 16384 + 1024, voffB1, rsB, (unsigned)(U) * 229376u);          \
  } while (0)

template <int FN>
DI void g3_body(char* smem, unsigned smem_base, int wm, int wn, int wid, int lane, int bm, int bn, rsrc_t rsA, rsrc_t rsB,
                rsrc_t rsP, unsigned voffA, unsigned voffB0, unsigned voffB1, bf16* __restrict__ act) {
  constexpr int NB = 2 * FN, NM = 4 * NB, RD = 5 + 2 * NB;
  const int fr = lane & 15, fq = lane >> 4;
  f32x4 acc[4][NB], accC[2];
  accC[0] = f32x4{0, 0, 0, 0}; accC[1] = f32x4{0, 0, 0, 0};
  asm volatile("" : "+v"(accC[0]), "+v"(accC[1]));
  const int rot = wm % FN;
  unsigned cad = smem_base + G3_CT + (fr < 4 ? fr * 64 : 256) + fq * 16;
#pragma unroll
  for (int i = 0; i < 4; ++i)
#pragma unroll
    for (int j = 0; j < NB; ++j) { acc[i][j] = f32x4{0, 0, 0, 0}; asm volatile("" : "+v"(acc[i][j])); }
  unsigned aad[2], bad[2][FN];
  aad[0] = smem_base + wm * 4096 + lane * 16; aad[1] = aad[0] + 65536;
  {
    const int q = (lane >> 2) & 3, p = lane & 3;
    const int r = 8 * fq + q;
    const int sw = (r & 3) | (((r >> 3) & 1) << 2);
#pragma unroll
    for (int j = 0; j < FN; ++j) {
      const int gi = wn * 4 + (j + rot) % FN + (bn & 1);
      bad[0][j] = smem_base + 16384 + r * 256 + ((gi ^ sw) * 32) + 8 * p;
      bad[1][j] = bad[0][j] + 65536;
    }
  }
  u32x4 aX[5], aY[5];
  u32x2 bXl[NB], bXh[NB], bYl[NB], bYh[NB];
  const unsigned m0w = smem_base + wid * 2048;
  const unsigned voffP = lane * 16;
  const int tu = wid < 4 ? G3B + wid * G3S : -1;
#define G3_READ(AR, BL, BH, SLOT, R)                                                                       \
  do {                                                                                                     \
    if ((R) < 2 * NB) {                                                                                    \
      constexpr int jb = ((R) < 2 * NB ? (R) : 0) >> 1;                                                    \
      constexpr int off = ((SLOT) & 1) * 32768 + (jb / FN) * 8192 + ((R) & 1) * 1024;                      \
      if ((R) & 1) dsr_tr<off>(BH[jb], bad[(SLOT) >> 1][jb % FN]);                                         \
      else dsr_tr<off>(BL[jb], bad[(SLOT) >> 1][jb % FN]);                                                 \
    } else if ((R) < 2 * NB + 4) {                                                                         \
      constexpr int ia = ((R) >= 2 * NB && (R) < 2 * NB + 4) ? (R) - 2 * NB : 0;                           \
      dsr128<((SLOT) & 1) * 32768 + ia * 1024>(AR[ia], aad[(SLOT) >> 1]);                                  \
    } else {                                                                                               \
      if ((SLOT) == 0) cad += 1280;                                      \
      dsr128<(SLOT) * 320>(AR[4], cad);                                                                    \
    }                                                                                                      \
  } while (0)
  auto rd = [&](auto SL, auto RR, u32x4 (&AR)[5], u32x2 (&BL)[NB], u32x2 (&BH)[NB]) { G3_READ(AR, BL, BH, SL.value, RR.value); };
#define G3_UNIT(AR, BL, BH, NAR, NBL, NBH, SLOT, U)                                                        \
  do {                                                                                                     \
    WAIT_LGKM0();                                                                                          \
    u32x4 bb[NB];                                                                                          \
    _Pragma("unroll") for (int j = 0; j < NB; ++j) bb[j] = u32x4{BL[j][0], BL[j][1], BH[j][0], BH[j][1]};  \
    SCHED0();                                                                                              \
    asm volatile("s_nop 1");                                                                               \
    static_for<NM>([&](auto N) {                                                                           \
      constexpr int n = N.value;                                                                           \
      if constexpr (n == NM / 2) {                                                                         \
        wait_vm<NBR ? 10 : 8>(); __builtin_amdgcn_s_barrier();                                             \
      }                                                                                                    \
      if constexpr (G3B && n == NM / 2 + TPOS) {                                                           \
        if ((U) == tu) touch8k(smem_base + 131072 + 4096, voffP * 8u, rsP, wid * 8192u);       \
      }                                                                                                    \
      if constexpr (!G3B && n == NM / 2 + TPOS) {                                                          \
                      \
        if (NBR) bload_lds<0>(smem_base + 131072 + 4096, voffP, rsP, (((U) & 7) == wid) ? (unsigned)(U) * 1024u : 0x40000000u); \
        else if (((U) & 7) == wid) bload_lds<0>(smem_base + 131072 + 4096, voffP, rsP, (unsigned)(U) * 1024u);  \
      }                                                                                                    \
      MFMA_AV(acc[n & 3][n >> 2], bb[n >> 2], AR[n & 3]);                                                  \
      if constexpr (n < NM / 2) {                                                                          \
        if constexpr (n % 3 == 1 && n / 3 < 4) G3_STAGE(((SLOT) + 3) & 3, n / 3, (U) + 3);                 \
      } else {                                                                                             \
        constexpr int r0 = (n - NM / 2) * 2;                                                               \
        if constexpr (r0 < RD) rd(ic<((SLOT) + 1) & 3>{}, ic<(r0 < RD ? r0 : 0)>{}, NAR, NBL, NBH);        \
        if constexpr (r0 + 1 < RD) rd(ic<((SLOT) + 1) & 3>{}, ic<(r0 + 1 < RD ? r0 + 1 : 0)>{}, NAR, NBL, NBH); \
      }                                                                                                    \
    });                                                                                                    \
    if (NBC || FN == 4 || wm < 3) {                                                                        \
      MFMA_AV(accC[0], bb[0], AR[4]);                                                                      \
      MFMA_AV(accC[1], bb[FN], AR[4]);                                                                     \
    }                                                                                                      \
    SCHED0();                                                                                              \
  } while (0)

  if (NBR) asm volatile("s_waitcnt vmcnt(10) lgkmcnt(0)" ::: "memory");
  else asm volatile("s_waitcnt vmcnt(8) lgkmcnt(0)" ::: "memory");
  __builtin_amdgcn_s_barrier();
  STAMPDECL;
  STAMPNW(5, 1);
  cad -= 1280;
  static_for<RD>([&](auto RR) { rd(ic<0>{}, RR, aX, bXl, bXh); });
  for (int u = 0; u < 32; u += 4) {
    G3_UNIT(aX, bXl, bXh, aY, bYl, bYh, 0, u);
    G3_UNIT(aY, bYl, bYh, aX, bXl, bXh, 1, u + 1);
    G3_UNIT(aX, bXl, bXh, aY, bYl, bYh, 2, u + 2);
    G3_UNIT(aY, bYl, bYh, aX, bXl, bXh, 3, u + 3);
  }
  asm volatile("s_waitcnt lgkmcnt(0)\n\ts_nop 15" ::: "memory");
#pragma unroll
  for (int i = 0; i < 5; ++i) asm volatile("" : "+v"(aX[i]));
#pragma unroll
  for (int j = 0; j < NB; ++j) asm volatile("" : "+v"(bXl[j]), "+v"(bXh[j]));
  SCHED0();
  STAMP(5, 2);
  STAMPFLUSH(5, 1);
  const f32x2* rstat = reinterpret_cast<const f32x2*>(smem + 131072);
  float* colc = reinterpret_cast<float*>(smem + 131072 + 2048);
#pragma unroll
  for (int m = 0; m < 2; ++m) {
    f32x4 v;
#pragma unroll
    for (int r = 0; r < 4; ++r) v[r] = accC[m][r] + __shfl_xor(accC[m][r], 1);
    if ((fr == 0 || fr == 2) && wm < FN) *reinterpret_cast<f32x4*>(colc + (m * 2 + (fr >> 1)) * 128 + (wn * 4 + rot) * 16 + fq * 4) = v;
  }
  asm volatile("s_waitcnt lgkmcnt(0)" ::: "memory");
  __builtin_amdgcn_s_barrier();
  asm volatile("" ::: "memory");
  f32x2 rs[4];
#pragma unroll
  for (int i = 0; i < 4; ++i) rs[i] = rstat[wm * 64 + i * 16 + fr];
#pragma unroll
  for (int j = 0; j < FN; ++j) {
    const int jg = (j + rot) % FN;
    const int cl = (wn * 4 + jg) * 16 + fq * 4;
    const f32x4 csg = *reinterpret_cast<const f32x4*>(colc + cl), bg = *reinterpret_cast<const f32x4*>(colc + 128 + cl);
    const f32x4 csu = *reinterpret_cast<const f32x4*>(colc + 256 + cl), bu = *reinterpret_cast<const f32x4*>(colc + 384 + cl);
    u32x2 pk[4];
#pragma unroll
    for (int i = 0; i < 4; ++i) {
#pragma unroll
      for (int hh = 0; hh < 2; ++hh) {
        const f32x2 ag = hh ? hi2(acc[i][j]) : lo2(acc[i][j]), au = hh ? hi2(acc[i][FN + j]) : lo2(acc[i][FN + j]);
        const f32x2 g = rs[i][0] * ag + (rs[i][1] * (hh ? hi2(csg) : lo2(csg)) + (hh ? hi2(bg) : lo2(bg)));
        const f32x2 uu = rs[i][0] * au + (rs[i][1] * (hh ? hi2(csu) : lo2(csu)) + (hh ? hi2(bu) : lo2(bu)));
        const f32x2 ea = g * -1.4426950408889634f;
        f32x2 e = {__builtin_amdgcn_exp2f(ea[0]), __builtin_amdgcn_exp2f(ea[1])};
        e = e + 1.0f;
        const f32x2 rc = {__builtin_amdgcn_rcpf(e[0]), __builtin_amdgcn_rcpf(e[1])};
        const f32x2 o = (g * uu) * rc;
        pk[i][hh] = pk_bf16(o[0], o[1]);
      }
    }
    const int J = bn * 7 + wn * 4 + jg;
    char* dst = (char*)act + ((size_t)((bm * 4 + wm) * (F_DIM / 32) + (J >> 1)) * 4 + (fq & 1)) * 1024 + (((J & 1) * 2 + (fq >> 1)) * 16 + fr) * 16;
#pragma unroll
    for (int i = 0; i < 4; i += 2) {
      const auto s0 = __builtin_amdgcn_permlane16_swap(pk[i][0], pk[i + 1][0], false, false);
      const auto s1 = __builtin_amdgcn_permlane16_swap(pk[i][1], pk[i + 1][1], false, false);
      gst128(dst + i * 1024, u32x4{s0[0], s1[0], s0[1], s1[1]});
    }
  }
}

__global__ __launch_bounds__(512, 2) void gemm3_kernel(const bf16* __restrict__ Y, const bf16* __restrict__ Wg,
                                                       const bf16* __restrict__ Wu, bf16* __restrict__ act,
                                                       const float* __restrict__ stat, const float* __restrict__ modp,
                                                       const bf16* __restrict__ Wd) {
  __shared__ __attribute__((aligned(16))) char smem[G3_CT + 33 * 320];
  STAMP(5, 0);
  const int tid = threadIdx.x, lane = tid & 63;
  const int wid = __builtin_amdgcn_readfirstlane(tid >> 6);
  const int wm = wid & 3, wn = wid >> 2;
  const int xcd = blockIdx.x & 7, idx = blockIdx.x >> 3;
  const int bm = idx & 7, bn = xcd * 4 + (idx >> 3);
  const int n0 = bn * 112, n0a = n0 & ~31;
  const unsigned smem_base = (unsigned)(uintptr_t)smem;
  const rsrc_t rsA = mk_rsrc((const char*)Y + (size_t)bm * 32 * 16384, 32 * 16384);
  const rsrc_t rsB = mk_rsrc(wn ? Wu : Wg, D_DIM * F_DIM * 2);
  const rsrc_t rsP = mk_rsrc((const char*)Wd + (size_t)blockIdx.x * 28672, 28672);
  const unsigned voffA = wid * 2048 + lane * 16;
  unsigned voffB[2];
#pragma unroll
  for (int i = 0; i < 2; ++i) {
    const int r = (wm * 2 + i) * 4 + (lane >> 4);
    const int sw = (r & 3) | (((r >> 3) & 1) << 2);
    const int c16 = lane & 15;
    voffB[i] = (unsigned)(r * (F_DIM * 2) + (n0a + (((c16 >> 1) ^ sw) * 16) + (c16 & 1) * 8) * 2);
  }
  const unsigned m0w = smem_base + wid * 2048;
  const unsigned voffB0 = voffB[0], voffB1 = voffB[1];
  if (wn == 0) {
    u32x4 tv[8];
    const rsrc_t rsS = mk_rsrc(stat, S_LEN * 128);
    const unsigned vo = (unsigned)((bm * 256 + tid) * 16);
    static_for<8>([&](auto I) { asm volatile("" : "=v"(tv[I.value])); bload_v<0>(tv[I.value], vo, rsS, I.value * 32768); });
    G3_STAGE(0, 0, 0); G3_STAGE(0, 1, 0); G3_STAGE(0, 2, 0); G3_STAGE(0, 3, 0);
    G3_STAGE(1, 0, 1); G3_STAGE(1, 1, 1); G3_STAGE(1, 2, 1); G3_STAGE(1, 3, 1);
    G3_DUMMY();
    G3_STAGE(2, 0, 2); G3_STAGE(2, 1, 2); G3_STAGE(2, 2, 2); G3_STAGE(2, 3, 2);
    G3_DUMMY();
    asm volatile("s_waitcnt vmcnt(" G3_TW ")" : "+v"(tv[0]), "+v"(tv[1]), "+v"(tv[2]), "+v"(tv[3]), "+v"(tv[4]), "+v"(tv[5]), "+v"(tv[6]), "+v"(tv[7])::"memory");
    float s = 0;
#pragma unroll
    for (int i = 0; i < 8; ++i) s += __uint_as_float(tv[i][0]) + __uint_as_float(tv[i][2]);
    const float mean = s * (1.0f / D_DIM);
    float m2 = 0;
#pragma unroll
    for (int i = 0; i < 8; ++i) {
      const float d0 = __uint_as_float(tv[i][0]) * (1.0f / 64.0f) - mean, d1 = __uint_as_float(tv[i][2]) * (1.0f / 64.0f) - mean;
      m2 += __uint_as_float(tv[i][1]) + __uint_as_float(tv[i][3]) + 64.0f * (d0 * d0 + d1 * d1);
    }
    const float rstd = rsqrtf(m2 * (1.0f / D_DIM) + 1e-5f);
    reinterpret_cast<f32x2*>(smem + 131072)[tid] = f32x2{rstd, -mean * rstd};
    g3_body<4>(smem, smem_base, wm, wn, wid, lane, bm, bn, rsA, rsB, rsP, voffA, voffB0, voffB1, act);
  } else {
    const rsrc_t rsC = mk_rsrc(modp, 4 * 6144 * 4);
    const int t = tid - 256;
    const unsigned vo = (unsigned)((3072 + t * 4) * 4);
    u32x4 tc[8];
#pragma unroll
    for (int i = 0; i < 8; ++i) { asm volatile("" : "=v"(tc[i])); bload_v<0>(tc[i], vo, rsC, (i >> 1) * 24576 + (i & 1) * 4096); }
    G3_STAGE(0, 0, 0); G3_STAGE(0, 1, 0); G3_STAGE(0, 2, 0); G3_STAGE(0, 3, 0);
    G3_STAGE(1, 0, 1); G3_STAGE(1, 1, 1); G3_STAGE(1, 2, 1); G3_STAGE(1, 3, 1);
    G3_DUMMY();
    G3_STAGE(2, 0, 2); G3_STAGE(2, 1, 2); G3_STAGE(2, 2, 2); G3_STAGE(2, 3, 2);
    G3_DUMMY();
    asm volatile("s_waitcnt vmcnt(" G3_TW ")" : "+v"(tc[0]), "+v"(tc[1]), "+v"(tc[2]), "+v"(tc[3]), "+v"(tc[4]), "+v"(tc[5]), "+v"(tc[6]), "+v"(tc[7])::"memory");
    const f32x4 sv = (__builtin_bit_cast(f32x4, tc[0]) + __builtin_bit_cast(f32x4, tc[2])) + (__builtin_bit_cast(f32x4, tc[4]) + __builtin_bit_cast(f32x4, tc[6]));
    const f32x4 gw = (__builtin_bit_cast(f32x4, tc[1]) + __builtin_bit_cast(f32x4, tc[3])) + (__builtin_bit_cast(f32x4, tc[5]) + __builtin_bit_cast(f32x4, tc[7])) + 1.0f;
    u32x2 gh, gl, sh, sl;
    split4(gw, gh, gl); split4(sv, sh, sl);
    char* d = smem + G3_CT + (t >> 3) * 320 + ((t >> 1) & 3) * 16 + (t & 1) * 8;
    *reinterpret_cast<u32x2*>(d) = gh; *reinterpret_cast<u32x2*>(d + 64) = gl;
    *reinterpret_cast<u32x2*>(d + 128) = sh; *reinterpret_cast<u32x2*>(d + 192) = sl;
    *reinterpret_cast<u32x2*>(d + 256) = u32x2{0, 0};
    g3_body<3>(smem, smem_base, wm, wn, wid, lane, bm, bn, rsA, rsB, rsP, voffA, voffB0, voffB1, act);
  }
  STAMP(5, 3);
  STAMP(5, 4);
}

#define XST 0
#define G4NF 11
#define HFS 3
#define HFL 4
#define HCK 5
#define XMAP 1
template <int OFF>
DI void bstore_sc1(u32x4 d, unsigned voff, rsrc_t rs, unsigned soff) {
  asm volatile("buffer_store_dwordx4 %0, %1, %2, %3 offen offset:%4 sc1\n\ts_nop 1" ::"v"(d), "v"(voff), "s"(rs), "s"(soff), "i"(OFF) : "memory");
}
namespace g4 {
constexpr int NF = G4NF, NH = 28 - 2 * NF, NS = NF + NH, HEP = NH - 3;
constexpr int nslot(int g) { return g < NF ? 64 : 32; }
constexpr int wslot(int g) { return g < NF - 1 ? 48 : (g == NF - 1 ? 56 : 24); }
constexpr int ring(int g, int n) {
  if (g < NF - 1) {
    if (n >= 48 || n % 3 != 1) return -1;
    const int l = n / 3;
    if (g + 2 < NF) return (g + 2) * 16 + l;
    return l < 12 ? (g + 2) * 16 + (l < 8 ? l : l + 4) : -1;
  }
  if (g == NF - 1) { if (n >= 48 || n % 4 != 1) return -1; const int l = n / 4; return (g + 2) * 16 + (l < 8 ? l : l + 4); }
  if (g == NF) { if (n >= 24) return -1; const int l = n % 12; return (g + 2 + n / 12) * 16 + (l < 8 ? l : l + 4); }
  if (g + 3 >= NS || n >= 24 || (n & 1)) return -1;
  const int l = n >> 1;
  return (g + 3) * 16 + (l < 8 ? l : l + 4);
}
constexpr int pwrite(int g, int n) {
  const int m = g == NF - 1 ? n : 64 + n;
  for (int e = 0; e < 24; ++e) if (12 + (13 * e) / 4 == m) return e;
  return -1;
}
constexpr int red0(int n) { return n < 8 ? 0 : (64 * (n - 8)) / 24; }
#define SPS 4
constexpr int sslot(int f) {
  const int ready = (f >> 2) * 32 + 8 + (((f & 3) + 1) * 16 * 24 + 63) / 64;
  const int paced = (f / SPS) * 32 + 15 + (f % SPS) * (32 / SPS);
  int m = ready > paced ? ready : paced;
  if ((m & 1) == 0) ++m;
  return m;
}
constexpr int dstore_f(int g, int n) {
  for (int f = 0; f < 8; ++f) if (sslot(f) == (g - NF) * 32 + n) return f;
  return -1;
}
constexpr bool dstore(int g, int n) { return g >= NF && dstore_f(g, n) >= 0; }
constexpr int vmops(int g, int n) {
  int c = (ring(g, n) >= 0 ? 1 : 0) + (dstore(g, n) ? 1 : 0);
  const int h = g - NF;
  if (h == HFS && n == 24) c += 1;
  if (h == HFL && n == 25) c += 1;
  if (h == HCK && n >= 24) c += 1;
  if (h == HEP && (n == 1 || n == 3)) c += 4;
  return c;
}
constexpr int between(int g0, int n0, int g1, int n1) {
  int c = 0;
  for (int g = g0; g <= g1; ++g)
    for (int n = (g == g0 ? n0 + 1 : 0); n < (g == g1 ? n1 : nslot(g)); ++n) c += vmops(g, n);
  return c;
}
constexpr int imin(int a, int b) { return a < b ? a : b; }
constexpr int waitcnt(int g) {
  int c = 63;
  if (g + 1 < NS && g + 1 >= 2) {
    for (int s = 0; s <= g; ++s)
      for (int n = 0; n < nslot(s); ++n)
        if (ring(s, n) == (g + 1) * 16 + 15) c = imin(c, between(s, n, g, wslot(g)) + (dstore(s, n) ? 1 : 0));
  }
  if (g == 0) c = 16;
  if (g - NF == HFS) c = imin(c, between(NF + sslot(7) / 32, sslot(7) % 32, g, 24));
  if (g - NF == HCK) c = imin(c, between(NF + HFL, 25, g, 24));
  return c;
}
constexpr int bslot(int b) { return b < NF + 3 ? b % 3 : ((b - NF) % 4 == 3 ? 3 : (NF + (b - NF) % 4) % 3); }
}
__global__ __launch_bounds__(256, 1) void gemm4_kernel(const bf16* __restrict__ At, const bf16* __restrict__ W,
                                                       const bf16* __restrict__ Yt, const float* __restrict__ modp,
                                                       bf16* __restrict__ C, float* xbuf, unsigned* flags) {
  using namespace g4;
  constexpr int KB = F_DIM / 32;
  constexpr int GT = 96 * 1024;
  __shared__ __attribute__((aligned(16))) char smem[GT + 1024];
  STAMPDECL;
  STAMP(6, 0);
  const int tid = threadIdx.x, lane = tid & 63;
  const int w = __builtin_amdgcn_readfirstlane(tid >> 6);
  const int xcd = blockIdx.x & 7, idx = blockIdx.x >> 3;
  const int kh = xcd & 1, bm = ((xcd >> 1) & 1) * 8 + (idx & 7), bn = (xcd >> 2) * 4 + (idx >> 3);
  const unsigned partner = blockIdx.x ^ 1;
  const int n0 = bn * 128, bm64 = bm * 2 + kh;
  const int fr = lane & 15, fq = lane >> 4;
  const rsrc_t rsA0 = mk_rsrc((const char*)At + (size_t)(bm64 ^ 1) * KB * 4096, KB * 4096);
  const rsrc_t rsA1 = mk_rsrc((const char*)At + (size_t)bm64 * KB * 4096, KB * 4096);
  const rsrc_t rsB = mk_rsrc((const char*)W + n0 * 2, KB * 65536 - n0 * 2);
  const unsigned voffL = lane * 16;
  unsigned voffA[4];
#pragma unroll
  for (int i = 0; i < 4; ++i) voffA[i] = voffL + ((i + w) & 3) * 1024;
  const unsigned voffB = fq * 8 * 2048 + fr * 16;
  u32x4 braw[4][8], afr[3][8], afa[4][4];
  u32x4 T[2][2];
  u32x4 xv[4], pv[8];
  unsigned gv[4], flg;
  f32x4 acc[8][8];
#pragma unroll
  for (int r = 0; r < 3; ++r)
#pragma unroll
    for (int j = 0; j < 8; ++j) asm volatile("" : "=v"(braw[r][j]), "=v"(afr[r][j]));
  const int kbF = (kh ? KB - 4 * NF : 0) + w, kbH = w;
#define ELOAD(B, L)                                                                                \
  do {                                                                                             \
    const unsigned kbv = (unsigned)(((B) < NF ? kbF : kbH) + (B) * 4);                             \
    if constexpr ((L) < 8) {                                                                       \
      const unsigned so = kbv * 65536u + ((L) >> 1) * 4096u;                                       \
      if constexpr (bslot(B) == 3 || (B) >= NF + 3) asm volatile("" : "=v"(braw[bslot(B)][(L) & 7]));        \
      if constexpr ((L) & 1) bload_v<2048>(braw[bslot(B)][(L) & 7], voffB, rsB, so);               \
      else bload_v<0>(braw[bslot(B)][(L) & 7], voffB, rsB, so);                                    \
    } else if constexpr ((B) >= NF + 2) {                                                          \
      asm volatile("" : "=a"(afa[((B) - NF) & 3][(L) & 3]));                                       \
      bload_a<0>(afa[((B) - NF) & 3][(L) & 3], voffA[(L) & 3], rsA1, kbv * 4096u);                 \
    } else {                                                                                       \
      bload_v<0>(afr[(B) % 3][(L) & 7], voffA[(L) & 3], ((L) & 4) ? rsA1 : rsA0, kbv * 4096u);      \
    }                                                                                              \
  } while (0)
#define DPERM(TB, Q, SL, PP)                                                                       \
  do {                                                                                             \
    if ((Q) & 1) PERM_HI(T[TB][1][(Q) >> 1], braw[SL][((Q) >> 1) * 2][PP], braw[SL][((Q) >> 1) * 2 + 1][PP]); \
    else PERM_LO(T[TB][0][(Q) >> 1], braw[SL][((Q) >> 1) * 2][PP], braw[SL][((Q) >> 1) * 2 + 1][PP]);         \
  } while (0)

#define MFMA_E(C, B, A) asm volatile("v_mfma_f32_16x16x32_bf16 %0, %1, %2, %0" : "+a"(C) : "v"(B), "v"(A))
  static_for<16>([&](auto L) { ELOAD(0, L.value); });
  static_for<16>([&](auto L) { ELOAD(1, L.value); });
#pragma unroll
  for (int i = 0; i < 8; ++i)
#pragma unroll
    for (int f = 0; f < 8; ++f) { acc[i][f] = f32x4{0, 0, 0, 0}; asm volatile("" : "+a"(acc[i][f])); }
  const unsigned sbase = (unsigned)(uintptr_t)smem + voffL;
  unsigned wlo[3];
#pragma unroll
  for (int i = 1; i < 4; ++i) wlo[i - 1] = sbase + (((w + i) & 3) * 3 + (i - 1)) * 8192;
  const unsigned rlo = sbase + w * 24576;
  const rsrc_t rsEx = mk_rsrc(xbuf, 256 * 32768);
  const unsigned exo = (unsigned)(blockIdx.x * 32768 + w * 8192);
  const rsrc_t rsFo = mk_rsrc(flags + blockIdx.x * 4 + w, 4), rsFi = mk_rsrc(flags + partner * 4 + w, 4);
  wait_vm<16>();
  STAMPNW(6, 1);
#pragma unroll
  for (int q = 0; q < 8; ++q) DPERM(0, q, 0, 0);
  asm volatile("s_nop 1");
  u32x4 pr[3][4];
  f32x4 tt[8], uu;
  static_for<NS>([&](auto G) {
    constexpr int g = G.value, h = g - NF, sl = bslot(g), ns = bslot(g + 1);
    constexpr bool full = g < NF - 1, lastf = g == NF - 1;
    constexpr bool red = (h == 0 || h == 1);
    constexpr int fb = h == 1 ? 4 : 0;
    if constexpr (h == 0 || h == 1) { WAIT_LGKM0(); __builtin_amdgcn_s_barrier(); }
    if constexpr (h == 3) __builtin_amdgcn_s_barrier();
    static_for<nslot(g)>([&](auto N) {
      constexpr int n = N.value;
      if constexpr (n == wslot(g)) {
        if constexpr (waitcnt(g) < 63) {
          if constexpr (h == HCK) asm volatile("s_waitcnt vmcnt(%1)" : "+v"(flg) : "n"(waitcnt(g)) : "memory");
          else wait_vm<waitcnt(g)>();
        }
        if constexpr (h == HFS) asm volatile("buffer_store_dword %0, %1, %2, 0 offen sc1" ::"v"(1u), "v"(voffL >> 2), "s"(rsFo) : "memory");
        if constexpr (h == HCK) {
          unsigned spins = 0;
          while (__builtin_amdgcn_readfirstlane(flg) == 0u && spins < (1u << 20)) {
            __builtin_amdgcn_s_sleep(1);
            bload1_sc1(flg, 0u, rsFi);
            asm volatile("s_waitcnt vmcnt(0)" : "+v"(flg)::"memory");
            ++spins;
          }
        }
      }
      if constexpr (red && n == 8) WAIT_LGKM0();
      if constexpr (full) {
        constexpr int p = n >> 4, q = n & 15;
        MFMA_E(acc[q & 7][2 * p + (q >> 3)], T[p & 1][q >> 3], afr[g % 3][q & 7]);
        if constexpr ((q & 1) == 0) {
          if constexpr (p < 3) DPERM((p + 1) & 1, q >> 1, sl, p + 1);
          else DPERM(0, q >> 1, ns, 0);
        }
      } else {
        constexpr int p = (n >> 3) & 3, q = n & 7, ib = (lastf && n < 32) ? 0 : 4;
        if constexpr (h >= 2) MFMA_A(acc[ib + (q & 3)][2 * p + (q >> 2)], T[p & 1][q >> 2], afa[h & 3][q & 3]);
        else MFMA_E(acc[ib + (q & 3)][2 * p + (q >> 2)], T[p & 1][q >> 2], afr[g % 3][ib + (q & 3)]);
        if constexpr (p < 3) DPERM((p + 1) & 1, q, sl, p + 1);
        else if constexpr (ib == 0) DPERM(0, q, sl, 0);
        else if constexpr (g + 1 < NS) DPERM(0, q, ns, 0);
      }
      if constexpr (ring(g, n) >= 0) ELOAD((ring(g, n) >> 4), (ring(g, n) & 15));
      if constexpr ((lastf || h == 0) && pwrite(g, n) >= 0) {
        constexpr int e = pwrite(g, n), i = 1 + e % 3, f = e / 3;
        dsw128a<f * 1024>(wlo[i - 1], acc[i][f]);
      }
      if constexpr (red) {
        if constexpr (n < 6) dsr128<(n % 3) * 8192 + (fb + n / 3) * 1024>(pr[n % 3][n / 3], rlo);
        if constexpr (n == 6 || n == 7) {
          static_for<3>([&](auto E) { dsr128<E.value * 8192 + (fb + n - 4) * 1024>(pr[E.value][n - 4], rlo); });
        }
        if constexpr (n >= 8) {
          constexpr int o0 = red0(n), o1 = red0(n + 1);
          static_for<o1 - o0>([&](auto O) {
            constexpr int o = o0 + O.value, fl = o / 16, k = o % 16, r = k & 3;
            (void)&uu; (void)&tt; (void)&pr;
            if constexpr (k < 4) asm volatile("v_accvgpr_read_b32 %0, %1" : "=v"(tt[fb + fl][r]) : "a"(acc[0][fb + fl][r]));
            else if constexpr (k < 8) asm volatile("v_add_f32 %0, %0, %1" : "+v"(tt[fb + fl][r]) : "v"(pr[0][fl][r]));
            else if constexpr (k < 12) asm volatile("v_add_f32 %0, %1, %2" : "=v"(uu[r]) : "v"(pr[1][fl][r]), "v"(pr[2][fl][r]));
            else asm volatile("v_add_f32 %0, %0, %1" : "+v"(tt[fb + fl][r]) : "v"(uu[r]));
          });
        }
      }
      if constexpr (h >= 0 && dstore(g, n)) {
        constexpr int f = dstore_f(g, n);
        bstore_sc1<(f & 3) * 1024>(__builtin_bit_cast(u32x4, tt[f]), voffL, rsEx, exo + (f >> 2) * 4096);
      }
      if constexpr (h == HFL && n == 25) {
        asm volatile("" : "=v"(flg));
        bload1_sc1(flg, 0u, rsFi);
      }
      if constexpr (h == HCK && n >= 24) {
        constexpr int f = n - 24;
        asm volatile("" : "=v"(pv[f]));
        bload_sc1<(f & 3) * 1024>(pv[f], voffL, rsEx, partner * 32768u + w * 8192u + (f >> 2) * 4096u);
      }
      if constexpr (h == HEP && n == 1) {
        const rsrc_t rsG = mk_rsrc(modp + (w >= 2 ? 4096 : 5120), 4 * 6144 * 4);
        const unsigned vo = (unsigned)((n0 + (tid & 127)) * 4);
#pragma unroll
        for (int r = 0; r < 4; ++r) { asm volatile("" : "=v"(gv[r])); bload1_v<0>(gv[r], vo, rsG, r * 24576); }
      }
      if constexpr (h == HEP && n == 3) {
        const rsrc_t rsX = mk_rsrc(Yt, S_LEN * D_DIM * 2);
        const unsigned vx = (unsigned)(((bm64 >> 2) * 32 + bn * 4 + fq) * 16384 + ((bm64 & 3) * 4 + w) * 1024 + fr * 16);
#pragma unroll
        for (int r = 0; r < 4; ++r) asm volatile("" : "=v"(xv[r]));
        bload_v<0>(xv[0], vx, rsX, 0); bload_v<256>(xv[1], vx, rsX, 0); bload_v<512>(xv[2], vx, rsX, 0); bload_v<768>(xv[3], vx, rsX, 0);
      }
      if constexpr (g == NS - 1 && n >= 8 && (n & 7) >= 2) {
        constexpr int e = (n & 7) - 2, i = 1 + e % 3, f = 2 * ((n >> 3) - 1) + e / 3;
        dsw128a<f * 1024>(wlo[i - 1], acc[4 + i][f]);
      }
    });
  });
  asm volatile("s_nop 15" ::: "memory");
#pragma unroll
  for (int f = 0; f < 8; ++f) asm volatile("" : "+a"(acc[4][f]));
  static_for<6>([&](auto E) { constexpr int i = 1 + E.value % 3, f = 6 + E.value / 3; dsw128a<f * 1024>(wlo[i - 1], acc[4 + i][f]); });
  asm volatile("s_waitcnt vmcnt(%8)" : "+v"(xv[0]), "+v"(xv[1]), "+v"(xv[2]), "+v"(xv[3]), "+v"(gv[0]), "+v"(gv[1]), "+v"(gv[2]), "+v"(gv[3]) : "n"(between(NF + HEP, 3, NS - 1, 32)) : "memory");
  STAMP(6, 2); STAMPFLUSH(6, 1);
  float* gtab = reinterpret_cast<float*>(smem + GT);
  {
    float sum = (__uint_as_float(gv[0]) + __uint_as_float(gv[1])) + (__uint_as_float(gv[2]) + __uint_as_float(gv[3]));
    if (w >= 2) {
      sum += 1.0f;
      if (__builtin_fabsf(sum) < 8.67361738e-19f) sum = 8.67361738e-19f;
      sum = 1.0f / sum;
    }
    const int col = tid & 127;
    gtab[(tid & 128) + (col & 96) + (col & 7) * 4 + ((col >> 3) & 3)] = sum;
  }
  asm volatile("s_waitcnt lgkmcnt(0)" ::: "memory");
  __builtin_amdgcn_s_barrier();
  u32x4 pq[3][8], gt[8], ct[8];
  f32x4 fin[8];
  {
    const unsigned gsrc = (unsigned)(uintptr_t)smem + GT + fq * 128;
    static_for<24>([&](auto E) { dsr128<E.value * 1024>(pq[E.value >> 3][E.value & 7], rlo); });
    static_for<8>([&](auto F) { dsr128<F.value * 16>(gt[F.value], gsrc); });
    static_for<8>([&](auto F) { dsr128<512 + F.value * 16>(ct[F.value], gsrc); });
#pragma unroll
    for (int f = 0; f < 8; ++f) { fin[f] = acc[4][f]; asm volatile("" : "+v"(fin[f])); }
    asm volatile("s_waitcnt lgkmcnt(0)" ::: "memory");
#pragma unroll
    for (int f = 0; f < 8; ++f) asm volatile("" : "+v"(pq[0][f]), "+v"(pq[1][f]), "+v"(pq[2][f]), "+v"(gt[f]), "+v"(ct[f]));
  }
#pragma unroll
  for (int f = 0; f < 8; ++f) {
    fin[f] = (fin[f] + __builtin_bit_cast(f32x4, pq[0][f])) + (__builtin_bit_cast(f32x4, pq[1][f]) + __builtin_bit_cast(f32x4, pq[2][f]));
    asm volatile("" : "+v"(fin[f]));
  }
  asm volatile("s_waitcnt vmcnt(0)" : "+v"(pv[0]), "+v"(pv[1]), "+v"(pv[2]), "+v"(pv[3]), "+v"(pv[4]), "+v"(pv[5]), "+v"(pv[6]), "+v"(pv[7])::"memory");
  STAMP(6, 6);
#pragma unroll
  for (int f = 0; f < 8; ++f) {
    fin[f] += __builtin_bit_cast(f32x4, pv[f]);
    f32x4 xf;
#pragma unroll
    for (int r = 0; r < 4; ++r) xf[r] = (f & 1) ? bf_hi(xv[r][f >> 1]) : bf_lo(xv[r][f >> 1]);
    xf *= __builtin_bit_cast(f32x4, ct[f]);
    fin[f] = xf + __builtin_bit_cast(f32x4, gt[f]) * fin[f];
  }
  {
    char* tb = smem + w * 24576;
#pragma unroll
    for (int r = 0; r < 4; ++r) {
      const u32x4 ov = {pk_bf16(fin[0][r], fin[1][r]), pk_bf16(fin[2][r], fin[3][r]), pk_bf16(fin[4][r], fin[5][r]), pk_bf16(fin[6][r], fin[7][r])};
      *reinterpret_cast<u32x4*>(tb + fr * 256 + (((fq * 4 + r) ^ (fr & 7)) * 16)) = ov;
    }
#pragma unroll
    for (int j = 0; j < 4; ++j) {
      const int row = j * 4 + fq;
      const u32x4 ov = *reinterpret_cast<const u32x4*>(tb + row * 256 + ((fr ^ (row & 7)) * 16));
      gst128(C + (size_t)(bm64 * 64 + w * 16 + row) * D_DIM + n0 + fr * 8, ov);
    }
  }
  STAMP(6, 3);
  STAMP(6, 4);
}

extern "C" void kernel_launch(void* const* d_in, const int* in_sizes, int n_in, void* d_out, int out_size,
                              void* d_ws, size_t ws_size, hipStream_t stream) {
  const bf16* x = (const bf16*)d_in[0];
  const bf16* wqkv = (const bf16*)d_in[1];
  const bf16* wo = (const bf16*)d_in[2];
  const bf16* wg = (const bf16*)d_in[4];
  const bf16* wu = (const bf16*)d_in[5];
  const bf16* wd = (const bf16*)d_in[6];
  const bf16* c = (const bf16*)d_in[7];
  const bf16* wmod = (const bf16*)d_in[8];
  bf16* out = (bf16*)d_out;

  char* ws = (char*)d_ws;
  float* modp = (float*)ws;
  float* rstat1 = (float*)(ws + 114688);
  float* stat = (float*)(ws + 524288);
  bf16* ot = (bf16*)(ws + (1 << 20));
  bf16* yt = (bf16*)(ws + (9 << 20));
  bf16* ht = (bf16*)(ws + (13 << 20));
  bf16* act = (bf16*)(ws + (17 << 20));
  char* wc = ws + (5 << 20);
  float* xbuf = (float*)(ws + (32 << 20));
  unsigned* flags = (unsigned*)(ws + 262144);

  pre_kernel<<<320, 512, 0, stream>>>(x, c, wmod, modp, rstat1);
  ln_apply_kernel<<<256, 256, 0, stream>>>(x, modp, rstat1, ht, wqkv, wo, flags, wc);
  qkv_attn_kernel<<<256, 512, 0, stream>>>(ht, wqkv, ot, wg, wu, wo, x, modp, yt, stat, flags + 1024, wc);
  gemm3_kernel<<<256, 512, 0, stream>>>(yt, wg, wu, act, stat, modp, wd);
  gemm4_kernel<<<256, 256, 0, stream>>>(act, wd, yt, modp, out, xbuf, flags);
}
```

```cpp
#include <hip/hip_runtime.h>
#include <hip/hip_bf16.h>
#include <stdint.h>

typedef __hip_bfloat16 bf16;
typedef __attribute__((ext_vector_type(8))) short bf16x8;
typedef __attribute__((ext_vector_type(4))) float f32x4;
typedef __attribute__((ext_vector_type(2))) float f32x2;
typedef __attribute__((ext_vector_type(4))) unsigned u32x4;
typedef __attribute__((ext_vector_type(2))) unsigned u32x2;
typedef __attribute__((ext_vector_type(2))) __bf16 bf16x2_t;
typedef __amdgpu_buffer_rsrc_t rsrc_t;

#define DI __device__ __forceinline__
#define QBAR 2
#define QRN 17
#define TQ 28
#define DWO 1
#define DW0 4
#define DWS 6
#define OFS 4
#define ORP 1
#define YFS 32
#define YSL 6
#define CODEPF 53

#define STAMP(k, s) do { } while (0)
#define STAMPT(k, s, T) do { } while (0)
#define STAMPNW(k, s)
#define STAMPFLUSH(k, s)
#define STAMPDECL

constexpr int S_LEN = 2048;
constexpr int D_DIM = 1024;
constexpr int F_DIM = 3584;

template <int V> struct ic { static constexpr int value = V; };
template <int N, int I = 0, class F>
DI void static_for(F&& f) {
  if constexpr (I < N) { f(ic<I>{}); static_for<N, I + 1>(f); }
}

DI float bf_lo(unsigned u) { return __uint_as_float(u << 16); }
DI float bf_hi(unsigned u) { return __uint_as_float(u & 0xffff0000u); }
DI unsigned pk_bf16(float lo, float hi) {
  const f32x2 v = {lo, hi};
  return __builtin_bit_cast(unsigned, __builtin_convertvector(v, bf16x2_t));
}
DI rsrc_t mk_rsrc(const void* p, unsigned bytes) { return __builtin_amdgcn_make_buffer_rsrc((void*)p, (short)0, (int)bytes, 0x00020000); }

template <int N>
DI void wait_vm() { asm volatile("s_waitcnt vmcnt(%0)" ::"n"(N > 63 ? 63 : N) : "memory"); }
#define WAIT_LGKM0() do { asm volatile("s_waitcnt lgkmcnt(0)" ::: "memory"); __builtin_amdgcn_sched_barrier(0); } while (0)
#define SCHED0() __builtin_amdgcn_sched_barrier(0)

#define MFMA_A(C, B, A) asm volatile("v_mfma_f32_16x16x32_bf16 %0, %1, %2, %0" : "+a"(C) : "v"(B), "a"(A))
#define MFMA_AV(C, B, A) asm volatile("v_mfma_f32_16x16x32_bf16 %0, %1, %2, %0" : "+v"(C) : "v"(B), "v"(A))
#define PERM_LO(D, X, Y) asm volatile("v_perm_b32 %0, %1, %2, %3" : "=v"(D) : "v"(Y), "v"(X), "s"(0x05040100u))
#define PERM_HI(D, X, Y) asm volatile("v_perm_b32 %0, %1, %2, %3" : "=v"(D) : "v"(Y), "v"(X), "s"(0x07060302u))
template <int OFF>
DI void bload_v(u32x4& d, unsigned voff, rsrc_t rs, unsigned soff) {
  asm volatile("buffer_load_dwordx4 %0, %1, %2, %3 offen offset:%4" : "+v"(d) : "v"(voff), "s"(rs), "s"(soff), "i"(OFF) : "memory");
}
template <int OFF>
DI void bload_a(u32x4& d, unsigned voff, rsrc_t rs, unsigned soff) {
  asm volatile("buffer_load_dwordx4 %0, %1, %2, %3 offen offset:%4" : "+a"(d) : "v"(voff), "s"(rs), "s"(soff), "i"(OFF) : "memory");
}
template <int OFF>
DI void bload2_v(u32x2& d, unsigned voff, rsrc_t rs, unsigned soff) {
  asm volatile("buffer_load_dwordx2 %0, %1, %2, %3 offen offset:%4" : "+v"(d) : "v"(voff), "s"(rs), "s"(soff), "i"(OFF) : "memory");
}
template <int OFF>
DI void bload1_v(unsigned& d, unsigned voff, rsrc_t rs, unsigned soff) {
  asm volatile("buffer_load_dword %0, %1, %2, %3 offen offset:%4" : "+v"(d) : "v"(voff), "s"(rs), "s"(soff), "i"(OFF) : "memory");
}
template <int OFF>
DI void bload_lds(unsigned m0v, unsigned voff, rsrc_t rs, unsigned soff) {
  asm volatile("s_mov_b32 m0, %0\n\ts_nop 0\n\tbuffer_load_dwordx4 %1, %2, %3 offen offset:%4 lds" ::"s"(m0v), "v"(voff), "s"(rs), "s"(soff), "i"(OFF) : "memory");
}
DI void touch8k(unsigned m0v, unsigned voff, rsrc_t rs, unsigned soff) {
  asm volatile("s_mov_b32 m0, %0\n\ts_nop 0\n\tbuffer_load_dword %1, %2, %3 offen lds" ::"s"(m0v), "v"(voff), "s"(rs), "s"(soff) : "memory");
}
template <int OFF>
DI void bload_lds_sc1(unsigned m0v, unsigned voff, rsrc_t rs, unsigned soff) {
  asm volatile("s_mov_b32 m0, %0\n\ts_nop 0\n\tbuffer_load_dwordx4 %1, %2, %3 offen offset:%4 sc1 lds" ::"s"(m0v), "v"(voff), "s"(rs), "s"(soff), "i"(OFF) : "memory");
}
template <int OFF>
DI void dsr128(u32x4& d, unsigned addr) { asm volatile("ds_read_b128 %0, %1 offset:%2" : "=v"(d) : "v"(addr), "i"(OFF) : "memory"); }
template <int OFF>
DI void dsr64(u32x2& d, unsigned addr) { asm volatile("ds_read_b64 %0, %1 offset:%2" : "=v"(d) : "v"(addr), "i"(OFF) : "memory"); }
template <int OFF>
DI void dsr_tr(u32x2& d, unsigned addr) { asm volatile("ds_read_b64_tr_b16 %0, %1 offset:%2" : "=v"(d) : "v"(addr), "i"(OFF) : "memory"); }
template <int OFF>
DI void dsw128(unsigned addr, u32x4 d) { asm volatile("ds_write_b128 %0, %1 offset:%2" ::"v"(addr), "v"(d), "i"(OFF) : "memory"); }
template <int OFF>
DI void dsw128a(unsigned addr, const f32x4& d) { asm volatile("ds_write_b128 %0, %1 offset:%2" ::"v"(addr), "a"(d), "i"(OFF) : "memory"); }
template <int OFF>
DI void dsw32(unsigned addr, unsigned d) { asm volatile("ds_write_b32 %0, %1 offset:%2" ::"v"(addr), "v"(d), "i"(OFF) : "memory"); }

DI void gst128(void* p, u32x4 v) {
  asm volatile("global_store_dwordx4 %0, %1, off sc1\n\ts_nop 1" ::"v"(p), "v"(v) : "memory");
}
DI f32x2 lo2(f32x4 v) { return __builtin_shufflevector(v, v, 0, 1); }
DI f32x2 hi2(f32x4 v) { return __builtin_shufflevector(v, v, 2, 3); }
DI float rowsum4(float v) {
  const unsigned u = __float_as_uint(v);
  const auto a = __builtin_amdgcn_permlane16_swap(u, u, false, false);
  const unsigned t = __float_as_uint(__uint_as_float(a[0]) + __uint_as_float(a[1]));
  const auto b = __builtin_amdgcn_permlane32_swap(t, t, false, false);
  return __uint_as_float(b[0]) + __uint_as_float(b[1]);
}
DI void split4(const f32x4 v, u32x2& hi, u32x2& lo) {
  hi[0] = pk_bf16(v[0], v[1]); hi[1] = pk_bf16(v[2], v[3]);
  lo[0] = pk_bf16(v[0] - bf_lo(hi[0]), v[1] - bf_hi(hi[0]));
  lo[1] = pk_bf16(v[2] - bf_lo(hi[1]), v[3] - bf_hi(hi[1]));
}
DI int slow_col(const void* p) { return (int)((3u - ((unsigned)(uintptr_t)p >> 7)) & 7u); }
DI float silu_f(float v) { return v / (1.0f + __expf(-v)); }

__global__ __launch_bounds__(512) void pre_kernel(const bf16* __restrict__ x, const bf16* __restrict__ c,
                                                  const bf16* __restrict__ wmod, float* __restrict__ modp,
                                                  float* __restrict__ rstat1, unsigned* __restrict__ flags) {
  __shared__ __attribute__((aligned(16))) float red[8 * 2 * 128 + 256];
  STAMP(0, 0);
  const int tid = threadIdx.x, lane = tid & 63, w = tid >> 6;
  if (blockIdx.x < 192) {
    const int fr = lane & 15, fq = lane >> 4;
    const int cg = blockIdx.x >> 2, kq = blockIdx.x & 3;
    const int col0 = cg * 128;
    const int k0 = kq * 256 + w * 32 + fq * 8;
    u32x4 raw[8];
#pragma unroll
    for (int j = 0; j < 8; ++j) {
      const u32x4* p_ = reinterpret_cast<const u32x4*>(wmod + (size_t)(k0 + j) * 6144 + col0 + fr * 8);
      raw[j] = __builtin_nontemporal_load(p_);
    }
    const u32x4 cv = *reinterpret_cast<const u32x4*>(c + k0);
    u32x4 cf;
#pragma unroll
    for (int e = 0; e < 4; ++e) {
      const float a = silu_f(bf_lo(cv[e])), b = silu_f(bf_hi(cv[e]));
      const unsigned hi = pk_bf16(a, b);
      const unsigned lo = pk_bf16(a - bf_lo(hi), b - bf_hi(hi));
      cf[e] = (fr == 0) ? hi : ((fr == 1) ? lo : 0u);
    }
    f32x4 acc[8];
#pragma unroll
    for (int p = 0; p < 4; ++p) {
      u32x4 lo, hi;
#pragma unroll
      for (int t = 0; t < 4; ++t) {
        lo[t] = __builtin_amdgcn_perm(raw[2 * t + 1][p], raw[2 * t][p], 0x05040100u);
        hi[t] = __builtin_amdgcn_perm(raw[2 * t + 1][p], raw[2 * t][p], 0x07060302u);
      }
      acc[2 * p] = __builtin_amdgcn_mfma_f32_16x16x32_bf16(__builtin_bit_cast(bf16x8, lo), __builtin_bit_cast(bf16x8, cf), f32x4{0, 0, 0, 0}, 0, 0, 0);
      acc[2 * p + 1] = __builtin_amdgcn_mfma_f32_16x16x32_bf16(__builtin_bit_cast(bf16x8, hi), __builtin_bit_cast(bf16x8, cf), f32x4{0, 0, 0, 0}, 0, 0, 0);
    }
    if (fr < 2) {
#pragma unroll
      for (int r = 0; r < 4; ++r) {
        float* d = red + (w * 2 + fr) * 128 + fq * 32 + r * 8;
        *reinterpret_cast<f32x4*>(d) = f32x4{acc[0][r], acc[1][r], acc[2][r], acc[3][r]};
        *reinterpret_cast<f32x4*>(d + 4) = f32x4{acc[4][r], acc[5][r], acc[6][r], acc[7][r]};
      }
    }
    __syncthreads();
    if (tid < 128) {
      float s = 0;
#pragma unroll
      for (int g = 0; g < 16; ++g) s += red[g * 128 + tid];
      modp[kq * 6144 + col0 + tid] = s;
    }
    STAMP(0, 1);
    return;
  }
  const int row0 = (blockIdx.x - 192) * 16 + w * 2;
  u32x4 a[2][2];
#pragma unroll
  for (int i = 0; i < 2; ++i) {
    a[i][0] = *reinterpret_cast<const u32x4*>(x + (size_t)(row0 + i) * D_DIM + lane * 8);
    a[i][1] = *reinterpret_cast<const u32x4*>(x + (size_t)(row0 + i) * D_DIM + 512 + lane * 8);
  }
  if (blockIdx.x < 192 + 8) {
    unsigned long long pc;
    asm volatile("s_getpc_b64 %0" : "=s"(pc));
    const rsrc_t rsC = mk_rsrc((const void*)(uintptr_t)(pc & ~255ull), CODEPF * 1024);
#pragma unroll
    for (int i = 0; i < (CODEPF + 7) / 8; ++i) bload_lds<0>((unsigned)(uintptr_t)(red + 2048), tid * 16, rsC, i * 8192);
  }
#pragma unroll
  for (int i = 0; i < 2; ++i) {
    float v[16];
#pragma unroll
    for (int j = 0; j < 8; ++j) { v[2 * j] = bf_lo(a[i][j >> 2][j & 3]); v[2 * j + 1] = bf_hi(a[i][j >> 2][j & 3]); }
    float s = 0;
#pragma unroll
    for (int j = 0; j < 16; ++j) s += v[j];
#pragma unroll
    for (int o = 32; o > 0; o >>= 1) s += __shfl_xor(s, o);
    const float mean = s * (1.0f / D_DIM);
    float q = 0;
#pragma unroll
    for (int j = 0; j < 16; ++j) { const float d = v[j] - mean; q += d * d; }
#pragma unroll
    for (int o = 32; o > 0; o >>= 1) q += __shfl_xor(q, o);
    const float rstd = rsqrtf(q * (1.0f / D_DIM) + 1e-5f);
    if (lane == 0) *reinterpret_cast<f32x2*>(rstat1 + (row0 + i) * 2) = f32x2{rstd, -mean * rstd};
  }
  if (tid < 144) *reinterpret_cast<u32x4*>(flags + ((blockIdx.x - 192) * 144 + tid) * 4) = u32x4{0, 0, 0, 0};
  STAMP(0, 2);
}

__global__ __launch_bounds__(320) void ln_apply_kernel(const bf16* __restrict__ x, const float* __restrict__ modp,
                                                       const float* __restrict__ rstat1, bf16* __restrict__ H,
                                                       const bf16* __restrict__ Wq, const bf16* __restrict__ Wo,
                                                       char* __restrict__ wc,
                                                       char* __restrict__ tabs) {
  __shared__ __attribute__((aligned(16))) float tab[4][64];
  __shared__ __attribute__((aligned(16))) char sink[1024];
  STAMP(1, 0);
  if (threadIdx.x >= 256) {
    if (blockIdx.x >= 4) return;
    const int t = blockIdx.x * 64 + threadIdx.x - 256;
    const f32x4* mp = reinterpret_cast<const f32x4*>(modp + 3072) + t;
    const f32x4 sv = (mp[0] + mp[1536]) + (mp[3072] + mp[4608]);
    const f32x4 gw = (mp[256] + mp[1792]) + (mp[3328] + mp[4864]) + 1.0f;
    u32x2 gh, gl, sh, sl;
    split4(gw, gh, gl); split4(sv, sh, sl);
    char* d = tabs + (t >> 3) * 320 + (t & 7) * 8;
    *reinterpret_cast<u32x2*>(d) = gh; *reinterpret_cast<u32x2*>(d + 64) = gl;
    *reinterpret_cast<u32x2*>(d + 128) = sh; *reinterpret_cast<u32x2*>(d + 192) = sl;
    *reinterpret_cast<u32x2*>(d + 256) = u32x2{0, 0};
    f32x4 cb;
#pragma unroll
    for (int e = 0; e < 4; ++e) cb[e] = __builtin_fabsf(gw[e]) < 8.67361738e-19f ? 8.67361738e-19f : gw[e];
    f32x4* g = reinterpret_cast<f32x4*>(tabs + 16384) + (t >> 4) * 32 + (t & 15);
    g[0] = (mp[-256] + mp[1280]) + (mp[2816] + mp[4352]);
    g[16] = cb;
    return;
  }
  const int lane = threadIdx.x & 63, w = threadIdx.x >> 6;
  const int fr = lane & 15, fq = lane >> 4;
  const int kb = (blockIdx.x & 7) * 4 + w, mb0 = (blockIdx.x >> 3) * 4;
  u32x4 mr, xv[4], cp;
  u32x2 rr[4];
  asm volatile("" : "=v"(mr), "=v"(cp));
  const int wu_ = __builtin_amdgcn_readfirstlane(w);
  const int crow = (wu_ < 3 ? blockIdx.x * 12 + wu_ * 4 : blockIdx.x * 4) + ((lane >> 3) & 3);
  {
    bload_v<0>(mr, (unsigned)((fq * 6144 + ((lane >> 3) & 1) * 1024 + kb * 32 + (lane & 7) * 4) * 4), mk_rsrc(modp, 4 * 6144 * 4), 0);
    const rsrc_t rsX = mk_rsrc(x, S_LEN * D_DIM * 2), rsS = mk_rsrc(rstat1, S_LEN * 8);
    const unsigned vx = (unsigned)(((mb0 * 16 + fr) * D_DIM + kb * 32 + fq * 8) * 2), vs = (unsigned)((mb0 * 16 + fr) * 8);
#pragma unroll
    for (int i = 0; i < 4; ++i) {
      asm volatile("" : "=v"(xv[i]), "=v"(rr[i]));
      bload_v<0>(xv[i], vx, rsX, i * 32768);
      bload2_v<0>(rr[i], vs, rsS, i * 128);
    }
    const bf16* cs = wu_ < 3 ? Wq : Wo;
    bload_v<0>(cp, (unsigned)(crow * 2048 + (slow_col(cs) + (lane >> 5) * 8) * 128 + (lane & 7) * 16), mk_rsrc(cs, wu_ < 3 ? 3 * D_DIM * D_DIM * 2 : D_DIM * D_DIM * 2), 0);
    const rsrc_t rsP = mk_rsrc((const char*)Wq + (size_t)blockIdx.x * 24576, 24576);
    const unsigned vo = threadIdx.x * 16;
#pragma unroll
    for (int i = 0; i < 6; ++i) bload_lds<0>((unsigned)(uintptr_t)sink, vo, rsP, i * 4096);
#define K1T 6
  }
  asm volatile("s_waitcnt vmcnt(%9)" : "+v"(mr), "+v"(xv[0]), "+v"(xv[1]), "+v"(xv[2]), "+v"(xv[3]), "+v"(rr[0]), "+v"(rr[1]), "+v"(rr[2]), "+v"(rr[3]) : "n"(1 + K1T) : "memory");
  f32x4 mv = __builtin_bit_cast(f32x4, mr);
  f32x2 rs[4];
#pragma unroll
  for (int i = 0; i < 4; ++i) rs[i] = __builtin_bit_cast(f32x2, rr[i]);
#pragma unroll
  for (int e = 0; e < 4; ++e) { mv[e] += __shfl_xor(mv[e], 16); mv[e] += __shfl_xor(mv[e], 32); }
  if (lane < 16) *reinterpret_cast<f32x4*>(&tab[w][lane * 4]) = mv;
  __builtin_amdgcn_wave_barrier();
  asm volatile("s_waitcnt lgkmcnt(0)" ::: "memory");
  const f32x4 s0 = *reinterpret_cast<const f32x4*>(&tab[w][fq * 8]), s1 = *reinterpret_cast<const f32x4*>(&tab[w][fq * 8 + 4]);
  const f32x4 g0 = *reinterpret_cast<const f32x4*>(&tab[w][32 + fq * 8]) + 1.0f, g1 = *reinterpret_cast<const f32x4*>(&tab[w][32 + fq * 8 + 4]) + 1.0f;
#pragma unroll
  for (int i = 0; i < 4; ++i) {
    u32x4 ho;
#pragma unroll
    for (int d = 0; d < 4; ++d) {
      const f32x4 gg = d < 2 ? g0 : g1, ss = d < 2 ? s0 : s1;
      const float a = (bf_lo(xv[i][d]) * rs[i][0] + rs[i][1]) * gg[(d & 1) * 2] + ss[(d & 1) * 2];
      const float b = (bf_hi(xv[i][d]) * rs[i][0] + rs[i][1]) * gg[(d & 1) * 2 + 1] + ss[(d & 1) * 2 + 1];
      ho[d] = pk_bf16(a, b);
    }
    gst128((char*)H + ((size_t)(mb0 + i) * 32 + kb) * 1024 + lane * 16, ho);
  }
  asm volatile("s_waitcnt vmcnt(%1)" : "+v"(cp) : "n"(4 + K1T) : "memory");
  gst128(wc + (wu_ < 3 ? (lane >> 5) * 393216 : 786432 + (lane >> 5) * 131072) + crow * 128 + (lane & 7) * 16, cp);
  STAMP(1, 1);
}

constexpr int Q_SLOT = 21504, Q_NSL = 7;
constexpr int WO_H0 = 4 * Q_SLOT;
constexpr int QX_OFF = Q_SLOT * Q_NSL + 1024;
constexpr int QH_OFF = QX_OFF + 8192;
constexpr int QT_OFF = QH_OFF + 1024;
constexpr int Q_LDS = QT_OFF + 1024;
#define C3Q 1
#define C3O 1
#define OMAP 2
#define POLLW 0
#define OD 3
#define OMB 6
template <int OFF>
DI void bload_sc1(u32x4& d, unsigned voff, rsrc_t rs, unsigned soff) {
  asm volatile("buffer_load_dwordx4 %0, %1, %2, %3 offen offset:%4 sc1" : "+v"(d) : "v"(voff), "s"(rs), "s"(soff), "i"(OFF) : "memory");
}
DI void bload2_sc1(u32x2& d, unsigned voff, rsrc_t rs, unsigned soff) {
  asm volatile("buffer_load_dwordx2 %0, %1, %2, %3 offen sc1" : "+v"(d) : "v"(voff), "s"(rs), "s"(soff) : "memory");
}
DI void bload1_sc1(unsigned& d, unsigned voff, rsrc_t rs) {
  asm volatile("buffer_load_dword %0, %1, %2, 0 offen sc1" : "+v"(d) : "v"(voff), "s"(rs) : "memory");
}
DI unsigned wo_voff(int lane, int odd, int sh) {
  const int q = (lane >> 3) & 3, h = lane >> 5, ch = lane & 7, g = (q >> 1) | (odd << 1);
  return (unsigned)(((h * 16 + q) << sh) + ((((ch >> 1) ^ g) * 16) + (ch & 1) * 8) * 2);
}
DI rsrc_t wo_rsrc(const bf16* wo, const char* wc, int cn) {
  const bool c3 = C3O && (cn & 7) == slow_col(wo);
  return mk_rsrc(c3 ? wc + 786432 + (cn >> 3) * 131072 : (const char*)wo + cn * 128, c3 ? 131072 : D_DIM * D_DIM * 2 - cn * 128);
}

DI void qkv_loader(unsigned smem_base, int lane, int sw, int m0, int n0, int cn, const bf16* __restrict__ H, const bf16* __restrict__ W,
                   const bf16* __restrict__ wo, const char* __restrict__ wc) {
  const rsrc_t rsH = mk_rsrc(H, S_LEN * D_DIM * 2);
  const bool c3 = C3Q && ((n0 >> 6) & 7) == slow_col(W);
  const rsrc_t rsW = mk_rsrc(c3 ? wc + (n0 >> 9) * 393216 : (const char*)W + n0 * 2, c3 ? 393216 : D_DIM * 3 * D_DIM * 2 - n0 * 2);
  const unsigned sec = c3 ? 128u : 2048u, p16 = sec * 48u;
  int fb0 = (m0 >> 4) - 1 + 3 * sw; fb0 = fb0 < 0 ? 0 : fb0;
  const unsigned voffH0 = (unsigned)(fb0 * 32768 + lane * 16);
  const unsigned voffH1 = (unsigned)(((m0 >> 4) + 3 * sw) * 32768 + lane * 16);
  unsigned voffW[2];
#pragma unroll
  for (int i = 0; i < 2; ++i) {
    const int r = i * 8 + (lane >> 3), ch = lane & 7;
    const int g = ((r >> 1) & 1) | (((r >> 3) & 1) << 1);
    voffW[i] = (unsigned)((r * 3 + sw) * sec + ((((ch >> 1) ^ g) * 16) + (ch & 1) * 8) * 2);
  }
  const unsigned m0a = smem_base + sw * 3072, m0w = smem_base + 9216 + sw * 4096;
#define Q_LOAD(SOFF, U)                                                                                          \
  do {                                                                                                           \
    const unsigned so_ = (unsigned)(U) * 1024u;                                                                  \
    bload_lds<0>(m0a + (SOFF), voffH0, rsH, so_);                                                                \
    bload_lds<0>(m0a + (SOFF) + 1024, voffH1, rsH, so_);                                                         \
    bload_lds<0>(m0a + (SOFF) + 2048, voffH1, rsH, so_ + 32768u);                                                \
    _Pragma("unroll") for (int i = 0; i < 4; ++i)                                                                \
      bload_lds<0>(m0w + (SOFF) + i * 1024, voffW[i & 1], rsW, ((unsigned)(U) * 2u + (i >> 1)) * p16);           \
  } while (0)
#pragma unroll
  for (int u = 0; u < Q_NSL - 1; ++u) Q_LOAD(u * Q_SLOT, u);
  wait_vm<(Q_NSL - 2) * 7>();
  __builtin_amdgcn_s_barrier();
  unsigned sb = (Q_NSL - 1) * Q_SLOT;
  for (int u = 0; u < 33 - Q_NSL; ++u) {
    Q_LOAD(sb, u + Q_NSL - 1);
    sb = (sb == (Q_NSL - 1) * Q_SLOT) ? 0u : sb + Q_SLOT;
    wait_vm<(Q_NSL - 2) * 7>();
    __builtin_amdgcn_s_barrier();
  }
  const rsrc_t rsO = wo_rsrc(wo, wc, cn);
  const int sh = (C3O && (cn & 7) == slow_col(wo)) ? 7 : 11;
  const unsigned voffO[2] = {wo_voff(lane, sw & 1, sh), wo_voff(lane, (sw & 1) ^ 1, sh)};
  static_for<Q_NSL>([&](auto U) {
    constexpr int u = U.value, left = Q_NSL - 3 - u;
    (void)&rsO; (void)&voffO;
    if constexpr (u < 6) {
#pragma unroll
      for (int j = 0; j < 7; ++j) {
        const int pc = (u < 3 ? u * 21 : 64 + (u - 3) * 21) + 3 * j;
        const unsigned p = pc + sw;
        bload_lds<0>(smem_base + (u < 3 ? WO_H0 : -65536) + p * 1024u, voffO[pc & 1], rsO, ((p >> 2) * 32u + (p & 3) * 4u) << sh);
      }
    }
    if constexpr (left >= 0) wait_vm<left * 7 + (u + 1) * 7>();
    __builtin_amdgcn_s_barrier();
  });
  wait_vm<0>();
}

DI void qkv_spare(char* smem, unsigned smem_base, int lane, int cn, const char* __restrict__ tabs, const bf16* __restrict__ wo,
                  const bf16* __restrict__ wg, const bf16* __restrict__ wu, const char* __restrict__ wc) {
  const rsrc_t rsO = wo_rsrc(wo, wc, cn);
  const int sh = (C3O && (cn & 7) == slow_col(wo)) ? 7 : 11;
  const unsigned voffO = wo_voff(lane, 1, sh);
  __builtin_amdgcn_s_barrier();
  const rsrc_t rsP = mk_rsrc((const char*)wo + (size_t)blockIdx.x * 8192, 8192);
  const rsrc_t rsG = mk_rsrc((const char*)wg + (size_t)blockIdx.x * (TQ * 1024), TQ * 1024);
  const rsrc_t rsU = mk_rsrc((const char*)wu + (size_t)blockIdx.x * (TQ * 1024), TQ * 1024);
  static_for<33>([&](auto U) {
    constexpr int u = U.value;
    (void)&rsG; (void)&rsU; (void)&rsP;
    if constexpr (u == 26) bload_lds<0>(smem_base + WO_H0 + 63 * 1024, voffO, rsO, (15 * 32u + 12u) << sh);
    if constexpr (u == 32) bload_lds<0>(smem_base + 63 * 1024, voffO, rsO, (31 * 32u + 12u) << sh);
    if constexpr (u == 27) bload_lds<0>(smem_base + QT_OFF, lane * 16u, mk_rsrc(tabs + 16384 + cn * 512, 512), 0);
    if constexpr (u == DWO) touch8k(smem_base + QH_OFF, lane * 128u, rsP, 0);
#pragma unroll
    for (int j = 0; j < 2 * ((TQ + 7) / 8); ++j)
      if (DW0 + (j * DWS) / 2 == u) touch8k(smem_base + QH_OFF, lane * 128u, (j & 1) ? rsU : rsG, (j >> 1) * 8192u);
    __builtin_amdgcn_s_barrier();
  });
  asm volatile("s_waitcnt vmcnt(0) lgkmcnt(0)" ::: "memory");
}

DI void qkv_mfma(char* smem, unsigned smem_base, int tid, int wid, int m0, int n0, bf16* O, unsigned* oflags) {
  const int lane = tid & 63;
  const int wm = wid & 1, wn = wid >> 1;
  const int fr = lane & 15, fq = lane >> 4;
  unsigned brd[6];
  const unsigned hrd = smem_base + lane * 16, ard = hrd + 1024 + wm * 4096;
  {
    const int q = (lane >> 2) & 3, p = lane & 3;
    const int r = 8 * fq + q;
    const int g = ((r >> 1) & 1) | (((r >> 3) & 1) << 1);
#pragma unroll
    for (int jj = 0; jj < 6; ++jj) {
      brd[jj] = smem_base + 9216 + (jj >> 1) * 4096 + r * 128 + (((wn * 2 + ((jj & 1) ^ wm)) ^ g) * 32) + 8 * p;
    }
  }
  f32x4 acc[4][6], acch[2];
#pragma unroll
  for (int i = 0; i < 4; ++i)
#pragma unroll
    for (int j = 0; j < 6; ++j) { acc[i][j] = f32x4{0, 0, 0, 0}; asm volatile("" : "+v"(acc[i][j])); }
#pragma unroll
  for (int j = 0; j < 2; ++j) { acch[j] = f32x4{0, 0, 0, 0}; asm volatile("" : "+v"(acch[j])); }
  __builtin_amdgcn_s_setprio(2);
  __builtin_amdgcn_s_barrier();
  STAMPDECL;
  STAMPNW(2, 1);
  u32x4 aX[5], aY[5];
  u32x2 bXl[6], bXh[6], bYl[6], bYh[6];
#define Q_READ(AR, BL, BH, SO, R)                                                                                \
  do {                                                                                                           \
    if ((R) < 12) {                                                                                              \
      constexpr int jb = ((R) < 12 ? (R) : 0) >> 1;                                                              \
      if ((R) & 1) dsr_tr<512>(BH[jb], brd[jb] + (SO)); else dsr_tr<0>(BL[jb], brd[jb] + (SO));                  \
    } else if ((R) < 16) {                                                                                       \
      constexpr int ia = ((R) >= 12 && (R) < 16) ? (R) - 12 : 0;                                                 \
      dsr128<ia * 1024>(AR[ia], ard + (SO));                                                                     \
    } else {                                                                                                     \
      dsr128<0>(AR[4], hrd + (SO));                                                                              \
    }                                                                                                            \
  } while (0)
#define Q_MFMA(AR, BB, N)                                                                                        \
  do {                                                                                                           \
                                     \
    if ((N) < 16) MFMA_AV(acc[(N) & 3][((N) < 24 ? (N) : 0) >> 2], BB[((N) < 24 ? (N) : 0) >> 2], AR[(N) & 3]);  \
    else if ((N) < 24) MFMA_AV(acc[(N) & 3][((N) < 24 ? (N) : 0) >> 2], AR[(N) & 3], BB[((N) < 24 ? (N) : 0) >> 2]);  \
    else if ((N) == 24) MFMA_AV(acch[0], BB[2], AR[4]);                                                          \
    else MFMA_AV(acch[1], AR[4], BB[4]);                                                                         \
  } while (0)
#define Q_UNIT(AR, BL, BH, NAR, NBL, NBH)                                                                        \
  do {                                                                                                           \
    so = (so == (Q_NSL - 1) * Q_SLOT) ? 0u : so + Q_SLOT;                                                        \
    WAIT_LGKM0();                                                                                                \
    u32x4 bb[6];                                                                                                 \
    _Pragma("unroll") for (int j = 0; j < 6; ++j) bb[j] = u32x4{BL[j][0], BL[j][1], BH[j][0], BH[j][1]};         \
    SCHED0();                                                                                                    \
    asm volatile("s_nop 1");                                                                                     \
    static_for<26>([&](auto N) {                                                                                 \
      constexpr int n = N.value;                                                                                 \
      if constexpr (n == QBAR) __builtin_amdgcn_s_barrier();                                                   \
      Q_MFMA(AR, bb, n);                                                                                         \
      if constexpr (n >= QBAR && n < QBAR + QRN) {                                                               \
        constexpr int r0 = ((n - QBAR) * 17) / QRN, r1 = ((n - QBAR + 1) * 17) / QRN;                            \
        Q_READ(NAR, NBL, NBH, so, r0);                                                                           \
        if constexpr (r0 + 1 < r1) Q_READ(NAR, NBL, NBH, so, (r0 + 1 < 17 ? r0 + 1 : 0));                        \
      }                                                                                                          \
    });                                                                                                          \
    SCHED0();                                                                                                    \
  } while (0)
  unsigned so = 0;
  static_for<17>([&](auto R) { Q_READ(aX, bXl, bXh, 0u, R.value); });
  for (int u = 0; u < 32; u += 2) {
    Q_UNIT(aX, bXl, bXh, aY, bYl, bYh);
    Q_UNIT(aY, bYl, bYh, aX, bXl, bXh);
  }
  asm volatile("s_waitcnt vmcnt(0) lgkmcnt(0)\n\ts_nop 15" ::: "memory");
#pragma unroll
  for (int i = 0; i < 5; ++i) asm volatile("" : "+v"(aX[i]));
#pragma unroll
  for (int j = 0; j < 6; ++j) asm volatile("" : "+v"(bXl[j]), "+v"(bXh[j]));
  SCHED0();
  __builtin_amdgcn_s_setprio(0);
  STAMP(2, 2); STAMPFLUSH(2, 1);
#pragma unroll
  for (int i = 0; i < 4; ++i)
#pragma unroll
    for (int j = 0; j < 6; ++j) asm volatile("" : "+v"(acc[i][j]));
  asm volatile("" : "+v"(acch[0]), "+v"(acch[1]));
  auto pk2 = [](const f32x4 v) { return u32x2{pk_bf16(v[0], v[1]), pk_bf16(v[2], v[3])}; };
  auto cat = [](const u32x2 a, const u32x2 b) { return __builtin_bit_cast(bf16x8, u32x4{a[0], a[1], b[0], b[1]}); };
  u32x2 kp[5][2], vp[5][2];
  {
    char* xb = smem + QX_OFF + wn * 4096 + lane * 8;
#pragma unroll
    for (int f = 0; f < 2; ++f) { kp[4][f] = pk2(acc[3][2 + f]); vp[4][f] = pk2(acc[3][4 + f]); }
    *reinterpret_cast<u32x2*>(xb + wm * 512) = pk2(acch[0]);
    *reinterpret_cast<u32x2*>(xb + 1024 + wm * 512) = pk2(acch[1]);
    if (wm == 0) {
#pragma unroll
      for (int f = 0; f < 2; ++f) {
        *reinterpret_cast<u32x2*>(xb + 2048 + f * 512) = kp[4][f];
        *reinterpret_cast<u32x2*>(xb + 2048 + 1024 + f * 512) = vp[4][f];
      }
    }
    asm volatile("s_waitcnt lgkmcnt(0)" ::: "memory");
    __builtin_amdgcn_s_barrier();
    asm volatile("" ::: "memory");
#pragma unroll
    for (int f = 0; f < 2; ++f) {
      kp[0][f] = *reinterpret_cast<const u32x2*>(xb + wm * 2048 + (f ^ wm) * 512);
      vp[0][f] = *reinterpret_cast<const u32x2*>(xb + wm * 2048 + 1024 + (f ^ wm) * 512);
    }
  }
  STAMP(2, 3);
#pragma unroll
  for (int i = 0; i < 3; ++i)
#pragma unroll
    for (int f = 0; f < 2; ++f) { kp[1 + i][f] = pk2(acc[i][2 + f]); vp[1 + i][f] = pk2(acc[i][4 + f]); }
  const bool par = fr & 1;
  const bool cura = fq * 4 + (fr & 1) <= fr, curb = fq * 4 + 2 + (fr & 1) <= fr;
  const unsigned mh = par ? 0xffff0000u : 0x0000ffffu;
  const unsigned mca = cura ? mh : 0u, mpa = cura ? 0u : mh, mcb = curb ? mh : 0u, mpb = curb ? 0u : mh;
  const float cs = 0.17677669529663687f * 1.4426950408889634f;
  char* op = (char*)O + ((size_t)((m0 >> 6) + wm) * (D_DIM / 32) + (n0 >> 5) + wn) * 4096 + lane * 16;
  const f32x4 z4 = {0, 0, 0, 0};
#pragma unroll
  for (int ii = 0; ii < 4; ++ii) {
    const int i = 3 - ii;
    const bf16x8 qf = cat(pk2(acc[i][0]), pk2(acc[i][1]));
    const f32x4 sc = __builtin_amdgcn_mfma_f32_16x16x32_bf16(cat(kp[i + 1][0], kp[i + 1][1]), qf, z4, 0, 0, 0);
    f32x4 sp = __builtin_amdgcn_mfma_f32_16x16x32_bf16(cat(kp[i][0], kp[i][1]), qf, z4, 0, 0, 0);
    if (i == 0 && m0 == 0 && wm == 0) sp = f32x4{-5.656854249e9f, -5.656854249e9f, -5.656854249e9f, -5.656854249e9f};
    const float la = cura ? (par ? sc[1] : sc[0]) : (par ? sp[1] : sp[0]);
    const float lb = curb ? (par ? sc[3] : sc[2]) : (par ? sp[3] : sp[2]);
    float mx = fmaxf(la, lb);
    {
      const auto a = __builtin_amdgcn_permlane16_swap(__float_as_uint(mx), __float_as_uint(mx), false, false);
      mx = fmaxf(__uint_as_float(a[0]), __uint_as_float(a[1]));
      const auto b = __builtin_amdgcn_permlane32_swap(__float_as_uint(mx), __float_as_uint(mx), false, false);
      mx = fmaxf(__uint_as_float(b[0]), __uint_as_float(b[1]));
    }
    const float ea = __builtin_amdgcn_exp2f((la - mx) * cs), eb = __builtin_amdgcn_exp2f((lb - mx) * cs);
    const float inv = __builtin_amdgcn_rcpf(rowsum4(ea + eb));
    const unsigned wa = pk_bf16(ea, ea), wb = pk_bf16(eb, eb);
    const bf16x8 pf = __builtin_bit_cast(bf16x8, u32x4{wa & mpa, wb & mpb, wa & mca, wb & mcb});
    const f32x4 o0 = __builtin_amdgcn_mfma_f32_16x16x32_bf16(cat(vp[i][0], vp[i + 1][0]), pf, z4, 0, 0, 0) * inv;
    const f32x4 o1 = __builtin_amdgcn_mfma_f32_16x16x32_bf16(cat(vp[i][1], vp[i + 1][1]), pf, z4, 0, 0, 0) * inv;
    const u32x2 u0 = pk2(o0), u1 = pk2(o1);
    gst128(op + i * 1024, wm ? u32x4{u1[0], u1[1], u0[0], u0[1]} : u32x4{u0[0], u0[1], u1[0], u1[1]});
  }
  STAMP(2, 4);
  wait_vm<0>();
  asm volatile("buffer_store_dword %0, %1, %2, 0 offen sc1" ::"v"(1u), "v"(lane * (4096 * OFS)), "s"(mk_rsrc(oflags + ((m0 >> 1) + wm * 32 + (n0 >> 5) + wn) * OFS, (ORP - 1) * 4096 * OFS + 4)) : "memory");
  STAMP(2, 5);
}

DI void o_phase(char* smem, unsigned smem_base, int lane, int w, int cm, int cn, const bf16* O, const unsigned* oflags,
                const bf16* __restrict__ x, bf16* Y, float* stat) {
  const int wm = w & 3, kh = w >> 2;
  const int fr = lane & 15, fq = lane >> 4;
  const int R = cm * 8 + wm * 2 + kh;
  u32x2 xv[4];
  {
    const rsrc_t rsX = mk_rsrc(x, S_LEN * D_DIM * 2);
    const unsigned vx = (unsigned)(((R * 16 + fr) * D_DIM + cn * 64 + fq * 4) * 2);
#pragma unroll
    for (int f = 0; f < 4; ++f) asm volatile("" : "=v"(xv[f]));
    bload2_v<0>(xv[0], vx, rsX, 0); bload2_v<32>(xv[1], vx, rsX, 0); bload2_v<64>(xv[2], vx, rsX, 0); bload2_v<96>(xv[3], vx, rsX, 0);
  }
  const unsigned hb = smem_base + (kh ? 0 : WO_H0);
  unsigned brd[4];
  {
    const int q = (lane >> 2) & 3, p = lane & 3;
    const int r = 8 * fq + q;
    const int g = ((r >> 1) & 1) | (((r >> 3) & 1) << 1);
#pragma unroll
    for (int f = 0; f < 4; ++f) brd[f] = hb + r * 128 + ((f ^ g) * 32) + 8 * p;
  }
  const rsrc_t rsA = mk_rsrc((const char*)O + (size_t)(cm * 2 + (wm >> 1)) * 131072 + kh * 65536, 65536);
  unsigned va0 = lane * 16 + ((wm & 1) * 2 + kh) * 1024, va1 = va0 ^ 1024;
  unsigned tsrc = smem_base + QT_OFF + fq * 16, xw = hb + lane * 16 + wm * 4096, xr = smem_base + (kh ? WO_H0 : 0) + lane * 16 + wm * 4096;
  char* yb = (char*)Y + ((size_t)(R >> 4) * 32 + cn * 2 + (fq & 1)) * 16384 + (R & 15) * 1024 + ((fq >> 1) * 16 + fr) * 16;
  asm volatile("" : "+v"(tsrc), "+v"(xw), "+v"(xr), "+v"(yb), "+v"(va0), "+v"(va1), "+v"(brd[0]), "+v"(brd[1]), "+v"(brd[2]), "+v"(brd[3]));
  f32x4 acc[2][4];
#pragma unroll
  for (int i = 0; i < 2; ++i)
#pragma unroll
    for (int f = 0; f < 4; ++f) { acc[i][f] = f32x4{0, 0, 0, 0}; asm volatile("" : "+v"(acc[i][f])); }
  if (w == POLLW) {
    const rsrc_t rsF = mk_rsrc(oflags + ((blockIdx.x & (ORP - 1)) * 1024 + cm * 64) * OFS, 256 * OFS);
    unsigned spins = 0, f;
    do {
      asm volatile("buffer_load_dword %0, %1, %2, 0 offen sc1\n\ts_waitcnt vmcnt(0)" : "=v"(f) : "v"(lane * (4 * OFS)), "s"(rsF) : "memory");
    } while (!__all(f != 0u) && ++spins < (1u << 18));
  }
  __builtin_amdgcn_s_barrier();
  STAMPDECL;
  STAMPNW(3, 1);
  u32x4 a[16][2], gt[4], ct[4];
  u32x2 bl[2][4], bh[2][4];
  auto rdb = [&](auto S) {
    constexpr int s = S.value;
#pragma unroll
    for (int f = 0; f < 4; ++f) { dsr_tr<s * 4096>(bl[s & 1][f], brd[f]); dsr_tr<s * 4096 + 512>(bh[s & 1][f], brd[f]); }
  };
  auto lda = [&](auto S) {
    constexpr int s = S.value;
    asm volatile("" : "=v"(a[s][0]), "=v"(a[s][1]));
    bload_sc1<0>(a[s][0], va0, rsA, s * 4096);
    bload_sc1<0>(a[s][1], va1, rsA, s * 4096);
  };
  static_for<OD>([&](auto S) {
    lda(S);
    if constexpr (S.value == 0) rdb(ic<0>{});
    if constexpr (S.value == 1) rdb(ic<1>{});
  });
  static_for<16>([&](auto S) {
    constexpr int s = S.value;
    asm volatile("s_waitcnt vmcnt(%0) lgkmcnt(8)" ::"n"(2 * ((s + OD - 1 < 15 ? s + OD - 1 : 15) - s)) : "memory");
    SCHED0();
    u32x4 bb[4];
#pragma unroll
    for (int f = 0; f < 4; ++f) bb[f] = u32x4{bl[s & 1][f][0], bl[s & 1][f][1], bh[s & 1][f][0], bh[s & 1][f][1]};
    SCHED0();
    if constexpr (s + OD < 16) lda(ic<(s + OD < 16 ? s + OD : 0)>{});
    if constexpr (s == OMB) __builtin_amdgcn_s_barrier();
#pragma unroll
    for (int n = 0; n < 8; ++n) MFMA_AV(acc[1 - (n >> 2)][n & 3], bb[n & 3], a[s][1 - (n >> 2)]);
    if constexpr (s < 14) rdb(ic<(s < 14 ? s + 2 : 0)>{});
    if constexpr (s == 14) {
      static_for<4>([&](auto F) { dsr128<F.value * 64>(gt[F.value], tsrc); dsr128<256 + F.value * 64>(ct[F.value], tsrc); });
    }
    SCHED0();
  });
  STAMP(3, 2); STAMPFLUSH(3, 1);
  asm volatile("s_nop 7" ::: "memory");
  static_for<4>([&](auto F) { dsw128<F.value * 1024>(xw, __builtin_bit_cast(u32x4, acc[1][F.value])); });
  asm volatile("s_waitcnt lgkmcnt(0)" ::: "memory");
  __builtin_amdgcn_s_barrier();
  u32x4 pr[4];
  static_for<4>([&](auto F) { dsr128<F.value * 1024>(pr[F.value], xr); });
#pragma unroll
  for (int f = 0; f < 4; ++f) asm volatile("" : "+v"(acc[0][f]), "+v"(xv[f]), "+v"(gt[f]), "+v"(ct[f]));
  asm volatile("s_waitcnt lgkmcnt(0)" : "+v"(pr[0]), "+v"(pr[1]), "+v"(pr[2]), "+v"(pr[3])::"memory");
  f32x4 fin[4];
  u32x2 pk[4];
#pragma unroll
  for (int f = 0; f < 4; ++f) {
    const f32x4 xf = {bf_lo(xv[f][0]), bf_hi(xv[f][0]), bf_lo(xv[f][1]), bf_hi(xv[f][1])};
    fin[f] = xf + __builtin_bit_cast(f32x4, gt[f]) * (acc[0][f] + __builtin_bit_cast(f32x4, pr[f]));
    const f32x4 yv = fin[f] * __builtin_bit_cast(f32x4, ct[f]);
    pk[f] = u32x2{pk_bf16(yv[0], yv[1]), pk_bf16(yv[2], yv[3])};
  }
#pragma unroll
  for (int j = 0; j < 2; ++j) {
    const auto s0 = __builtin_amdgcn_permlane16_swap(pk[j][0], pk[j + 2][0], false, false);
    const auto s1 = __builtin_amdgcn_permlane16_swap(pk[j][1], pk[j + 2][1], false, false);
    gst128(yb + j * 512, u32x4{s0[0], s1[0], s0[1], s1[1]});
  }
  const f32x4 s4 = (fin[0] + fin[1]) + (fin[2] + fin[3]);
  const float sm = rowsum4((s4[0] + s4[1]) + (s4[2] + s4[3]));
  const float mb = sm * (1.0f / 64.0f);
  f32x4 q4 = {0, 0, 0, 0};
#pragma unroll
  for (int f = 0; f < 4; ++f) { const f32x4 d = fin[f] - mb; q4 += d * d; }
  const float m2 = rowsum4((q4[0] + q4[1]) + (q4[2] + q4[3]));
  asm volatile("buffer_store_dwordx2 %0, %1, %2, 0 offen sc1" ::"v"(f32x2{sm, m2}), "v"(lane * 8), "s"(mk_rsrc(stat + (cn * S_LEN + R * 16) * 2, 128)) : "memory");
}

#define SPOS 6
#define TPOS 6
#define G3B 30
#define NBC 1
constexpr int G3_CT = 131072 + 4096 + 1024;
#define G3_STAGE(SLOT, P, U)                                                                               \
  do {                                                                                                     \
    if ((P) == 0) bload_lds_sc1<0>(m0w + (SLOT) * 32768, voffA, rsA, (unsigned)(U) * 16384u);              \
    else if ((P) == 1) bload_lds_sc1<1024>(m0w + (SLOT) * 32768, voffA, rsA, (unsigned)(U) * 16384u);      \
    else if ((P) == 2) bload_lds<0>(m0w + (SLOT) * 32768 + 16384, voffB0, rsB, (unsigned)(U) * 229376u);   \
    else bload_lds<0>(m0w + (SLOT) * 32768 + 16384 + 1024, voffB1, rsB, (unsigned)(U) * 229376u);          \
  } while (0)

template <int FN>
DI void g3_body(char* smem, unsigned smem_base, int wm, int wn, int wid, int lane, int bm, int bn, rsrc_t rsA, rsrc_t rsB,
                rsrc_t rsP, unsigned voffA, unsigned voffB0, unsigned voffB1, bf16* __restrict__ act, const float* stat) {
  constexpr int NB = 2 * FN, NM = 4 * NB, RD = 5 + 2 * NB;
  const int fr = lane & 15, fq = lane >> 4;
  f32x4 acc[4][NB], accC[2];
  accC[0] = f32x4{0, 0, 0, 0}; accC[1] = f32x4{0, 0, 0, 0};
  asm volatile("" : "+v"(accC[0]), "+v"(accC[1]));
  const int rot = wm % FN;
  unsigned cad = smem_base + G3_CT + (fr < 4 ? fr * 64 : 256) + fq * 16;
#pragma unroll
  for (int i = 0; i < 4; ++i)
#pragma unroll
    for (int j = 0; j < NB; ++j) { acc[i][j] = f32x4{0, 0, 0, 0}; asm volatile("" : "+v"(acc[i][j])); }
  unsigned aad[2], bad[2][FN];
  aad[0] = smem_base + wm * 4096 + lane * 16; aad[1] = aad[0] + 65536;
  {
    const int q = (lane >> 2) & 3, p = lane & 3;
    const int r = 8 * fq + q;
    const int sw = (r & 3) | (((r >> 3) & 1) << 2);
#pragma unroll
    for (int j = 0; j < FN; ++j) {
      const int gi = wn * 4 + (j + rot) % FN + (bn & 1);
      bad[0][j] = smem_base + 16384 + r * 256 + ((gi ^ sw) * 32) + 8 * p;
      bad[1][j] = bad[0][j] + 65536;
    }
  }
  u32x4 aX[5], aY[5];
  u32x2 bXl[NB], bXh[NB], bYl[NB], bYh[NB];
  const unsigned m0w = smem_base + wid * 2048;
  u32x2 tv[16];
  const rsrc_t rsS = mk_rsrc(stat, S_LEN * 128);
  const unsigned voffS = (unsigned)((bm * 256 + wm * 64 + lane) * 8);
  if constexpr (FN == 3) {
#pragma unroll
    for (int i = 0; i < 16; ++i) asm volatile("" : "=v"(tv[i]));
  }
  auto lds_ = [&](auto SL) {
#pragma unroll
    for (int i = SL.value * 4; i < SL.value * 4 + 4; ++i)
      bload2_sc1(tv[i], voffS, rsS, i * 16384);
  };
#define G3_READ(AR, BL, BH, SLOT, R)                                                                       \
  do {                                                                                                     \
    if ((R) < 2 * NB) {                                                                                    \
      constexpr int jb = ((R) < 2 * NB ? (R) : 0) >> 1;                                                    \
      constexpr int off = ((SLOT) & 1) * 32768 + (jb / FN) * 8192 + ((R) & 1) * 1024;                      \
      if ((R) & 1) dsr_tr<off>(BH[jb], bad[(SLOT) >> 1][jb % FN]);                                         \
      else dsr_tr<off>(BL[jb], bad[(SLOT) >> 1][jb % FN]);                                                 \
    } else if ((R) < 2 * NB + 4) {                                                                         \
      constexpr int ia = ((R) >= 2 * NB && (R) < 2 * NB + 4) ? (R) - 2 * NB : 0;                           \
      dsr128<((SLOT) & 1) * 32768 + ia * 1024>(AR[ia], aad[(SLOT) >> 1]);                                  \
    } else {                                                                                               \
      if ((SLOT) == 0) cad += 1280;                                      \
      dsr128<(SLOT) * 320>(AR[4], cad);                                                                    \
    }                                                                                                      \
  } while (0)
  auto rd = [&](auto SL, auto RR, u32x4 (&AR)[5], u32x2 (&BL)[NB], u32x2 (&BH)[NB]) { G3_READ(AR, BL, BH, SL.value, RR.value); };
#define G3_UNIT(AR, BL, BH, NAR, NBL, NBH, SLOT, U)                                                        \
  do {                                                                                                     \
    WAIT_LGKM0();                                                                                          \
    u32x4 bb[NB];                                                                                          \
    _Pragma("unroll") for (int j = 0; j < NB; ++j) bb[j] = u32x4{BL[j][0], BL[j][1], BH[j][0], BH[j][1]};  \
    SCHED0();                                                                                              \
    asm volatile("s_nop 1");                                                                               \
    static_for<NM>([&](auto N) {                                                                           \
      constexpr int n = N.value;                                                                           \
      if constexpr (n == NM / 2) {                                                                         \
        wait_vm<8>();           __builtin_amdgcn_s_barrier();                                             \
      }                                                                                                    \
      if constexpr (FN == 3 && n == NM / 2 + SPOS) {                                                       \
        if (__builtin_expect(u == 4, 0)) lds_(ic<SLOT>{});                                                 \
      }                                                                                                    \
      if constexpr (FN == 4 && (G3B & 3) == (SLOT) && n == NM / 2 + TPOS) {                                \
                                                                 \
        if (__builtin_expect((U) == G3B, 0)) touch8k(smem_base + 131072 + 4096, lane * 128u, rsP, wid * 8192u); \
      }                                                                                                    \
      MFMA_AV(acc[n & 3][n >> 2], bb[n >> 2], AR[n & 3]);                                                  \
      if constexpr (n < NM / 2) {                                                                          \
        if constexpr (n % 3 == 1 && n / 3 < 4) G3_STAGE(((SLOT) + 3) & 3, n / 3, (U) + 3);                 \
      } else {                                                                                             \
        constexpr int r0 = (n - NM / 2) * 2;                                                               \
        if constexpr (r0 < RD) rd(ic<((SLOT) + 1) & 3>{}, ic<(r0 < RD ? r0 : 0)>{}, NAR, NBL, NBH);        \
        if constexpr (r0 + 1 < RD) rd(ic<((SLOT) + 1) & 3>{}, ic<(r0 + 1 < RD ? r0 + 1 : 0)>{}, NAR, NBL, NBH); \
      }                                                                                                    \
    });                                                                                                    \
    if (NBC || FN == 4 || wm < 3) {                                                                        \
      MFMA_AV(accC[0], bb[0], AR[4]);                                                                      \
      MFMA_AV(accC[1], bb[FN], AR[4]);                                                                     \
    }                                                                                                      \
    SCHED0();                                                                                              \
  } while (0)

  asm volatile("s_waitcnt vmcnt(6) lgkmcnt(0)" ::: "memory");
  __builtin_amdgcn_s_barrier();
  STAMPDECL;
  STAMPNW(5, 1);
  cad -= 1280;
  static_for<RD>([&](auto RR) { rd(ic<0>{}, RR, aX, bXl, bXh); });
  for (int u = 0; u < 32; u += 4) {
    if constexpr (FN == 3) {
#pragma unroll
      for (int i = 0; i < 16; ++i) asm volatile("" : "+v"(tv[i]));
      if (__builtin_expect(u == 12, 0)) {
        float s = 0;
#pragma unroll
        for (int i = 0; i < 16; i += 2) s += __uint_as_float(tv[i][0]) + __uint_as_float(tv[i + 1][0]);
        const float mean = s * (1.0f / D_DIM);
        float m2 = 0;
#pragma unroll
        for (int i = 0; i < 16; i += 2) {
          const float d0 = __uint_as_float(tv[i][0]) * (1.0f / 64.0f) - mean, d1 = __uint_as_float(tv[i + 1][0]) * (1.0f / 64.0f) - mean;
          m2 += __uint_as_float(tv[i][1]) + __uint_as_float(tv[i + 1][1]) + 64.0f * (d0 * d0 + d1 * d1);
        }
        const float rstd = rsqrtf(m2 * (1.0f / D_DIM) + 1e-5f);
        reinterpret_cast<f32x2*>(smem + 131072)[wm * 64 + lane] = f32x2{rstd, -mean * rstd};
      }
    }
    G3_UNIT(aX, bXl, bXh, aY, bYl, bYh, 0, u);
    G3_UNIT(aY, bYl, bYh, aX, bXl, bXh, 1, u + 1);
    G3_UNIT(aX, bXl, bXh, aY, bYl, bYh, 2, u + 2);
    G3_UNIT(aY, bYl, bYh, aX, bXl, bXh, 3, u + 3);
  }
  asm volatile("s_waitcnt lgkmcnt(0)\n\ts_nop 15" ::: "memory");
#pragma unroll
  for (int i = 0; i < 5; ++i) asm volatile("" : "+v"(aX[i]));
#pragma unroll
  for (int j = 0; j < NB; ++j) asm volatile("" : "+v"(bXl[j]), "+v"(bXh[j]));
  SCHED0();
  STAMP(5, 2);
  STAMPFLUSH(5, 1);
  const f32x2* rstat = reinterpret_cast<const f32x2*>(smem + 131072);
  float* colc = reinterpret_cast<float*>(smem + 131072 + 2048);
#pragma unroll
  for (int m = 0; m < 2; ++m) {
    f32x4 v;
#pragma unroll
    for (int r = 0; r < 4; ++r) v[r] = accC[m][r] + __shfl_xor(accC[m][r], 1);
    if ((fr == 0 || fr == 2) && wm < FN) *reinterpret_cast<f32x4*>(colc + (m * 2 + (fr >> 1)) * 128 + (wn * 4 + rot) * 16 + fq * 4) = v;
  }
  asm volatile("s_waitcnt lgkmcnt(0)" ::: "memory");
  __builtin_amdgcn_s_barrier();
  asm volatile("" ::: "memory");
  f32x2 rs[4];
#pragma unroll
  for (int i = 0; i < 4; ++i) rs[i] = rstat[wm * 64 + i * 16 + fr];
#pragma unroll
  for (int j = 0; j < FN; ++j) {
    const int jg = (j + rot) % FN;
    const int cl = (wn * 4 + jg) * 16 + fq * 4;
    const f32x4 csg = *reinterpret_cast<const f32x4*>(colc + cl), bg = *reinterpret_cast<const f32x4*>(colc + 128 + cl);
    const f32x4 csu = *reinterpret_cast<const f32x4*>(colc + 256 + cl), bu = *reinterpret_cast<const f32x4*>(colc + 384 + cl);
    u32x2 pk[4];
#pragma unroll
    for (int i = 0; i < 4; ++i) {
#pragma unroll
      for (int hh = 0; hh < 2; ++hh) {
        const f32x2 ag = hh ? hi2(acc[i][j]) : lo2(acc[i][j]), au = hh ? hi2(acc[i][FN + j]) : lo2(acc[i][FN + j]);
        const f32x2 g = rs[i][0] * ag + (rs[i][1] * (hh ? hi2(csg) : lo2(csg)) + (hh ? hi2(bg) : lo2(bg)));
        const f32x2 uu = rs[i][0] * au + (rs[i][1] * (hh ? hi2(csu) : lo2(csu)) + (hh ? hi2(bu) : lo2(bu)));
        const f32x2 ea = g * -1.4426950408889634f;
        f32x2 e = {__builtin_amdgcn_exp2f(ea[0]), __builtin_amdgcn_exp2f(ea[1])};
        e = e + 1.0f;
        const f32x2 rc = {__builtin_amdgcn_rcpf(e[0]), __builtin_amdgcn_rcpf(e[1])};
        const f32x2 o = (g * uu) * rc;
        pk[i][hh] = pk_bf16(o[0], o[1]);
      }
    }
    const int J = bn * 7 + wn * 4 + jg;
    char* dst = (char*)act + ((size_t)((bm * 4 + wm) * (F_DIM / 32) + (J >> 1)) * 4 + (fq & 1)) * 1024 + (((J & 1) * 2 + (fq >> 1)) * 16 + fr) * 16;
#pragma unroll
    for (int i = 0; i < 4; i += 2) {
      const auto s0 = __builtin_amdgcn_permlane16_swap(pk[i][0], pk[i + 1][0], false, false);
      const auto s1 = __builtin_amdgcn_permlane16_swap(pk[i][1], pk[i + 1][1], false, false);
      gst128(dst + i * 1024, u32x4{s0[0], s1[0], s0[1], s1[1]});
    }
  }
}

DI void g3_phase(char* smem, unsigned smem_base, int tid, int wid, const bf16* Y, const bf16* __restrict__ Wg,
                 const bf16* __restrict__ Wu, bf16* __restrict__ act, const float* stat,
                 const bf16* __restrict__ Wd, unsigned* yflags, int own, const char* __restrict__ tabs) {
  const int lane = tid & 63;
  const int wm = wid & 3, wn = wid >> 2;
  const int xcd = blockIdx.x & 7, idx = blockIdx.x >> 3;
  const int bm = idx & 7, bn = xcd * 4 + (idx >> 3);
  const int n0 = bn * 112, n0a = n0 & ~31;
  const rsrc_t rsA = mk_rsrc((const char*)Y + (size_t)bm * 32 * 16384, 32 * 16384);
  const rsrc_t rsB = mk_rsrc(wn ? Wu : Wg, D_DIM * F_DIM * 2);
  const rsrc_t rsP = mk_rsrc((const char*)Wd + (size_t)blockIdx.x * 28672, 28672);
  const unsigned voffA = wid * 2048 + lane * 16;
  unsigned voffB[2];
#pragma unroll
  for (int i = 0; i < 2; ++i) {
    const int r = (wm * 2 + i) * 4 + (lane >> 4);
    const int sw = (r & 3) | (((r >> 3) & 1) << 2);
    const int c16 = lane & 15;
    voffB[i] = (unsigned)(r * (F_DIM * 2) + (n0a + (((c16 >> 1) ^ sw) * 16) + (c16 & 1) * 8) * 2);
  }
  const unsigned m0w = smem_base + wid * 2048;
  const unsigned voffB0 = voffB[0], voffB1 = voffB[1];
  G3_STAGE(0, 2, 0); G3_STAGE(0, 3, 0); G3_STAGE(1, 2, 1); G3_STAGE(1, 3, 1);
  wait_vm<4>();
  __builtin_amdgcn_s_barrier();
  STAMP(3, 4);
  if (wn == 0) {
    if (wm == 0) {
      asm volatile("buffer_store_dword %0, %1, %2, 0 offen sc1" ::"v"(1u), "v"(lane * (1024 * YFS)), "s"(mk_rsrc(yflags + own * YFS, 7 * 1024 * YFS + 4)) : "memory");
      const rsrc_t rsF = mk_rsrc(yflags + (xcd * 256 + bm * 32) * YFS, 128 * YFS);
      if (YSL) __builtin_amdgcn_s_sleep(YSL);
      unsigned spins = 0, f;
      do {
        asm volatile("buffer_load_dword %0, %1, %2, 0 offen sc1\n\ts_waitcnt vmcnt(0)" : "=v"(f) : "v"((lane & 31) * (4 * YFS)), "s"(rsF) : "memory");
      } while (!__all(f != 0u) && ++spins < (1u << 18));
    }
    __builtin_amdgcn_s_barrier();
    STAMP(5, 0);
    G3_STAGE(0, 0, 0); G3_STAGE(0, 1, 0); G3_STAGE(1, 0, 1); G3_STAGE(1, 1, 1);
    G3_STAGE(2, 0, 2); G3_STAGE(2, 1, 2); G3_STAGE(2, 2, 2); G3_STAGE(2, 3, 2);
    g3_body<4>(smem, smem_base, wm, wn, wid, lane, bm, bn, rsA, rsB, rsP, voffA, voffB0, voffB1, act, stat);
  } else {
    const rsrc_t rsT = mk_rsrc(tabs, 32 * 320);
#pragma unroll
    for (int j = 0; j < 3; ++j) bload_lds<0>(smem_base + G3_CT + (wm * 3 + j) * 1024, lane * 16u, rsT, (wm * 3 + j) * 1024u);
    __builtin_amdgcn_s_barrier();
    G3_STAGE(0, 0, 0); G3_STAGE(0, 1, 0); G3_STAGE(1, 0, 1); G3_STAGE(1, 1, 1);
    G3_STAGE(2, 0, 2); G3_STAGE(2, 1, 2); G3_STAGE(2, 2, 2); G3_STAGE(2, 3, 2);
    g3_body<3>(smem, smem_base, wm, wn, wid, lane, bm, bn, rsA, rsB, rsP, voffA, voffB0, voffB1, act, stat);
  }
}

__global__ __launch_bounds__(512, 2) void qkv_attn_kernel(const bf16* __restrict__ H, const bf16* __restrict__ W,
                                                          bf16* O, const bf16* __restrict__ wg,
                                                          const bf16* __restrict__ wu, const bf16* __restrict__ wo,
                                                          const bf16* __restrict__ x,
                                                          bf16* Y, float* stat, unsigned* oflags,
                                                          const char* __restrict__ wc, bf16* __restrict__ act,
                                                          const bf16* __restrict__ wd, const char* __restrict__ tabs) {
  __shared__ __attribute__((aligned(16))) char smem[Q_LDS];
  STAMP(2, 0);
  const int tid = threadIdx.x;
  const int wid = __builtin_amdgcn_readfirstlane(tid >> 6);
  const unsigned smem_base = (unsigned)(uintptr_t)smem;
  const int xcd = blockIdx.x & 7, idx = blockIdx.x >> 3;
  const int bm = (xcd >> 2) * 8 + (idx & 7), bn = (xcd & 3) * 4 + (idx >> 3);
  const int m0 = bm * 128, n0 = bn * 64;
  const int cm = (xcd >> 1) * 4 + (idx & 3), cn = __builtin_amdgcn_readfirstlane((xcd & 1) * 8 + (idx >> 2));
  if (wid < 4) qkv_mfma(smem, smem_base, tid, wid, m0, n0, O, oflags);
  else if (wid < 7) qkv_loader(smem_base, tid & 63, wid - 4, m0, n0, cn, H, W, wo, wc);
  else qkv_spare(smem, smem_base, tid & 63, cn, tabs, wo, wg, wu, wc);
  STAMPT(2, 6, 320);
  STAMPT(3, 0, 448);
  o_phase(smem, smem_base, tid & 63, wid, cm, cn, O, oflags, x, Y, stat);
  g3_phase(smem, smem_base, tid, wid, Y, wg, wu, act, stat, wd, oflags + 7168, cm * 16 + cn, tabs);
  STAMP(5, 3);
  STAMP(5, 4);
}

#define XST 0
#define G4NF 11
#define HFS 3
#define HFL 4
#define HCK 5
#define XMAP 1
template <int OFF>
DI void bstore_sc1(u32x4 d, unsigned voff, rsrc_t rs, unsigned soff) {
  asm volatile("buffer_store_dwordx4 %0, %1, %2, %3 offen offset:%4 sc1\n\ts_nop 1" ::"v"(d), "v"(voff), "s"(rs), "s"(soff), "i"(OFF) : "memory");
}
namespace g4 {
constexpr int NF = G4NF, NH = 28 - 2 * NF, NS = NF + NH, HEP = NH - 3;
constexpr int nslot(int g) { return g < NF ? 64 : 32; }
constexpr int wslot(int g) { return g < NF - 1 ? 48 : (g == NF - 1 ? 56 : 24); }
constexpr int ring(int g, int n) {
  if (g < NF - 1) {
    if (n >= 48 || n % 3 != 1) return -1;
    const int l = n / 3;
    if (g + 2 < NF) return (g + 2) * 16 + l;
    return l < 12 ? (g + 2) * 16 + (l < 8 ? l : l + 4) : -1;
  }
  if (g == NF - 1) { if (n >= 48 || n % 4 != 1) return -1; const int l = n / 4; return (g + 2) * 16 + (l < 8 ? l : l + 4); }
  if (g == NF) { if (n >= 24) return -1; const int l = n % 12; return (g + 2 + n / 12) * 16 + (l < 8 ? l : l + 4); }
  if (g + 3 >= NS || n >= 24 || (n & 1)) return -1;
  const int l = n >> 1;
  return (g + 3) * 16 + (l < 8 ? l : l + 4);
}
constexpr int pwrite(int g, int n) {
  const int m = g == NF - 1 ? n : 64 + n;
  for (int e = 0; e < 24; ++e) if (12 + (13 * e) / 4 == m) return e;
  return -1;
}
constexpr int red0(int n) { return n < 8 ? 0 : (64 * (n - 8)) / 24; }
#define SPS 4
constexpr int sslot(int f) {
  const int ready = (f >> 2) * 32 + 8 + (((f & 3) + 1) * 16 * 24 + 63) / 64;
  const int paced = (f / SPS) * 32 + 15 + (f % SPS) * (32 / SPS);
  int m = ready > paced ? ready : paced;
  if ((m & 1) == 0) ++m;
  return m;
}
constexpr int dstore_f(int g, int n) {
  for (int f = 0; f < 8; ++f) if (sslot(f) == (g - NF) * 32 + n) return f;
  return -1;
}
constexpr bool dstore(int g, int n) { return g >= NF && dstore_f(g, n) >= 0; }
constexpr int vmops(int g, int n) {
  int c = (ring(g, n) >= 0 ? 1 : 0) + (dstore(g, n) ? 1 : 0);
  const int h = g - NF;
  if (h == HFS && n == 24) c += 1;
  if (h == HFL && n == 25) c += 1;
  if (h == HCK && n >= 24) c += 1;
  if (h == HEP && (n == 1 || n == 3)) c += 4;
  return c;
}
constexpr int between(int g0, int n0, int g1, int n1) {
  int c = 0;
  for (int g = g0; g <= g1; ++g)
    for (int n = (g == g0 ? n0 + 1 : 0); n < (g == g1 ? n1 : nslot(g)); ++n) c += vmops(g, n);
  return c;
}
constexpr int imin(int a, int b) { return a < b ? a : b; }
constexpr int waitcnt(int g) {
  int c = 63;
  if (g + 1 < NS && g + 1 >= 2) {
    for (int s = 0; s <= g; ++s)
      for (int n = 0; n < nslot(s); ++n)
        if (ring(s, n) == (g + 1) * 16 + 15) c = imin(c, between(s, n, g, wslot(g)) + (dstore(s, n) ? 1 : 0));
  }
  if (g == 0) c = 16;
  if (g - NF == HFS) c = imin(c, between(NF + sslot(7) / 32, sslot(7) % 32, g, 24));
  if (g - NF == HCK) c = imin(c, between(NF + HFL, 25, g, 24));
  return c;
}
constexpr int bslot(int b) { return b < NF + 3 ? b % 3 : ((b - NF) % 4 == 3 ? 3 : (NF + (b - NF) % 4) % 3); }
}
__global__ __launch_bounds__(256, 1) void gemm4_kernel(const bf16* __restrict__ At, const bf16* __restrict__ W,
                                                       const bf16* __restrict__ Yt, const float* __restrict__ modp,
                                                       bf16* __restrict__ C, float* xbuf, unsigned* flags) {
  using namespace g4;
  constexpr int KB = F_DIM / 32;
  constexpr int GT = 96 * 1024;
  __shared__ __attribute__((aligned(16))) char smem[GT + 1024];
  STAMPDECL;
  STAMP(6, 0);
  const int tid = threadIdx.x, lane = tid & 63;
  const int w = __builtin_amdgcn_readfirstlane(tid >> 6);
  const int xcd = blockIdx.x & 7, idx = blockIdx.x >> 3;
  const int kh = xcd & 1, bm = ((xcd >> 1) & 1) * 8 + (idx & 7), bn = (xcd >> 2) * 4 + (idx >> 3);
  const unsigned partner = blockIdx.x ^ 1;
  const int n0 = bn * 128, bm64 = bm * 2 + kh;
  const int fr = lane & 15, fq = lane >> 4;
  const rsrc_t rsA0 = mk_rsrc((const char*)At + (size_t)(bm64 ^ 1) * KB * 4096, KB * 4096);
  const rsrc_t rsA1 = mk_rsrc((const char*)At + (size_t)bm64 * KB * 4096, KB * 4096);
  const rsrc_t rsB = mk_rsrc((const char*)W + n0 * 2, KB * 65536 - n0 * 2);
  const unsigned voffL = lane * 16;
  unsigned voffA[4];
#pragma unroll
  for (int i = 0; i < 4; ++i) voffA[i] = voffL + ((i + w) & 3) * 1024;
  const unsigned voffB = fq * 8 * 2048 + fr * 16;
  u32x4 braw[4][8], afr[3][8], afa[4][4];
  u32x4 T[2][2];
  u32x4 xv[4], pv[8];
  unsigned gv[4], flg;
  f32x4 acc[8][8];
#pragma unroll
  for (int r = 0; r < 3; ++r)
#pragma unroll
    for (int j = 0; j < 8; ++j) asm volatile("" : "=v"(braw[r][j]), "=v"(afr[r][j]));
  const int kbF = (kh ? KB - 4 * NF : 0) + w, kbH = w;
#define ELOAD(B, L)                                                                                \
  do {                                                                                             \
    const unsigned kbv = (unsigned)(((B) < NF ? kbF : kbH) + (B) * 4);                             \
    if constexpr ((L) < 8) {                                                                       \
      const unsigned so = kbv * 65536u + ((L) >> 1) * 4096u;                                       \
      if constexpr (bslot(B) == 3 || (B) >= NF + 3) asm volatile("" : "=v"(braw[bslot(B)][(L) & 7]));        \
      if constexpr ((L) & 1) bload_v<2048>(braw[bslot(B)][(L) & 7], voffB, rsB, so);               \
      else bload_v<0>(braw[bslot(B)][(L) & 7], voffB, rsB, so);                                    \
    } else if constexpr ((B) >= NF + 2) {                                                          \
      asm volatile("" : "=a"(afa[((B) - NF) & 3][(L) & 3]));                                       \
      bload_a<0>(afa[((B) - NF) & 3][(L) & 3], voffA[(L) & 3], rsA1, kbv * 4096u);                 \
    } else {                                                                                       \
      bload_v<0>(afr[(B) % 3][(L) & 7], voffA[(L) & 3], ((L) & 4) ? rsA1 : rsA0, kbv * 4096u);      \
    }                                                                                              \
  } while (0)
#define DPERM(TB, Q, SL, PP)                                                                       \
  do {                                                                                             \
    if ((Q) & 1) PERM_HI(T[TB][1][(Q) >> 1], braw[SL][((Q) >> 1) * 2][PP], braw[SL][((Q) >> 1) * 2 + 1][PP]); \
    else PERM_LO(T[TB][0][(Q) >> 1], braw[SL][((Q) >> 1) * 2][PP], braw[SL][((Q) >> 1) * 2 + 1][PP]);         \
  } while (0)

#define MFMA_E(C, B, A) asm volatile("v_mfma_f32_16x16x32_bf16 %0, %1, %2, %0" : "+a"(C) : "v"(B), "v"(A))
  static_for<16>([&](auto L) { ELOAD(0, L.value); });
  static_for<16>([&](auto L) { ELOAD(1, L.value); });
#pragma unroll
  for (int i = 0; i < 8; ++i)
#pragma unroll
    for (int f = 0; f < 8; ++f) { acc[i][f] = f32x4{0, 0, 0, 0}; asm volatile("" : "+a"(acc[i][f])); }
  const unsigned sbase = (unsigned)(uintptr_t)smem + voffL;
  unsigned wlo[3];
#pragma unroll
  for (int i = 1; i < 4; ++i) wlo[i - 1] = sbase + (((w + i) & 3) * 3 + (i - 1)) * 8192;
  const unsigned rlo = sbase + w * 24576;
  const rsrc_t rsEx = mk_rsrc(xbuf, 256 * 32768);
  const unsigned exo = (unsigned)(blockIdx.x * 32768 + w * 8192);
  const rsrc_t rsFo = mk_rsrc(flags + blockIdx.x * 4 + w, 4), rsFi = mk_rsrc(flags + partner * 4 + w, 4);
  wait_vm<16>();
  STAMPNW(6, 1);
#pragma unroll
  for (int q = 0; q < 8; ++q) DPERM(0, q, 0, 0);
  asm volatile("s_nop 1");
  u32x4 pr[3][4];
  f32x4 tt[8], uu;
  static_for<NS>([&](auto G) {
    constexpr int g = G.value, h = g - NF, sl = bslot(g), ns = bslot(g + 1);
    constexpr bool full = g < NF - 1, lastf = g == NF - 1;
    constexpr bool red = (h == 0 || h == 1);
    constexpr int fb = h == 1 ? 4 : 0;
    if constexpr (h == 0 || h == 1) { WAIT_LGKM0(); __builtin_amdgcn_s_barrier(); }
    if constexpr (h == 3) __builtin_amdgcn_s_barrier();
    static_for<nslot(g)>([&](auto N) {
      constexpr int n = N.value;
      if constexpr (n == wslot(g)) {
        if constexpr (waitcnt(g) < 63) {
          if constexpr (h == HCK) asm volatile("s_waitcnt vmcnt(%1)" : "+v"(flg) : "n"(waitcnt(g)) : "memory");
          else wait_vm<waitcnt(g)>();
        }
        if constexpr (h == HFS) asm volatile("buffer_store_dword %0, %1, %2, 0 offen sc1" ::"v"(1u), "v"(voffL >> 2), "s"(rsFo) : "memory");
        if constexpr (h == HCK) {
          unsigned spins = 0;
          while (__builtin_amdgcn_readfirstlane(flg) == 0u && spins < (1u << 20)) {
            __builtin_amdgcn_s_sleep(1);
            bload1_sc1(flg, 0u, rsFi);
            asm volatile("s_waitcnt vmcnt(0)" : "+v"(flg)::"memory");
            ++spins;
          }
        }
      }
      if constexpr (red && n == 8) WAIT_LGKM0();
      if constexpr (full) {
        constexpr int p = n >> 4, q = n & 15;
        MFMA_E(acc[q & 7][2 * p + (q >> 3)], T[p & 1][q >> 3], afr[g % 3][q & 7]);
        if constexpr ((q & 1) == 0) {
          if constexpr (p < 3) DPERM((p + 1) & 1, q >> 1, sl, p + 1);
          else DPERM(0, q >> 1, ns, 0);
        }
      } else {
        constexpr int p = (n >> 3) & 3, q = n & 7, ib = (lastf && n < 32) ? 0 : 4;
        if constexpr (h >= 2) MFMA_A(acc[ib + (q & 3)][2 * p + (q >> 2)], T[p & 1][q >> 2], afa[h & 3][q & 3]);
        else MFMA_E(acc[ib + (q & 3)][2 * p + (q >> 2)], T[p & 1][q >> 2], afr[g % 3][ib + (q & 3)]);
        if constexpr (p < 3) DPERM((p + 1) & 1, q, sl, p + 1);
        else if constexpr (ib == 0) DPERM(0, q, sl, 0);
        else if constexpr (g + 1 < NS) DPERM(0, q, ns, 0);
      }
      if constexpr (ring(g, n) >= 0) ELOAD((ring(g, n) >> 4), (ring(g, n) & 15));
      if constexpr ((lastf || h == 0) && pwrite(g, n) >= 0) {
        constexpr int e = pwrite(g, n), i = 1 + e % 3, f = e / 3;
        dsw128a<f * 1024>(wlo[i - 1], acc[i][f]);
      }
      if constexpr (red) {
        if constexpr (n < 6) dsr128<(n % 3) * 8192 + (fb + n / 3) * 1024>(pr[n % 3][n / 3], rlo);
        if constexpr (n == 6 || n == 7) {
          static_for<3>([&](auto E) { dsr128<E.value * 8192 + (fb + n - 4) * 1024>(pr[E.value][n - 4], rlo); });
        }
        if constexpr (n >= 8) {
          constexpr int o0 = red0(n), o1 = red0(n + 1);
          static_for<o1 - o0>([&](auto O) {
            constexpr int o = o0 + O.value, fl = o / 16, k = o % 16, r = k & 3;
            (void)&uu; (void)&tt; (void)&pr;
            if constexpr (k < 4) asm volatile("v_accvgpr_read_b32 %0, %1" : "=v"(tt[fb + fl][r]) : "a"(acc[0][fb + fl][r]));
            else if constexpr (k < 8) asm volatile("v_add_f32 %0, %0, %1" : "+v"(tt[fb + fl][r]) : "v"(pr[0][fl][r]));
            else if constexpr (k < 12) asm volatile("v_add_f32 %0, %1, %2" : "=v"(uu[r]) : "v"(pr[1][fl][r]), "v"(pr[2][fl][r]));
            else asm volatile("v_add_f32 %0, %0, %1" : "+v"(tt[fb + fl][r]) : "v"(uu[r]));
          });
        }
      }
      if constexpr (h >= 0 && dstore(g, n)) {
        constexpr int f = dstore_f(g, n);
        bstore_sc1<(f & 3) * 1024>(__builtin_bit_cast(u32x4, tt[f]), voffL, rsEx, exo + (f >> 2) * 4096);
      }
      if constexpr (h == HFL && n == 25) {
        asm volatile("" : "=v"(flg));
        bload1_sc1(flg, 0u, rsFi);
      }
      if constexpr (h == HCK && n >= 24) {
        constexpr int f = n - 24;
        asm volatile("" : "=v"(pv[f]));
        bload_sc1<(f & 3) * 1024>(pv[f], voffL, rsEx, partner * 32768u + w * 8192u + (f >> 2) * 4096u);
      }
      if constexpr (h == HEP && n == 1) {
        const rsrc_t rsG = mk_rsrc(modp + (w >= 2 ? 4096 : 5120), 4 * 6144 * 4);
        const unsigned vo = (unsigned)((n0 + (tid & 127)) * 4);
#pragma unroll
        for (int r = 0; r < 4; ++r) { asm volatile("" : "=v"(gv[r])); bload1_v<0>(gv[r], vo, rsG, r * 24576); }
      }
      if constexpr (h == HEP && n == 3) {
        const rsrc_t rsX = mk_rsrc(Yt, S_LEN * D_DIM * 2);
        const unsigned vx = (unsigned)(((bm64 >> 2) * 32 + bn * 4 + fq) * 16384 + ((bm64 & 3) * 4 + w) * 1024 + fr * 16);
#pragma unroll
        for (int r = 0; r < 4; ++r) asm volatile("" : "=v"(xv[r]));
        bload_v<0>(xv[0], vx, rsX, 0); bload_v<256>(xv[1], vx, rsX, 0); bload_v<512>(xv[2], vx, rsX, 0); bload_v<768>(xv[3], vx, rsX, 0);
      }
      if constexpr (g == NS - 1 && n >= 8 && (n & 7) >= 2) {
        constexpr int e = (n & 7) - 2, i = 1 + e % 3, f = 2 * ((n >> 3) - 1) + e / 3;
        dsw128a<f * 1024>(wlo[i - 1], acc[4 + i][f]);
      }
    });
  });
  asm volatile("s_nop 15" ::: "memory");
#pragma unroll
  for (int f = 0; f < 8; ++f) asm volatile("" : "+a"(acc[4][f]));
  static_for<6>([&](auto E) { constexpr int i = 1 + E.value % 3, f = 6 + E.value / 3; dsw128a<f * 1024>(wlo[i - 1], acc[4 + i][f]); });
  asm volatile("s_waitcnt vmcnt(%8)" : "+v"(xv[0]), "+v"(xv[1]), "+v"(xv[2]), "+v"(xv[3]), "+v"(gv[0]), "+v"(gv[1]), "+v"(gv[2]), "+v"(gv[3]) : "n"(between(NF + HEP, 3, NS - 1, 32)) : "memory");
  STAMP(6, 2); STAMPFLUSH(6, 1);
  float* gtab = reinterpret_cast<float*>(smem + GT);
  {
    float sum = (__uint_as_float(gv[0]) + __uint_as_float(gv[1])) + (__uint_as_float(gv[2]) + __uint_as_float(gv[3]));
    if (w >= 2) {
      sum += 1.0f;
      if (__builtin_fabsf(sum) < 8.67361738e-19f) sum = 8.67361738e-19f;
      sum = 1.0f / sum;
    }
    const int col = tid & 127;
    gtab[(tid & 128) + (col & 96) + (col & 7) * 4 + ((col >> 3) & 3)] = sum;
  }
  asm volatile("s_waitcnt lgkmcnt(0)" ::: "memory");
  __builtin_amdgcn_s_barrier();
  u32x4 pq[3][8], gt[8], ct[8];
  f32x4 fin[8];
  {
    const unsigned gsrc = (unsigned)(uintptr_t)smem + GT + fq * 128;
    static_for<24>([&](auto E) { dsr128<E.value * 1024>(pq[E.value >> 3][E.value & 7], rlo); });
    static_for<8>([&](auto F) { dsr128<F.value * 16>(gt[F.value], gsrc); });
    static_for<8>([&](auto F) { dsr128<512 + F.value * 16>(ct[F.value], gsrc); });
#pragma unroll
    for (int f = 0; f < 8; ++f) { fin[f] = acc[4][f]; asm volatile("" : "+v"(fin[f])); }
    asm volatile("s_waitcnt lgkmcnt(0)" ::: "memory");
#pragma unroll
    for (int f = 0; f < 8; ++f) asm volatile("" : "+v"(pq[0][f]), "+v"(pq[1][f]), "+v"(pq[2][f]), "+v"(gt[f]), "+v"(ct[f]));
  }
#pragma unroll
  for (int f = 0; f < 8; ++f) {
    fin[f] = (fin[f] + __builtin_bit_cast(f32x4, pq[0][f])) + (__builtin_bit_cast(f32x4, pq[1][f]) + __builtin_bit_cast(f32x4, pq[2][f]));
    asm volatile("" : "+v"(fin[f]));
  }
  asm volatile("s_waitcnt vmcnt(0)" : "+v"(pv[0]), "+v"(pv[1]), "+v"(pv[2]), "+v"(pv[3]), "+v"(pv[4]), "+v"(pv[5]), "+v"(pv[6]), "+v"(pv[7])::"memory");
  STAMP(6, 6);
#pragma unroll
  for (int f = 0; f < 8; ++f) {
    fin[f] += __builtin_bit_cast(f32x4, pv[f]);
    f32x4 xf;
#pragma unroll
    for (int r = 0; r < 4; ++r) xf[r] = (f & 1) ? bf_hi(xv[r][f >> 1]) : bf_lo(xv[r][f >> 1]);
    xf *= __builtin_bit_cast(f32x4, ct[f]);
    fin[f] = xf + __builtin_bit_cast(f32x4, gt[f]) * fin[f];
  }
  {
    char* tb = smem + w * 24576;
#pragma unroll
    for (int r = 0; r < 4; ++r) {
      const u32x4 ov = {pk_bf16(fin[0][r], fin[1][r]), pk_bf16(fin[2][r], fin[3][r]), pk_bf16(fin[4][r], fin[5][r]), pk_bf16(fin[6][r], fin[7][r])};
      *reinterpret_cast<u32x4*>(tb + fr * 256 + (((fq * 4 + r) ^ (fr & 7)) * 16)) = ov;
    }
#pragma unroll
    for (int j = 0; j < 4; ++j) {
      const int row = j * 4 + fq;
      const u32x4 ov = *reinterpret_cast<const u32x4*>(tb + row * 256 + ((fr ^ (row & 7)) * 16));
      gst128(C + (size_t)(bm64 * 64 + w * 16 + row) * D_DIM + n0 + fr * 8, ov);
    }
  }
  STAMP(6, 3);
  STAMP(6, 4);
}

extern "C" void kernel_launch(void* const* d_in, const int* in_sizes, int n_in, void* d_out, int out_size,
                              void* d_ws, size_t ws_size, hipStream_t stream) {
  const bf16* x = (const bf16*)d_in[0];
  const bf16* wqkv = (const bf16*)d_in[1];
  const bf16* wo = (const bf16*)d_in[2];
  const bf16* wg = (const bf16*)d_in[4];
  const bf16* wu = (const bf16*)d_in[5];
  const bf16* wd = (const bf16*)d_in[6];
  const bf16* c = (const bf16*)d_in[7];
  const bf16* wmod = (const bf16*)d_in[8];
  bf16* out = (bf16*)d_out;

  char* ws = (char*)d_ws;
  float* modp = (float*)ws;
  float* rstat1 = (float*)(ws + 114688);
  float* stat = (float*)(ws + 524288);
  bf16* ot = (bf16*)(ws + (1 << 20));
  bf16* yt = (bf16*)(ws + (9 << 20));
  bf16* ht = (bf16*)(ws + (13 << 20));
  bf16* act = (bf16*)(ws + (17 << 20));
  char* wc = ws + (5 << 20);
  float* xbuf = (float*)(ws + (32 << 20));
  unsigned* flags = (unsigned*)(ws + (7 << 20));

  pre_kernel<<<320, 512, 0, stream>>>(x, c, wmod, modp, rstat1, flags);
  ln_apply_kernel<<<256, 320, 0, stream>>>(x, modp, rstat1, ht, wqkv, wo, wc, ws + 262144);
  qkv_attn_kernel<<<256, 512, 0, stream>>>(ht, wqkv, ot, wg, wu, wo, x, yt, stat, flags + 1024, wc, act, wd, ws + 262144);
  gemm4_kernel<<<256, 256, 0, stream>>>(act, wd, yt, modp, out, xbuf, flags);
}
```

```cpp
#include <hip/hip_runtime.h>
#include <hip/hip_bf16.h>
#include <stdint.h>

typedef __hip_bfloat16 bf16;
typedef __attribute__((ext_vector_type(8))) short bf16x8;
typedef __attribute__((ext_vector_type(4))) float f32x4;
typedef __attribute__((ext_vector_type(2))) float f32x2;
typedef __attribute__((ext_vector_type(4))) unsigned u32x4;
typedef __attribute__((ext_vector_type(2))) unsigned u32x2;
typedef __attribute__((ext_vector_type(2))) __bf16 bf16x2_t;
typedef __attribute__((ext_vector_type(8))) unsigned long long u64x8;
typedef __amdgpu_buffer_rsrc_t rsrc_t;

#define DI __device__ __forceinline__
#define QBAR 2
#define QRN 17
#define TQ 28
#define DWO 6
#define DW0 8
#define DWS 5
#define OFS 4
#define ORP 1
#define YFS 32
#define YSL 6
#define CODEPF 60

#define STAMP(k, s) do { } while (0)
#define STAMPT(k, s, T) do { } while (0)
#define STAMPNW(k, s)
#define STAMPFLUSH(k, s)
#define STAMPDECL

constexpr int S_LEN = 2048;
constexpr int D_DIM = 1024;
constexpr int F_DIM = 3584;

template <int V> struct ic { static constexpr int value = V; };
template <int N, int I = 0, class F>
DI void static_for(F&& f) {
  if constexpr (I < N) { f(ic<I>{}); static_for<N, I + 1>(f); }
}

DI float bf_lo(unsigned u) { return __uint_as_float(u << 16); }
DI float bf_hi(unsigned u) { return __uint_as_float(u & 0xffff0000u); }
DI unsigned pk_bf16(float lo, float hi) {
  const f32x2 v = {lo, hi};
  return __builtin_bit_cast(unsigned, __builtin_convertvector(v, bf16x2_t));
}
DI rsrc_t mk_rsrc(const void* p, unsigned bytes) { return __builtin_amdgcn_make_buffer_rsrc((void*)p, (short)0, (int)bytes, 0x00020000); }

template <int N>
DI void wait_vm() { asm volatile("s_waitcnt vmcnt(%0)" ::"n"(N > 63 ? 63 : N) : "memory"); }
#define WAIT_LGKM0() do { asm volatile("s_waitcnt lgkmcnt(0)" ::: "memory"); __builtin_amdgcn_sched_barrier(0); } while (0)
#define SCHED0() __builtin_amdgcn_sched_barrier(0)

#define MFMA_A(C, B, A) asm volatile("v_mfma_f32_16x16x32_bf16 %0, %1, %2, %0" : "+a"(C) : "v"(B), "a"(A))
#define MFMA_AV(C, B, A) asm volatile("v_mfma_f32_16x16x32_bf16 %0, %1, %2, %0" : "+v"(C) : "v"(B), "v"(A))
#define PERM_LO(D, X, Y) asm volatile("v_perm_b32 %0, %1, %2, %3" : "=v"(D) : "v"(Y), "v"(X), "s"(0x05040100u))
#define PERM_HI(D, X, Y) asm volatile("v_perm_b32 %0, %1, %2, %3" : "=v"(D) : "v"(Y), "v"(X), "s"(0x07060302u))
template <int OFF>
DI void bload_v(u32x4& d, unsigned voff, rsrc_t rs, unsigned soff) {
  asm volatile("buffer_load_dwordx4 %0, %1, %2, %3 offen offset:%4" : "+v"(d) : "v"(voff), "s"(rs), "s"(soff), "i"(OFF) : "memory");
}
template <int OFF>
DI void bload_a(u32x4& d, unsigned voff, rsrc_t rs, unsigned soff) {
  asm volatile("buffer_load_dwordx4 %0, %1, %2, %3 offen offset:%4" : "+a"(d) : "v"(voff), "s"(rs), "s"(soff), "i"(OFF) : "memory");
}
template <int OFF>
DI void bload2_v(u32x2& d, unsigned voff, rsrc_t rs, unsigned soff) {
  asm volatile("buffer_load_dwordx2 %0, %1, %2, %3 offen offset:%4" : "+v"(d) : "v"(voff), "s"(rs), "s"(soff), "i"(OFF) : "memory");
}
template <int OFF>
DI void bload1_v(unsigned& d, unsigned voff, rsrc_t rs, unsigned soff) {
  asm volatile("buffer_load_dword %0, %1, %2, %3 offen offset:%4" : "+v"(d) : "v"(voff), "s"(rs), "s"(soff), "i"(OFF) : "memory");
}
template <int OFF>
DI void bload_lds(unsigned m0v, unsigned voff, rsrc_t rs, unsigned soff) {
  asm volatile("s_mov_b32 m0, %0\n\ts_nop 0\n\tbuffer_load_dwordx4 %1, %2, %3 offen offset:%4 lds" ::"s"(m0v), "v"(voff), "s"(rs), "s"(soff), "i"(OFF) : "memory");
}
DI void touch8k(unsigned m0v, unsigned voff, rsrc_t rs, unsigned soff) {
  asm volatile("s_mov_b32 m0, %0\n\ts_nop 0\n\tbuffer_load_dword %1, %2, %3 offen lds" ::"s"(m0v), "v"(voff), "s"(rs), "s"(soff) : "memory");
}
template <int OFF>
DI void bload_lds_sc1(unsigned m0v, unsigned voff, rsrc_t rs, unsigned soff) {
  asm volatile("s_mov_b32 m0, %0\n\ts_nop 0\n\tbuffer_load_dwordx4 %1, %2, %3 offen offset:%4 sc1 lds" ::"s"(m0v), "v"(voff), "s"(rs), "s"(soff), "i"(OFF) : "memory");
}
template <int OFF>
DI void dsr128(u32x4& d, unsigned addr) { asm volatile("ds_read_b128 %0, %1 offset:%2" : "=v"(d) : "v"(addr), "i"(OFF) : "memory"); }
template <int OFF>
DI void dsr64(u32x2& d, unsigned addr) { asm volatile("ds_read_b64 %0, %1 offset:%2" : "=v"(d) : "v"(addr), "i"(OFF) : "memory"); }
template <int OFF>
DI void dsr_tr(u32x2& d, unsigned addr) { asm volatile("ds_read_b64_tr_b16 %0, %1 offset:%2" : "=v"(d) : "v"(addr), "i"(OFF) : "memory"); }
template <int OFF>
DI void dsw128(unsigned addr, u32x4 d) { asm volatile("ds_write_b128 %0, %1 offset:%2" ::"v"(addr), "v"(d), "i"(OFF) : "memory"); }
template <int OFF>
DI void dsw128a(unsigned addr, const f32x4& d) { asm volatile("ds_write_b128 %0, %1 offset:%2" ::"v"(addr), "a"(d), "i"(OFF) : "memory"); }
template <int OFF>
DI void dsw32(unsigned addr, unsigned d) { asm volatile("ds_write_b32 %0, %1 offset:%2" ::"v"(addr), "v"(d), "i"(OFF) : "memory"); }

DI void gst128(void* p, u32x4 v) {
  asm volatile("global_store_dwordx4 %0, %1, off sc1\n\ts_nop 1" ::"v"(p), "v"(v) : "memory");
}
DI f32x2 lo2(f32x4 v) { return __builtin_shufflevector(v, v, 0, 1); }
DI f32x2 hi2(f32x4 v) { return __builtin_shufflevector(v, v, 2, 3); }
DI float rowsum4(float v) {
  const unsigned u = __float_as_uint(v);
  const auto a = __builtin_amdgcn_permlane16_swap(u, u, false, false);
  const unsigned t = __float_as_uint(__uint_as_float(a[0]) + __uint_as_float(a[1]));
  const auto b = __builtin_amdgcn_permlane32_swap(t, t, false, false);
  return __uint_as_float(b[0]) + __uint_as_float(b[1]);
}
DI void split4(const f32x4 v, u32x2& hi, u32x2& lo) {
  hi[0] = pk_bf16(v[0], v[1]); hi[1] = pk_bf16(v[2], v[3]);
  lo[0] = pk_bf16(v[0] - bf_lo(hi[0]), v[1] - bf_hi(hi[0]));
  lo[1] = pk_bf16(v[2] - bf_lo(hi[1]), v[3] - bf_hi(hi[1]));
}
DI int slow_col(const void* p) { return (int)((3u - ((unsigned)(uintptr_t)p >> 7)) & 7u); }
DI float silu_f(float v) { return v / (1.0f + __expf(-v)); }

constexpr unsigned WS_G1M = 0x20000;
constexpr unsigned WS_G1S = 0x40000;
constexpr unsigned WS_G2 = 0x60000;
constexpr unsigned WS_STAT = 0x80000;
constexpr unsigned WS_TC = 0xC0000;
constexpr unsigned WS_TV = 0x100000;
constexpr unsigned WS_F4 = 0x140000;
constexpr unsigned WS_FO = 0x141000;
constexpr unsigned WS_FC = 0x146000;
constexpr unsigned WS_FY = 0x148000;
constexpr unsigned WS_FH = 0x188000;
constexpr unsigned WS_WC = 2u << 20;
constexpr unsigned WS_H = 4u << 20, WS_O = 8u << 20, WS_Y = 12u << 20, WS_ACT = 16u << 20, WS_X = 32u << 20;

template <int CTRL>
DI float dpp_get(float b) { return __uint_as_float(__builtin_amdgcn_update_dpp(0u, __float_as_uint(b), CTRL, 0xf, 0xf, true)); }
DI float swap32_add(float a, float b) {
  const auto sw = __builtin_amdgcn_permlane32_swap(__float_as_uint(a), __float_as_uint(b), false, false);
  return __uint_as_float(sw[0]) + __uint_as_float(sw[1]);
}
DI float swap16_add(float a, float b) {
  const auto sw = __builtin_amdgcn_permlane16_swap(__float_as_uint(a), __float_as_uint(b), false, false);
  return __uint_as_float(sw[0]) + __uint_as_float(sw[1]);
}
DI float scatter8(const f32x2 (&a)[4], int lane) {
  const float t0 = swap32_add(a[0][0], a[2][0]), t1 = swap32_add(a[0][1], a[2][1]), t2 = swap32_add(a[1][0], a[3][0]), t3 = swap32_add(a[1][1], a[3][1]);
  const float u0 = swap16_add(t0, t2), u1 = swap16_add(t1, t3);
  const bool b3 = lane & 8;
  return (b3 ? u1 : u0) + dpp_get<0x128>(b3 ? u0 : u1);
}
DI float sum4(const f32x2 (&a)[4]) {
  float t = swap16_add(swap32_add(a[0][0] + a[0][1], a[2][0] + a[2][1]), swap32_add(a[1][0] + a[1][1], a[3][0] + a[3][1]));
  t += dpp_get<0x128>(t); t += dpp_get<0x124>(t); t += dpp_get<0x122>(t); t += dpp_get<0x121>(t);
  return t;
}
DI f32x2 unpk(unsigned u) { return f32x2{bf_lo(u), bf_hi(u)}; }
DI f32x2 unpk_v(unsigned u) {
  f32x2 r;
  asm volatile("v_lshlrev_b32 %0, 16, %1" : "=v"(r[0]) : "v"(u));
  asm volatile("v_and_b32 %0, 0xffff0000, %1" : "=v"(r[1]) : "v"(u));
  return r;
}

constexpr int Q_SLOT = 21504, Q_NSL = 7;
constexpr int WO_H0 = 4 * Q_SLOT;
constexpr int QX_OFF = Q_SLOT * Q_NSL + 1024;
constexpr int QH_OFF = QX_OFF + 8192;
constexpr int QT_OFF = QH_OFF + 1024;
constexpr int Q_LDS = QT_OFF + 1024;
#define C3Q 1
#define C3O 1
#define OMAP 2
#define POLLW 0
#define OD 3
#define OMB 6
template <int OFF>
DI void bload_sc1(u32x4& d, unsigned voff, rsrc_t rs, unsigned soff) {
  asm volatile("buffer_load_dwordx4 %0, %1, %2, %3 offen offset:%4 sc1" : "+v"(d) : "v"(voff), "s"(rs), "s"(soff), "i"(OFF) : "memory");
}
DI void bload2_sc1(u32x2& d, unsigned voff, rsrc_t rs, unsigned soff) {
  asm volatile("buffer_load_dwordx2 %0, %1, %2, %3 offen sc1" : "+v"(d) : "v"(voff), "s"(rs), "s"(soff) : "memory");
}
DI void bload1_sc1(unsigned& d, unsigned voff, rsrc_t rs) {
  asm volatile("buffer_load_dword %0, %1, %2, 0 offen sc1" : "+v"(d) : "v"(voff), "s"(rs) : "memory");
}
DI unsigned wo_voff(int lane, int odd, int sh) {
  const int q = (lane >> 3) & 3, h = lane >> 5, ch = lane & 7, g = (q >> 1) | (odd << 1);
  return (unsigned)(((h * 16 + q) << sh) + ((((ch >> 1) ^ g) * 16) + (ch & 1) * 8) * 2);
}
DI rsrc_t wo_rsrc(const bf16* wo, const char* wc, int cn) {
  const bool c3 = C3O && (cn & 7) == slow_col(wo);
  return mk_rsrc(c3 ? wc + 786432 + (cn >> 3) * 131072 : (const char*)wo + cn * 128, c3 ? 131072 : D_DIM * D_DIM * 2 - cn * 128);
}

struct Args { const bf16 *x, *c, *wmod, *wq, *wo, *wg, *wu; char* ws; };
#define CK 3
#define OCU -1
#define TQP -1
#define TW 8
#define TO 4
#define TT 2
#define PSL 8
#define PSI 4
DI void gstore2_sc1(u32x2 d, unsigned voff, rsrc_t rs) {
  asm volatile("buffer_store_dwordx2 %0, %1, %2, 0 offen sc1" ::"v"(d), "v"(voff), "s"(rs) : "memory");
}
DI void gstore4_sc1(u32x4 d, unsigned voff, rsrc_t rs, unsigned soff) {
  asm volatile("buffer_store_dwordx4 %0, %1, %2, %3 offen sc1\n\ts_nop 1" ::"v"(d), "v"(voff), "s"(rs), "s"(soff) : "memory");
}
DI void bload_us(unsigned& d, unsigned voff, rsrc_t rs, unsigned soff) {
  asm volatile("buffer_load_ushort %0, %1, %2, %3 offen" : "+v"(d) : "v"(voff), "s"(rs), "s"(soff) : "memory");
}
template <int W0, int W1>
DI void gemv8(float* red, int lane, const u32x4 (&d)[8], unsigned cv) {
  wait_vm<W0>();
  const unsigned sv = __float_as_uint(silu_f(unpk_v(cv)[0]));
  f32x2 a[4] = {{0, 0}, {0, 0}, {0, 0}, {0, 0}};
  static_for<8>([&](auto J) {
    constexpr int j = J.value;
    if constexpr (j == 4 && W1 != W0) wait_vm<W1>();
    const float s = __uint_as_float(__builtin_amdgcn_update_dpp(__builtin_amdgcn_update_dpp(0u, sv, 0x150 + j, 0xf, 0x3, false), sv, 0x158 + j, 0xf, 0xc, false));
#pragma unroll
    for (int e = 0; e < 4; ++e) a[e] += s * unpk_v(d[j][e]);
  });
  red[(lane & 7) * 8 + (lane >> 3)] = scatter8(a, lane);
}
struct Pre { unsigned o1, n1, vo, st; };
DI unsigned front1(char* smem, unsigned smem_base, int lane, int w4, int bm, int bn, const Args& g, const Pre& p) {
  const bool gv = ((bm + bn) & 1) == 0;
  u32x4 d[8];
  unsigned tag, cv;
  asm volatile("" : "=v"(d[0]), "=v"(d[1]), "=v"(d[2]), "=v"(d[3]), "=v"(d[4]), "=v"(d[5]), "=v"(d[6]), "=v"(d[7]), "=v"(tag), "=v"(cv));
  {
    const rsrc_t rs = mk_rsrc((const char*)(gv ? g.wmod : g.x) + p.o1, p.n1), rsC = mk_rsrc(g.c, 2048);
#pragma unroll
    for (int i = 0; i < 4; ++i) bload_v<0>(d[i], p.vo, rs, i * p.st);
    bload_us(cv, lane * 2, rsC, (bm >> 2) * 512 + w4 * 128);
    bload1_v<0>(tag, 0u, mk_rsrc(g.ws, 4), 0);
#pragma unroll
    for (int i = 4; i < 8; ++i) bload_v<0>(d[i], p.vo, rs, i * p.st);
  }
  STAMPT(0, 6, 192);
  __builtin_amdgcn_s_barrier();
  float* red = reinterpret_cast<float*>(smem + QX_OFF);
  if (gv) {
    gemv8<4, 0>(red + w4 * 64, lane, d, cv);
  } else {
    f32x2 v[4][8], t[4];
    asm volatile("s_waitcnt vmcnt(4)" ::"v"(cv) : "memory");
#pragma unroll
    for (int r = 0; r < 4; ++r) {
      if (r == 2) wait_vm<0>();
#pragma unroll
      for (int e = 0; e < 8; ++e) v[r][e] = unpk_v(d[r * 2 + (e >> 2)][e & 3]);
      t[r] = ((v[r][0] + v[r][1]) + (v[r][2] + v[r][3])) + ((v[r][4] + v[r][5]) + (v[r][6] + v[r][7]));
    }
    const float mean = sum4(t) * (1.0f / D_DIM);
#pragma unroll
    for (int r = 0; r < 4; ++r) {
      const float m = __uint_as_float(__builtin_amdgcn_readlane(__float_as_uint(mean), r * 16));
      f32x2 q0 = {0, 0}, q1 = {0, 0};
#pragma unroll
      for (int e = 0; e < 8; e += 2) { const f32x2 e0 = v[r][e] - m, e1 = v[r][e + 1] - m; q0 += e0 * e0; q1 += e1 * e1; }
      t[r] = q0 + q1;
    }
    const float rstd = rsqrtf(sum4(t) * (1.0f / D_DIM) + 1e-5f);
    if ((lane & 15) == 0) *reinterpret_cast<f32x2*>(red + w4 * 8 + (lane >> 3)) = f32x2{rstd, -mean * rstd};
  }
  asm volatile("ds_add_u32 %1, %2" : "+v"(tag) : "v"(smem_base + QT_OFF), "v"(1u) : "memory");
  STAMPT(0, 1, 192);
  tag = __builtin_amdgcn_readfirstlane(tag) + 0x9E3779B9u;
  const unsigned o = gv ? WS_G1M + (w4 >> 1) * 65536 + ((bm & 2) * 2 + (bm >> 2)) * 8192 + (bn ^ 1) * 512 + (w4 & 1) * 256 : WS_G1S + w4 * 32768 + (bm * 128 + (bn >> 1) * 16) * 16;
  const rsrc_t rsG = mk_rsrc(g.ws + o, 256);
  const f32x2* rp = reinterpret_cast<const f32x2*>(red) + (lane & 15) + (gv ? (w4 & 1) * 16 : 0);
  unsigned n;
  do {
    asm volatile("ds_read_b32 %0, %1\n\ts_waitcnt lgkmcnt(0)" : "=v"(n) : "v"(smem_base + QT_OFF) : "memory");
  } while (__builtin_amdgcn_readfirstlane(n) != 256u);
  f32x2 m = rp[0];
  const f32x2 m2 = (m + rp[32]) + (rp[64] + rp[96]);
  m = gv ? m2 : m;
  gstore4_sc1(u32x4{__float_as_uint(m[0]), tag, __float_as_uint(m[1]), tag}, lane * 16, rsG, 0);
  STAMPT(0, 2, 192);
  return tag;
}
DI void front_ocld(int lane, int w4, int bm, int bn, const Args& g, u32x4 (&oc)[8], unsigned& c2) {
  const rsrc_t rs = mk_rsrc((const char*)g.wmod + ((bm & 3) * 4 + w4) * 786432 + 4096 + (bm >> 2) * 2048 + (bn ^ 2) * 128, 63 * 12288 + 128);
  asm volatile("" : "=v"(c2));
  bload_us(c2, lane * 2, mk_rsrc(g.c, 2048), (bm & 3) * 512 + w4 * 128);
#pragma unroll
  for (int i = 0; i < 8; ++i) {
    wait_vm<TO - 1>();
    asm volatile("" : "=v"(oc[i]));
    bload_v<0>(oc[i], (lane >> 3) * (8 * 12288) + (lane & 7) * 16, rs, i * 12288);
  }
}
template <int NW>
DI void front_oc(char* smem, int lane, int w4, const u32x4 (&oc)[8], unsigned c2) {
  gemv8<NW, NW>(reinterpret_cast<float*>(smem + QX_OFF + 1024) + w4 * 64, lane, oc, c2);
  asm volatile("s_waitcnt lgkmcnt(0)" ::: "memory");
}

DI void front_h(char* smem, unsigned smem_base, int lane, int wid, int bm, int bn, const Args& g, unsigned tag) {
  const int fr = lane & 15, fq = lane >> 4;
  const int kA = ((bn ^ 3) * 2) ^ (wid & 1), kB = ((bn ^ 3) * 2) ^ (2 + wid), f0 = (wid >> 1) * 4;
  u32x4 xa[5], xb[9];
  const unsigned hc = bm == 0 ? 1u : 0u;
  const unsigned la = smem_base + (wid & 1) * Q_SLOT + f0 * 1024 + lane * 16, lb = smem_base + (2 + wid) * Q_SLOT + lane * 16;
  {
    unsigned n;
    do {
      asm volatile("ds_read_b32 %0, %1 offset:4\n\ts_waitcnt lgkmcnt(0)" : "=v"(n) : "v"(smem_base + QT_OFF) : "memory");
    } while (__builtin_amdgcn_readfirstlane(n) != 192u);
  }
  const unsigned lx = fr * 64 + (fq ^ ((fr >> 1) & 2)) * 16 - lane * 16;
  static_for<5>([&](auto I) { dsr128<I.value * 1024>(xa[I.value], la + lx); });
  static_for<9>([&](auto I) { dsr128<I.value * 1024>(xb[I.value], lb + lx); });
  f32x2 fa[5][4], fb[9][4];
  asm volatile("s_waitcnt lgkmcnt(9)" ::: "memory");
#pragma unroll
  for (int i = 0; i < 5; ++i)
#pragma unroll
    for (int e = 0; e < 4; ++e) fa[i][e] = unpk_v(xa[i][e]);
  asm volatile("s_waitcnt lgkmcnt(0)" ::: "memory");
#pragma unroll
  for (int i = 0; i < 9; ++i)
#pragma unroll
    for (int e = 0; e < 4; ++e) fb[i][e] = unpk_v(xb[i][e]);
  STAMP(0, 4);
  u32x4 pm[4], ps[3];
  {
    const rsrc_t rsM = mk_rsrc(g.ws + WS_G1M + (bm >> 3) * 65536, 65536), rsS = mk_rsrc(g.ws + WS_G1S + (bn >> 2) * 32768, 32768);
    const unsigned vm = ((fr >> 2) & 1) * 32768 + (fr >> 3) * 8192 + fq * 64 + (fr & 3) * 16;
    const unsigned vs0 = (bm * 128 - 16 + lane + (lane < 16 ? hc * 16 : 0)) * 16, vs1 = (bm * 128 + 48 + lane) * 16, vs2 = (bm * 128 + 112 + fr) * 16;
    if (PSL) __builtin_amdgcn_s_sleep(PSL);
    unsigned spins = 0;
    for (;;) {
      asm volatile("buffer_load_dwordx4 %4, %8, %12, 0 offen sc1\n\tbuffer_load_dwordx4 %5, %9, %12, 0 offen sc1\n\tbuffer_load_dwordx4 %6, %10, %12, 0 offen sc1\n\t"
                   "buffer_load_dwordx4 %0, %7, %11, %13 offen sc1\n\tbuffer_load_dwordx4 %1, %7, %11, %14 offen sc1\n\t"
                   "buffer_load_dwordx4 %2, %7, %11, %15 offen sc1\n\tbuffer_load_dwordx4 %3, %7, %11, %16 offen sc1\n\t"
                   "s_waitcnt vmcnt(0)"
                   : "=&v"(pm[0]), "=&v"(pm[1]), "=&v"(pm[2]), "=&v"(pm[3]), "=&v"(ps[0]), "=&v"(ps[1]), "=&v"(ps[2])
                   : "v"(vm), "v"(vs0), "v"(vs1), "v"(vs2), "s"(rsM), "s"(rsS), "s"(kA * 256), "s"(kA * 256 + 16384), "s"(kB * 256), "s"(kB * 256 + 16384) : "memory");
      unsigned bad = 0;
#pragma unroll
      for (int i = 0; i < 4; ++i) bad |= (pm[i][1] ^ tag) | (pm[i][3] ^ tag);
#pragma unroll
      for (int i = 0; i < 3; ++i) bad |= (ps[i][1] ^ tag) | (ps[i][3] ^ tag);
      asm volatile("" : "+v"(bad));
      if (__all(bad == 0u) || ++spins > (1u << 18)) break;
      if (PSI) __builtin_amdgcn_s_sleep(PSI);
    }
  }
  STAMP(0, 3);
  f32x2 rs[9], ra[5];
  {
    u32x2* sp = reinterpret_cast<u32x2*>(smem + QX_OFF + 2048 + wid * 1536);
#pragma unroll
    for (int i = 0; i < 3; ++i) sp[i * 64 + lane] = u32x2{ps[i][0], ps[i][2]};
    __builtin_amdgcn_wave_barrier();
#pragma unroll
    for (int i = 0; i < 5; ++i) ra[i] = __builtin_bit_cast(f32x2, sp[(f0 + i) * 16 + fr]);
#pragma unroll
    for (int i = 0; i < 9; ++i) rs[i] = __builtin_bit_cast(f32x2, sp[i * 16 + fr]);
  }
  auto cols = [&](const u32x4& p0, const u32x4& p1, f32x2 (&sa)[4], f32x2 (&ca)[4]) {
    float m0 = __uint_as_float(p0[0]) + __uint_as_float(p1[0]), m1 = __uint_as_float(p0[2]) + __uint_as_float(p1[2]);
    const float one = (fr & 4) ? 1.0f : 0.0f;
    m0 = (m0 + dpp_get<0x128>(m0)) + one; m1 = (m1 + dpp_get<0x128>(m1)) + one;
    static_for<4>([&](auto E) {
      constexpr int e = E.value;
      sa[e] = f32x2{dpp_get<0x150 + e>(m0), dpp_get<0x150 + e>(m1)};
      ca[e] = f32x2{dpp_get<0x154 + e>(m0), dpp_get<0x154 + e>(m1)};
    });
  };
  auto frag = [&](const f32x2 (&xf)[4], const f32x2 r, const f32x2 (&sa)[4], const f32x2 (&ca)[4]) {
    u32x4 ho;
#pragma unroll
    for (int e = 0; e < 4; ++e) { const f32x2 h = (xf[e] * r[0] + r[1]) * ca[e] + sa[e]; ho[e] = pk_bf16(h[0], h[1]); }
    return ho;
  };
  f32x2 sa[4], ca[4];
  cols(pm[0], pm[1], sa, ca);
  const rsrc_t rsH = mk_rsrc(g.ws + WS_H + ((bm * 8 + f0) * 32 + kA) * 1024, 3 * 32768 + 1024);
  static_for<5>([&](auto I) {
    constexpr int i = (I.value + 1) % 5;
    const u32x4 ho = frag(fa[i], ra[i], sa, ca);
    if constexpr (i > 0) gstore4_sc1(ho, lane * 16, rsH, (i - 1) * 32768);
    dsw128<i * 1024>(i == 0 && f0 ? smem_base + QH_OFF + lane * 16 : la, ho);
  });
  cols(pm[2], pm[3], sa, ca);
  static_for<9>([&](auto I) { dsw128<I.value * 1024>(lb, frag(fb[I.value], rs[I.value], sa, ca)); });
  STAMP(0, 5);
  wait_vm<0>();
  asm volatile("buffer_store_dword %0, %1, %2, 0 offen sc1" ::"v"(tag), "v"(lane * 32768), "s"(mk_rsrc(g.ws + WS_FH + (bm * 16 + bn) * 128 + wid * 4, 7 * 32768 + 4)) : "memory");
  STAMP(1, 1);
}

namespace ql {
constexpr int ops(int u) { return (u < 0 ? 33 : (u <= CK ? 4 : (u == CK + 1 ? 3 * (CK + 1) + 7 : 7))) + (u == TQP); }
constexpr int issued(int u) { int c = 0; for (int t = -1; t <= u; ++t) c += ops(t); return c; }
constexpr int done(int v) { return v < 6 ? 4 * (v + 1) : (v <= CK + 6 ? issued(CK) + 3 * (v - 5) : issued(v - 7) + 7 + (v == CK + 7 ? 3 * (CK + 1) : 0)); }
constexpr int NP = CK + 6;
}
DI void qkv_loader(char* smem, unsigned smem_base, int lane, int sw, int bm, int bn, int cn, const Args& g) {
  using namespace ql;
  const int m0 = bm * 128, n0 = bn * 64;
  const unsigned u0 = (bn ^ 3) * 2;
  const char* wc = g.ws + WS_WC;
  unsigned m0a = smem_base + sw * 3072, m0w = smem_base + 9216 + sw * 4096;
  auto wrow = [&](int i, unsigned sec) {
    const int r = i * 8 + (lane >> 3), ch = lane & 7;
    const int gg = ((r >> 1) & 1) | (((r >> 3) & 1) << 1);
    return (unsigned)((r * 3 + sw) * sec + ((((ch >> 1) ^ gg) * 16) + (ch & 1) * 8) * 2);
  };
  int fb0 = (m0 >> 4) - 1 + 3 * sw; fb0 = fb0 < 0 ? 0 : fb0;
  {
    const rsrc_t rsX = mk_rsrc(g.x, S_LEN * D_DIM * 2);
    const unsigned vx = (lane >> 2) * 2048 + ((lane & 3) ^ ((lane >> 3) & 2)) * 16;
#pragma unroll
    for (int i = 0; i < 18; ++i) {
      const int u = (i / 6) * 2 + (i & 1), r = (i >> 1) % 3;
      if (i >= TW) wait_vm<TW - 1>();
      bload_lds<0>(m0a + u * Q_SLOT + r * 1024, vx, rsX, (r ? (m0 >> 4) - 1 + 3 * sw + r : fb0) * 32768 + (u ^ u0) * 64);
    }
    const rsrc_t rsW0 = mk_rsrc((const char*)g.wq + n0 * 2, D_DIM * 3 * D_DIM * 2 - n0 * 2);
    const unsigned voffW0[2] = {wrow(0, 2048), wrow(1, 2048)};
#pragma unroll
    for (int u = 0; u < Q_NSL - 1; ++u)
#pragma unroll
      for (int i = 0; i < 4; ++i) {
        wait_vm<TW - 1>();
        if (u * 4 + i == TW - 1) asm volatile("ds_add_u32 %0, %1 offset:4" ::"v"(smem_base + QT_OFF), "v"(1u) : "memory");
        bload_lds<0>(m0w + u * Q_SLOT + i * 1024, voffW0[i & 1], rsW0, ((u ^ u0) * 2u + (i >> 1)) * 98304u);
      }
  }
  u32x4 oc[8];
  unsigned c2;
  front_ocld(lane, sw, bm, bn, g, oc, c2);
  const rsrc_t rsT = mk_rsrc((const char*)g.wq + (blockIdx.x * 3 + sw) * 8192, 8192);
  if constexpr (TQP < 0) { wait_vm<TT>(); touch8k(smem_base + QH_OFF, lane * 128u, rsT, 0); }
  const rsrc_t rsH = mk_rsrc(g.ws + WS_H, S_LEN * D_DIM * 2);
  const bool c3 = C3Q && (bn & 7) == slow_col(g.wq);
  const rsrc_t rsW = mk_rsrc(c3 ? wc + (n0 >> 9) * 393216 : (const char*)g.wq + n0 * 2, c3 ? 393216 : D_DIM * 3 * D_DIM * 2 - n0 * 2);
  const unsigned sec = c3 ? 128u : 2048u, p16 = sec * 48u;
  unsigned voffH0 = (unsigned)(fb0 * 32768 + lane * 16);
  unsigned voffH1 = (unsigned)(((m0 >> 4) + 3 * sw) * 32768 + lane * 16);
  unsigned voffW[2] = {wrow(0, sec), wrow(1, sec)};
  asm volatile("" : "+v"(voffH0), "+v"(voffH1), "+v"(voffW[0]), "+v"(voffW[1]));
  asm volatile("" : "+s"(m0a), "+s"(m0w));
#define Q_LOADH(SOFF, U)                                                                                         \
  do {                                                                                                           \
    const unsigned so_ = ((unsigned)(U) ^ u0) * 1024u;                                                           \
    bload_lds_sc1<0>(m0a + (SOFF), voffH0, rsH, so_);                                                            \
    bload_lds_sc1<0>(m0a + (SOFF) + 1024, voffH1, rsH, so_);                                                     \
    bload_lds_sc1<0>(m0a + (SOFF) + 2048, voffH1, rsH, so_ + 32768u);                                            \
  } while (0)
#define Q_LOADW(SOFF, U)                                                                                         \
  do {                                                                                                           \
    _Pragma("unroll") for (int i = 0; i < 4; ++i)                                                                \
      bload_lds_sc1<0>(m0w + (SOFF) + i * 1024, voffW[i & 1], rsW, (((unsigned)(U) ^ u0) * 2u + (i >> 1)) * p16); \
  } while (0)
  if constexpr (OCU < 0) front_oc<issued(-1) - 33>(smem, lane, sw, oc, c2);
  else wait_vm<issued(-1) - done(0)>();
  __builtin_amdgcn_s_barrier();
  static_for<NP>([&](auto U) {
    constexpr int u = U.value;
    (void)&rsT;
    if constexpr (u == CK + 1) {
#pragma unroll
      for (int v = 6; v <= CK + 6; ++v) Q_LOADH((v % Q_NSL) * Q_SLOT, v);
    }
    if constexpr (u > CK) Q_LOADH(((u + 6) % Q_NSL) * Q_SLOT, u + 6);
    Q_LOADW(((u + 6) % Q_NSL) * Q_SLOT, u + 6);
    if constexpr (u == TQP) touch8k(smem_base + QH_OFF, lane * 128u, rsT, 0);
    if constexpr (u == OCU) front_oc<issued(u) - 33>(smem, lane, sw, oc, c2);
    wait_vm<issued(u) - done(u + 1)>();
    __builtin_amdgcn_s_barrier();
  });
  unsigned sb = ((NP + 6) % Q_NSL) * Q_SLOT;
  for (int u = NP; u < 33 - Q_NSL; ++u) {
    Q_LOADH(sb, u + Q_NSL - 1);
    Q_LOADW(sb, u + Q_NSL - 1);
    sb = (sb == (Q_NSL - 1) * Q_SLOT) ? 0u : sb + Q_SLOT;
    wait_vm<(Q_NSL - 2) * 7>();
    __builtin_amdgcn_s_barrier();
  }
  const rsrc_t rsO = wo_rsrc(g.wo, wc, cn);
  const int sh = (C3O && (cn & 7) == slow_col(g.wo)) ? 7 : 11;
  const unsigned voffO[2] = {wo_voff(lane, sw & 1, sh), wo_voff(lane, (sw & 1) ^ 1, sh)};
  static_for<Q_NSL>([&](auto U) {
    constexpr int u = U.value, left = Q_NSL - 3 - u;
    (void)&rsO; (void)&voffO;
    if constexpr (u < 6) {
#pragma unroll
      for (int j = 0; j < 7; ++j) {
        const int pc = (u < 3 ? u * 21 : 64 + (u - 3) * 21) + 3 * j;
        const unsigned p = pc + sw;
        bload_lds_sc1<0>(smem_base + (u < 3 ? WO_H0 : -65536) + p * 1024u, voffO[pc & 1], rsO, ((p >> 2) * 32u + (p & 3) * 4u) << sh);
      }
    }
    if constexpr (left >= 0) wait_vm<left * 7 + (u + 1) * 7>();
    __builtin_amdgcn_s_barrier();
  });
  wait_vm<0>();
}

#define UG 12
namespace w7 {
constexpr int UT = UG + 3, UD = UT + 4;
constexpr int ops(int u) {
  int c = (u == 26) + (u == 32) + (u == DWO) + (u == UG ? 16 : 0) + (u == UT ? 2 : 0);
  for (int j = 0; j < 2 * ((TQ + 7) / 8); ++j) c += DW0 + (j * DWS) / 2 == u;
  return c;
}
constexpr int after(int v, int u) { int c = 0; for (int t = v + 1; t < u; ++t) c += ops(t); return c > 63 ? 63 : c; }
}
DI unsigned qkv_spare(char* smem, unsigned smem_base, int lane, int bm, int bn, int cn, const Args& g) {
  using namespace w7;
  u32x2 pm[16];
  u32x4 cp[4], oc[8];
  unsigned tag, c2;
  const rsrc_t rsP = mk_rsrc((const char*)g.wo + (size_t)blockIdx.x * 8192, 8192);
  {
    const rsrc_t rsQ = mk_rsrc((const char*)g.wq + (size_t)blockIdx.x * 24576, 24576);
    const unsigned vc = ((lane >> 3) & 3) * 2048 + (lane >> 5) * 1024 + (lane & 7) * 16;
    asm volatile("" : "=v"(cp[0]), "=v"(cp[1]), "=v"(cp[2]), "=v"(cp[3]), "=v"(tag));
#pragma unroll
    for (int i = 0; i < 3; ++i) bload_v<0>(cp[i], vc, rsQ, i * 8192 + slow_col(g.wq) * 128);
    bload_v<0>(cp[3], vc, rsP, slow_col(g.wo) * 128);
    bload1_v<0>(tag, 0u, mk_rsrc(g.ws, 4), 0);
    asm volatile("s_waitcnt vmcnt(0)" : "+v"(cp[0]), "+v"(cp[1]), "+v"(cp[2]), "+v"(cp[3]), "+v"(tag)::"memory");
    tag = __builtin_amdgcn_readfirstlane(tag) + 0x9E3779B9u;
    const rsrc_t rsC = mk_rsrc(g.ws + WS_WC, 1 << 20);
#pragma unroll
    for (int i = 0; i < 3; ++i) gstore4_sc1(cp[i], (lane >> 5) * 393216 + (lane & 31) * 16, rsC, (blockIdx.x * 3 + i) * 512);
    gstore4_sc1(cp[3], (lane >> 5) * 131072 + (lane & 31) * 16, rsC, 786432 + blockIdx.x * 512);
  }
  wait_vm<0>();
  asm volatile("buffer_store_dword %0, %1, %2, 0 offen sc1" ::"v"(tag), "v"(lane * 4), "s"(mk_rsrc(g.ws + WS_FC + blockIdx.x * 16, 4)) : "memory");
  gstore4_sc1(u32x4{0, 0, 0, 0}, lane * 16, mk_rsrc(g.ws + WS_F4 + blockIdx.x * 16, 16), 0);
  STAMPT(1, 3, 448);
  front_ocld(lane, 3, bm, bn, g, oc, c2);
  if constexpr (OCU < 0) front_oc<0>(smem, lane, 3, oc, c2);
  STAMPT(1, 4, 448);
  if ((C3Q && (bn & 7) == slow_col(g.wq)) || (C3O && (cn & 7) == slow_col(g.wo))) {
    const rsrc_t rsF = mk_rsrc(g.ws + WS_FC, 4096);
    unsigned spins = 0, f0, f1, f2, f3;
    do {
      if (spins) __builtin_amdgcn_s_sleep(8);
      asm volatile("buffer_load_dword %0, %4, %5, 0 offen sc1\n\tbuffer_load_dword %1, %4, %5, 0 offen offset:1024 sc1\n\t"
                   "buffer_load_dword %2, %4, %5, 0 offen offset:2048 sc1\n\tbuffer_load_dword %3, %4, %5, 0 offen offset:3072 sc1\n\ts_waitcnt vmcnt(0)"
                   : "=&v"(f0), "=&v"(f1), "=&v"(f2), "=&v"(f3) : "v"(lane * 16), "s"(rsF) : "memory");
    } while (!__all(((f0 ^ tag) | (f1 ^ tag) | (f2 ^ tag) | (f3 ^ tag)) == 0u) && ++spins < (1u << 16));
  }
  STAMPT(1, 0, 448);
  char* sc = smem + QX_OFF + 4096;
  const rsrc_t rsX = mk_rsrc(g.ws + WS_G2 + (bn ^ 2) * 512, 131072);
  const rsrc_t rsO = wo_rsrc(g.wo, g.ws + WS_WC, cn);
  const int sh = (C3O && (cn & 7) == slow_col(g.wo)) ? 7 : 11;
  const unsigned voffO = wo_voff(lane, 1, sh);
  __builtin_amdgcn_s_barrier();
  const rsrc_t rsG = mk_rsrc((const char*)g.wg + (size_t)blockIdx.x * (TQ * 1024), TQ * 1024);
  const rsrc_t rsU = mk_rsrc((const char*)g.wu + (size_t)blockIdx.x * (TQ * 1024), TQ * 1024);
  static_for<33>([&](auto U) {
    constexpr int u = U.value;
    (void)&rsG; (void)&rsU; (void)&rsP; (void)&rsX; (void)&pm; (void)&oc;
    if constexpr (u == OCU) front_oc<0>(smem, lane, 3, oc, c2);
    if constexpr (u == OCU + 1) {
      asm volatile("" ::: "memory");
      const float* rp = reinterpret_cast<const float*>(smem + QX_OFF + 1024) + lane;
      gstore2_sc1(u32x2{__float_as_uint((rp[0] + rp[64]) + (rp[128] + rp[192])), tag}, bm * 8192 + lane * 8, rsX);
    }
    if constexpr (u == CK) {
      const rsrc_t rsF = mk_rsrc(g.ws + WS_FH + (blockIdx.x & 7) * 32768 + (bm == 0 ? 0 : (bm - 1) * 2048), 4096);
      unsigned spins = 0, f0, f1;
      do {
        asm volatile("buffer_load_dword %0, %2, %3, 0 offen sc1\n\tbuffer_load_dword %1, %2, %3, %4 offen sc1\n\ts_waitcnt vmcnt(0)"
                     : "=&v"(f0), "=&v"(f1) : "v"((lane >> 2) * 128 + (lane & 3) * 4), "s"(rsF), "s"(bm ? 2048 : 0) : "memory");
      } while (!__all(f0 == tag && f1 == tag) && ++spins < (1u << 18));
      STAMPT(1, 2, 448);
    }
    if constexpr (u == UT) {
      wait_vm<after(UG, u)>();
      unsigned spins = 0;
      for (;;) {
        unsigned bad = 0;
#pragma unroll
        for (int i = 0; i < 16; ++i) { asm volatile("" : "+v"(pm[i])); bad |= pm[i][1] ^ tag; }
        if (__builtin_expect(__all(bad == 0u) || ++spins > (1u << 16), 1)) break;
#pragma unroll
        for (int i = 0; i < 16; ++i) bload2_sc1(pm[i], lane * 8, rsX, i * 8192);
        wait_vm<0>();
      }
      float m[4];
#pragma unroll
      for (int v = 0; v < 4; ++v) m[v] = (__uint_as_float(pm[v * 4][0]) + __uint_as_float(pm[v * 4 + 1][0])) + (__uint_as_float(pm[v * 4 + 2][0]) + __uint_as_float(pm[v * 4 + 3][0]));
      const float gw = m[2] + 1.0f;
      const float cb = __builtin_fabsf(gw) < 8.67361738e-19f ? 8.67361738e-19f : gw;
      float* tv = reinterpret_cast<float*>(sc) + lane;
      tv[0] = m[0]; tv[64] = cb; tv[128] = m[3]; tv[192] = 1.0f / cb;
      const unsigned hi = pk_bf16(gw, m[1]), lo = pk_bf16(gw - bf_lo(hi), m[1] - bf_hi(hi));
      unsigned short* tc = reinterpret_cast<unsigned short*>(sc + 1024 + (lane >> 5) * 320) + (lane & 31);
      tc[0] = (unsigned short)hi; tc[32] = (unsigned short)lo; tc[64] = (unsigned short)(hi >> 16); tc[96] = (unsigned short)(lo >> 16); tc[128] = 0;
      __builtin_amdgcn_wave_barrier();
      const u32x4 t0 = *reinterpret_cast<const u32x4*>(sc + lane * 16), t1 = *reinterpret_cast<const u32x4*>(sc + 1024 + lane * 16);
      gstore4_sc1(t0, lane * 16, mk_rsrc(g.ws + WS_TV + (bm * 16 + (bn ^ 2)) * 1024, 1024), 0);
      gstore4_sc1(t1, lane * 16, mk_rsrc(g.ws + WS_TC + bm * 12288 + (bn ^ 2) * 640, 640), 0);
    }
    if constexpr (u == UD) wait_vm<after(UT, u)>();
    if constexpr (u == 26) bload_lds_sc1<0>(smem_base + WO_H0 + 63 * 1024, voffO, rsO, (15 * 32u + 12u) << sh);
    if constexpr (u == 32) bload_lds_sc1<0>(smem_base + 63 * 1024, voffO, rsO, (31 * 32u + 12u) << sh);
    if constexpr (u == DWO) touch8k(smem_base + QH_OFF, lane * 128u, rsP, 0);
#pragma unroll
    for (int j = 0; j < 2 * ((TQ + 7) / 8); ++j)
      if (DW0 + (j * DWS) / 2 == u) touch8k(smem_base + QH_OFF, lane * 128u, (j & 1) ? rsU : rsG, (j >> 1) * 8192u);
    if constexpr (u == UG) {
#pragma unroll
      for (int i = 0; i < 16; ++i) { asm volatile("" : "=v"(pm[i])); bload2_sc1(pm[i], lane * 8, rsX, i * 8192); }
    }
    __builtin_amdgcn_s_barrier();
  });
  asm volatile("s_waitcnt vmcnt(0) lgkmcnt(0)" ::: "memory");
  return tag;
}

DI void qkv_mfma(char* smem, unsigned smem_base, int tid, int wid, int m0, int n0, bf16* O, unsigned* oflags, unsigned tag) {
  const int lane = tid & 63;
  const int wm = wid & 1, wn = wid >> 1;
  const int fr = lane & 15, fq = lane >> 4;
  unsigned brd[6];
  const unsigned hrd = smem_base + lane * 16, ard = hrd + 1024 + wm * 4096;
  {
    const int q = (lane >> 2) & 3, p = lane & 3;
    const int r = 8 * fq + q;
    const int g = ((r >> 1) & 1) | (((r >> 3) & 1) << 1);
#pragma unroll
    for (int jj = 0; jj < 6; ++jj) {
      brd[jj] = smem_base + 9216 + (jj >> 1) * 4096 + r * 128 + (((wn * 2 + ((jj & 1) ^ wm)) ^ g) * 32) + 8 * p;
    }
  }
  f32x4 acc[4][6], acch[2];
#pragma unroll
  for (int i = 0; i < 4; ++i)
#pragma unroll
    for (int j = 0; j < 6; ++j) { acc[i][j] = f32x4{0, 0, 0, 0}; asm volatile("" : "+v"(acc[i][j])); }
#pragma unroll
  for (int j = 0; j < 2; ++j) { acch[j] = f32x4{0, 0, 0, 0}; asm volatile("" : "+v"(acch[j])); }
  asm volatile("s_waitcnt lgkmcnt(0)" ::: "memory");
  __builtin_amdgcn_s_barrier();
  STAMPDECL;
  STAMPNW(2, 1);
  u32x4 aX[5], aY[5];
  u32x2 bXl[6], bXh[6], bYl[6], bYh[6];
#define Q_READ(AR, BL, BH, SO, R)                                                                                \
  do {                                                                                                           \
    if ((R) < 12) {                                                                                              \
      constexpr int jb = ((R) < 12 ? (R) : 0) >> 1;                                                              \
      if ((R) & 1) dsr_tr<512>(BH[jb], brd[jb] + (SO)); else dsr_tr<0>(BL[jb], brd[jb] + (SO));                  \
    } else if ((R) < 16) {                                                                                       \
      constexpr int ia = ((R) >= 12 && (R) < 16) ? (R) - 12 : 0;                                                 \
      dsr128<ia * 1024>(AR[ia], ard + (SO));                                                                     \
    } else {                                                                                                     \
      dsr128<0>(AR[4], hrd + (SO));                                                                              \
    }                                                                                                            \
  } while (0)
#define Q_MFMA(AR, BB, N)                                                                                        \
  do {                                                                                                           \
                                     \
    if ((N) < 16) MFMA_AV(acc[(N) & 3][((N) < 24 ? (N) : 0) >> 2], BB[((N) < 24 ? (N) : 0) >> 2], AR[(N) & 3]);  \
    else if ((N) < 24) MFMA_AV(acc[(N) & 3][((N) < 24 ? (N) : 0) >> 2], AR[(N) & 3], BB[((N) < 24 ? (N) : 0) >> 2]);  \
    else if ((N) == 24) MFMA_AV(acch[0], BB[2], AR[4]);                                                          \
    else MFMA_AV(acch[1], AR[4], BB[4]);                                                                         \
  } while (0)
#define Q_UNIT(AR, BL, BH, NAR, NBL, NBH)                                                                        \
  do {                                                                                                           \
    so = (so == (Q_NSL - 1) * Q_SLOT) ? 0u : so + Q_SLOT;                                                        \
    WAIT_LGKM0();                                                                                                \
    u32x4 bb[6];                                                                                                 \
    _Pragma("unroll") for (int j = 0; j < 6; ++j) bb[j] = u32x4{BL[j][0], BL[j][1], BH[j][0], BH[j][1]};         \
    SCHED0();                                                                                                    \
    asm volatile("s_nop 1");                                                                                     \
    static_for<26>([&](auto N) {                                                                                 \
      constexpr int n = N.value;                                                                                 \
      if constexpr (n == QBAR) __builtin_amdgcn_s_barrier();                                                   \
      Q_MFMA(AR, bb, n);                                                                                         \
      if constexpr (n >= QBAR && n < QBAR + QRN) {                                                               \
        constexpr int r0 = ((n - QBAR) * 17) / QRN, r1 = ((n - QBAR + 1) * 17) / QRN;                            \
        Q_READ(NAR, NBL, NBH, so, r0);                                                                           \
        if constexpr (r0 + 1 < r1) Q_READ(NAR, NBL, NBH, so, (r0 + 1 < 17 ? r0 + 1 : 0));                        \
      }                                                                                                          \
    });                                                                                                          \
    SCHED0();                                                                                                    \
  } while (0)
  unsigned so = 0;
  static_for<17>([&](auto R) { Q_READ(aX, bXl, bXh, 0u, R.value); });
  for (int u = 0; u < 32; u += 2) {
    Q_UNIT(aX, bXl, bXh, aY, bYl, bYh);
    Q_UNIT(aY, bYl, bYh, aX, bXl, bXh);
  }
  asm volatile("s_waitcnt vmcnt(0) lgkmcnt(0)\n\ts_nop 15" ::: "memory");
#pragma unroll
  for (int i = 0; i < 5; ++i) asm volatile("" : "+v"(aX[i]));
#pragma unroll
  for (int j = 0; j < 6; ++j) asm volatile("" : "+v"(bXl[j]), "+v"(bXh[j]));
  SCHED0();
  __builtin_amdgcn_s_setprio(0);
  STAMP(2, 2); STAMPFLUSH(2, 1);
#pragma unroll
  for (int i = 0; i < 4; ++i)
#pragma unroll
    for (int j = 0; j < 6; ++j) asm volatile("" : "+v"(acc[i][j]));
  asm volatile("" : "+v"(acch[0]), "+v"(acch[1]));
  auto pk2 = [](const f32x4 v) { return u32x2{pk_bf16(v[0], v[1]), pk_bf16(v[2], v[3])}; };
  auto cat = [](const u32x2 a, const u32x2 b) { return __builtin_bit_cast(bf16x8, u32x4{a[0], a[1], b[0], b[1]}); };
  u32x2 kp[5][2], vp[5][2];
  {
    char* xb = smem + QX_OFF + wn * 4096 + lane * 8;
#pragma unroll
    for (int f = 0; f < 2; ++f) { kp[4][f] = pk2(acc[3][2 + f]); vp[4][f] = pk2(acc[3][4 + f]); }
    *reinterpret_cast<u32x2*>(xb + wm * 512) = pk2(acch[0]);
    *reinterpret_cast<u32x2*>(xb + 1024 + wm * 512) = pk2(acch[1]);
    if (wm == 0) {
#pragma unroll
      for (int f = 0; f < 2; ++f) {
        *reinterpret_cast<u32x2*>(xb + 2048 + f * 512) = kp[4][f];
        *reinterpret_cast<u32x2*>(xb + 2048 + 1024 + f * 512) = vp[4][f];
      }
    }
    asm volatile("s_waitcnt lgkmcnt(0)" ::: "memory");
    __builtin_amdgcn_s_barrier();
    asm volatile("" ::: "memory");
#pragma unroll
    for (int f = 0; f < 2; ++f) {
      kp[0][f] = *reinterpret_cast<const u32x2*>(xb + wm * 2048 + (f ^ wm) * 512);
      vp[0][f] = *reinterpret_cast<const u32x2*>(xb + wm * 2048 + 1024 + (f ^ wm) * 512);
    }
  }
  STAMP(2, 3);
#pragma unroll
  for (int i = 0; i < 3; ++i)
#pragma unroll
    for (int f = 0; f < 2; ++f) { kp[1 + i][f] = pk2(acc[i][2 + f]); vp[1 + i][f] = pk2(acc[i][4 + f]); }
  const bool par = fr & 1;
  const bool cura = fq * 4 + (fr & 1) <= fr, curb = fq * 4 + 2 + (fr & 1) <= fr;
  const unsigned mh = par ? 0xffff0000u : 0x0000ffffu;
  const unsigned mca = cura ? mh : 0u, mpa = cura ? 0u : mh, mcb = curb ? mh : 0u, mpb = curb ? 0u : mh;
  const float cs = 0.17677669529663687f * 1.4426950408889634f;
  char* op = (char*)O + ((size_t)((m0 >> 6) + wm) * (D_DIM / 32) + (n0 >> 5) + wn) * 4096 + lane * 16;
  const f32x4 z4 = {0, 0, 0, 0};
#pragma unroll
  for (int ii = 0; ii < 4; ++ii) {
    const int i = 3 - ii;
    const bf16x8 qf = cat(pk2(acc[i][0]), pk2(acc[i][1]));
    const f32x4 sc = __builtin_amdgcn_mfma_f32_16x16x32_bf16(cat(kp[i + 1][0], kp[i + 1][1]), qf, z4, 0, 0, 0);
    f32x4 sp = __builtin_amdgcn_mfma_f32_16x16x32_bf16(cat(kp[i][0], kp[i][1]), qf, z4, 0, 0, 0);
    if (i == 0 && m0 == 0 && wm == 0) sp = f32x4{-5.656854249e9f, -5.656854249e9f, -5.656854249e9f, -5.656854249e9f};
    const float la = cura ? (par ? sc[1] : sc[0]) : (par ? sp[1] : sp[0]);
    const float lb = curb ? (par ? sc[3] : sc[2]) : (par ? sp[3] : sp[2]);
    float mx = fmaxf(la, lb);
    {
      const auto a = __builtin_amdgcn_permlane16_swap(__float_as_uint(mx), __float_as_uint(mx), false, false);
      mx = fmaxf(__uint_as_float(a[0]), __uint_as_float(a[1]));
      const auto b = __builtin_amdgcn_permlane32_swap(__float_as_uint(mx), __float_as_uint(mx), false, false);
      mx = fmaxf(__uint_as_float(b[0]), __uint_as_float(b[1]));
    }
    const float ea = __builtin_amdgcn_exp2f((la - mx) * cs), eb = __builtin_amdgcn_exp2f((lb - mx) * cs);
    const float inv = __builtin_amdgcn_rcpf(rowsum4(ea + eb));
    const unsigned wa = pk_bf16(ea, ea), wb = pk_bf16(eb, eb);
    const bf16x8 pf = __builtin_bit_cast(bf16x8, u32x4{wa & mpa, wb & mpb, wa & mca, wb & mcb});
    const f32x4 o0 = __builtin_amdgcn_mfma_f32_16x16x32_bf16(cat(vp[i][0], vp[i + 1][0]), pf, z4, 0, 0, 0) * inv;
    const f32x4 o1 = __builtin_amdgcn_mfma_f32_16x16x32_bf16(cat(vp[i][1], vp[i + 1][1]), pf, z4, 0, 0, 0) * inv;
    const u32x2 u0 = pk2(o0), u1 = pk2(o1);
    gst128(op + i * 1024, wm ? u32x4{u1[0], u1[1], u0[0], u0[1]} : u32x4{u0[0], u0[1], u1[0], u1[1]});
  }
  STAMP(2, 4);
  wait_vm<0>();
  asm volatile("buffer_store_dword %0, %1, %2, 0 offen sc1" ::"v"(tag), "v"(lane * (4096 * OFS)), "s"(mk_rsrc(oflags + ((m0 >> 1) + wm * 32 + (n0 >> 5) + wn) * OFS, (ORP - 1) * 4096 * OFS + 4)) : "memory");
  STAMP(2, 5);
}

DI void o_phase(char* smem, unsigned smem_base, int lane, int w, int cm, int cn, const bf16* O, const unsigned* oflags,
                const bf16* __restrict__ x, bf16* Y, float* stat, const char* tabv, unsigned tag) {
  const int wm = w & 3, kh = w >> 2;
  const int fr = lane & 15, fq = lane >> 4;
  const int R = cm * 8 + wm * 2 + kh;
  u32x2 xv[4];
  {
    const rsrc_t rsX = mk_rsrc(x, S_LEN * D_DIM * 2);
    const unsigned vx = (unsigned)(((R * 16 + fr) * D_DIM + cn * 64 + fq * 4) * 2);
#pragma unroll
    for (int f = 0; f < 4; ++f) asm volatile("" : "=v"(xv[f]));
    bload2_v<0>(xv[0], vx, rsX, 0); bload2_v<32>(xv[1], vx, rsX, 0); bload2_v<64>(xv[2], vx, rsX, 0); bload2_v<96>(xv[3], vx, rsX, 0);
  }
  const unsigned hb = smem_base + (kh ? 0 : WO_H0);
  unsigned brd[4];
  {
    const int q = (lane >> 2) & 3, p = lane & 3;
    const int r = 8 * fq + q;
    const int g = ((r >> 1) & 1) | (((r >> 3) & 1) << 1);
#pragma unroll
    for (int f = 0; f < 4; ++f) brd[f] = hb + r * 128 + ((f ^ g) * 32) + 8 * p;
  }
  const rsrc_t rsA = mk_rsrc((const char*)O + (size_t)(cm * 2 + (wm >> 1)) * 131072 + kh * 65536, 65536);
  unsigned va0 = lane * 16 + ((wm & 1) * 2 + kh) * 1024, va1 = va0 ^ 1024;
  unsigned tsrc = smem_base + QT_OFF + fq * 16, xw = hb + lane * 16 + wm * 4096, xr = smem_base + (kh ? WO_H0 : 0) + lane * 16 + wm * 4096;
  char* yb = (char*)Y + ((size_t)(R >> 4) * 32 + cn * 2 + (fq & 1)) * 16384 + (R & 15) * 1024 + ((fq >> 1) * 16 + fr) * 16;
  asm volatile("" : "+v"(tsrc), "+v"(xw), "+v"(xr), "+v"(yb), "+v"(va0), "+v"(va1), "+v"(brd[0]), "+v"(brd[1]), "+v"(brd[2]), "+v"(brd[3]));
  f32x4 acc[2][4];
#pragma unroll
  for (int i = 0; i < 2; ++i)
#pragma unroll
    for (int f = 0; f < 4; ++f) { acc[i][f] = f32x4{0, 0, 0, 0}; asm volatile("" : "+v"(acc[i][f])); }
  if (w == POLLW) {
    const rsrc_t rsF = mk_rsrc(oflags + ((blockIdx.x & (ORP - 1)) * 1024 + cm * 64) * OFS, 256 * OFS);
    unsigned spins = 0, f;
    do {
      asm volatile("buffer_load_dword %0, %1, %2, 0 offen sc1\n\ts_waitcnt vmcnt(0)" : "=v"(f) : "v"(lane * (4 * OFS)), "s"(rsF) : "memory");
    } while (!__all(f == tag) && ++spins < (1u << 18));
  }
  __builtin_amdgcn_s_barrier();
  STAMPDECL;
  STAMPNW(3, 1);
  if (w == 7) {
    bload_lds_sc1<0>(smem_base + QT_OFF, lane * 16u, mk_rsrc(tabv + (cm * 16 + cn) * 1024, 512), 0);
    if (blockIdx.x == 0) asm volatile("buffer_store_dword %0, %1, %2, 0 offen sc1" ::"v"(tag), "v"(lane * 4), "s"(mk_rsrc(tabv - WS_TV, 4)) : "memory");
  }
  u32x4 a[16][2], gt[4], ct[4];
  u32x2 bl[2][4], bh[2][4];
  auto rdb = [&](auto S) {
    constexpr int s = S.value;
#pragma unroll
    for (int f = 0; f < 4; ++f) { dsr_tr<s * 4096>(bl[s & 1][f], brd[f]); dsr_tr<s * 4096 + 512>(bh[s & 1][f], brd[f]); }
  };
  auto lda = [&](auto S) {
    constexpr int s = S.value;
    asm volatile("" : "=v"(a[s][0]), "=v"(a[s][1]));
    bload_sc1<0>(a[s][0], va0, rsA, s * 4096);
    bload_sc1<0>(a[s][1], va1, rsA, s * 4096);
  };
  static_for<OD>([&](auto S) {
    lda(S);
    if constexpr (S.value == 0) rdb(ic<0>{});
    if constexpr (S.value == 1) rdb(ic<1>{});
  });
  static_for<16>([&](auto S) {
    constexpr int s = S.value;
    asm volatile("s_waitcnt vmcnt(%0) lgkmcnt(8)" ::"n"(2 * ((s + OD - 1 < 15 ? s + OD - 1 : 15) - s)) : "memory");
    SCHED0();
    u32x4 bb[4];
#pragma unroll
    for (int f = 0; f < 4; ++f) bb[f] = u32x4{bl[s & 1][f][0], bl[s & 1][f][1], bh[s & 1][f][0], bh[s & 1][f][1]};
    SCHED0();
    if constexpr (s + OD < 16) lda(ic<(s + OD < 16 ? s + OD : 0)>{});
    if constexpr (s == OMB) __builtin_amdgcn_s_barrier();
#pragma unroll
    for (int n = 0; n < 8; ++n) MFMA_AV(acc[1 - (n >> 2)][n & 3], bb[n & 3], a[s][1 - (n >> 2)]);
    if constexpr (s < 14) rdb(ic<(s < 14 ? s + 2 : 0)>{});
    if constexpr (s == 14) {
      static_for<4>([&](auto F) { dsr128<F.value * 64>(gt[F.value], tsrc); dsr128<256 + F.value * 64>(ct[F.value], tsrc); });
    }
    SCHED0();
  });
  STAMP(3, 2); STAMPFLUSH(3, 1);
  asm volatile("s_nop 7" ::: "memory");
  static_for<4>([&](auto F) { dsw128<F.value * 1024>(xw, __builtin_bit_cast(u32x4, acc[1][F.value])); });
  asm volatile("s_waitcnt lgkmcnt(0)" ::: "memory");
  __builtin_amdgcn_s_barrier();
  u32x4 pr[4];
  static_for<4>([&](auto F) { dsr128<F.value * 1024>(pr[F.value], xr); });
#pragma unroll
  for (int f = 0; f < 4; ++f) asm volatile("" : "+v"(acc[0][f]), "+v"(xv[f]), "+v"(gt[f]), "+v"(ct[f]));
  asm volatile("s_waitcnt lgkmcnt(0)" : "+v"(pr[0]), "+v"(pr[1]), "+v"(pr[2]), "+v"(pr[3])::"memory");
  f32x4 fin[4];
  u32x2 pk[4];
#pragma unroll
  for (int f = 0; f < 4; ++f) {
    const f32x4 xf = {bf_lo(xv[f][0]), bf_hi(xv[f][0]), bf_lo(xv[f][1]), bf_hi(xv[f][1])};
    fin[f] = xf + __builtin_bit_cast(f32x4, gt[f]) * (acc[0][f] + __builtin_bit_cast(f32x4, pr[f]));
    const f32x4 yv = fin[f] * __builtin_bit_cast(f32x4, ct[f]);
    pk[f] = u32x2{pk_bf16(yv[0], yv[1]), pk_bf16(yv[2], yv[3])};
  }
#pragma unroll
  for (int j = 0; j < 2; ++j) {
    const auto s0 = __builtin_amdgcn_permlane16_swap(pk[j][0], pk[j + 2][0], false, false);
    const auto s1 = __builtin_amdgcn_permlane16_swap(pk[j][1], pk[j + 2][1], false, false);
    gst128(yb + j * 512, u32x4{s0[0], s1[0], s0[1], s1[1]});
  }
  const f32x4 s4 = (fin[0] + fin[1]) + (fin[2] + fin[3]);
  const float sm = rowsum4((s4[0] + s4[1]) + (s4[2] + s4[3]));
  const float mb = sm * (1.0f / 64.0f);
  f32x4 q4 = {0, 0, 0, 0};
#pragma unroll
  for (int f = 0; f < 4; ++f) { const f32x4 d = fin[f] - mb; q4 += d * d; }
  const float m2 = rowsum4((q4[0] + q4[1]) + (q4[2] + q4[3]));
  asm volatile("buffer_store_dwordx2 %0, %1, %2, 0 offen sc1" ::"v"(f32x2{sm, m2}), "v"(lane * 8), "s"(mk_rsrc(stat + (cn * S_LEN + R * 16) * 2, 128)) : "memory");
}

#define SPOS 6
#define TPOS 6
#define G3B 30
#define NBC 1
constexpr int G3_CT = 131072 + 4096 + 1024;
#define G3_STAGE(SLOT, P, U)                                                                               \
  do {                                                                                                     \
    if ((P) == 0) bload_lds_sc1<0>(m0w + (SLOT) * 32768, voffA, rsA, (unsigned)(U) * 16384u);              \
    else if ((P) == 1) bload_lds_sc1<1024>(m0w + (SLOT) * 32768, voffA, rsA, (unsigned)(U) * 16384u);      \
    else if ((P) == 2) bload_lds<0>(m0w + (SLOT) * 32768 + 16384, voffB0, rsB, (unsigned)(U) * 229376u);   \
    else bload_lds<0>(m0w + (SLOT) * 32768 + 16384 + 1024, voffB1, rsB, (unsigned)(U) * 229376u);          \
  } while (0)

template <int FN>
DI void g3_body(char* smem, unsigned smem_base, int wm, int wn, int wid, int lane, int bm, int bn, rsrc_t rsA, rsrc_t rsB,
                rsrc_t rsP, unsigned voffA, unsigned voffB0, unsigned voffB1, bf16* __restrict__ act, const float* stat) {
  constexpr int NB = 2 * FN, NM = 4 * NB, RD = 5 + 2 * NB;
  const int fr = lane & 15, fq = lane >> 4;
  f32x4 acc[4][NB], accC[2];
  accC[0] = f32x4{0, 0, 0, 0}; accC[1] = f32x4{0, 0, 0, 0};
  asm volatile("" : "+v"(accC[0]), "+v"(accC[1]));
  const int rot = wm % FN;
  unsigned cad = smem_base + G3_CT + (fr < 4 ? fr * 64 : 256) + fq * 16;
#pragma unroll
  for (int i = 0; i < 4; ++i)
#pragma unroll
    for (int j = 0; j < NB; ++j) { acc[i][j] = f32x4{0, 0, 0, 0}; asm volatile("" : "+v"(acc[i][j])); }
  unsigned aad[2], bad[2][FN];
  aad[0] = smem_base + wm * 4096 + lane * 16; aad[1] = aad[0] + 65536;
  {
    const int q = (lane >> 2) & 3, p = lane & 3;
    const int r = 8 * fq + q;
    const int sw = (r & 3) | (((r >> 3) & 1) << 2);
#pragma unroll
    for (int j = 0; j < FN; ++j) {
      const int gi = wn * 4 + (j + rot) % FN + (bn & 1);
      bad[0][j] = smem_base + 16384 + r * 256 + ((gi ^ sw) * 32) + 8 * p;
      bad[1][j] = bad[0][j] + 65536;
    }
  }
  u32x4 aX[5], aY[5];
  u32x2 bXl[NB], bXh[NB], bYl[NB], bYh[NB];
  const unsigned m0w = smem_base + wid * 2048;
  u32x2 tv[16];
  const rsrc_t rsS = mk_rsrc(stat, S_LEN * 128);
  const unsigned voffS = (unsigned)((bm * 256 + wm * 64 + lane) * 8);
  if constexpr (FN == 3) {
#pragma unroll
    for (int i = 0; i < 16; ++i) asm volatile("" : "=v"(tv[i]));
  }
  auto lds_ = [&](auto SL) {
#pragma unroll
    for (int i = SL.value * 4; i < SL.value * 4 + 4; ++i)
      bload2_sc1(tv[i], voffS, rsS, i * 16384);
  };
#define G3_READ(AR, BL, BH, SLOT, R)                                                                       \
  do {                                                                                                     \
    if ((R) < 2 * NB) {                                                                                    \
      constexpr int jb = ((R) < 2 * NB ? (R) : 0) >> 1;                                                    \
      constexpr int off = ((SLOT) & 1) * 32768 + (jb / FN) * 8192 + ((R) & 1) * 1024;                      \
      if ((R) & 1) dsr_tr<off>(BH[jb], bad[(SLOT) >> 1][jb % FN]);                                         \
      else dsr_tr<off>(BL[jb], bad[(SLOT) >> 1][jb % FN]);                                                 \
    } else if ((R) < 2 * NB + 4) {                                                                         \
      constexpr int ia = ((R) >= 2 * NB && (R) < 2 * NB + 4) ? (R) - 2 * NB : 0;                           \
      dsr128<((SLOT) & 1) * 32768 + ia * 1024>(AR[ia], aad[(SLOT) >> 1]);                                  \
    } else {                                                                                               \
      if ((SLOT) == 0) cad += 1280;                                      \
      dsr128<(SLOT) * 320>(AR[4], cad);                                                                    \
    }                                                                                                      \
  } while (0)
  auto rd = [&](auto SL, auto RR, u32x4 (&AR)[5], u32x2 (&BL)[NB], u32x2 (&BH)[NB]) { G3_READ(AR, BL, BH, SL.value, RR.value); };
#define G3_UNIT(AR, BL, BH, NAR, NBL, NBH, SLOT, U)                                                        \
  do {                                                                                                     \
    WAIT_LGKM0();                                                                                          \
    u32x4 bb[NB];                                                                                          \
    _Pragma("unroll") for (int j = 0; j < NB; ++j) bb[j] = u32x4{BL[j][0], BL[j][1], BH[j][0], BH[j][1]};  \
    SCHED0();                                                                                              \
    asm volatile("s_nop 1");                                                                               \
    static_for<NM>([&](auto N) {                                                                           \
      constexpr int n = N.value;                                                                           \
      if constexpr (n == NM / 2) {                                                                         \
        wait_vm<8>();           __builtin_amdgcn_s_barrier();                                             \
      }                                                                                                    \
      if constexpr (FN == 3 && n == NM / 2 + SPOS) {                                                       \
        if (__builtin_expect(u == 4, 0)) lds_(ic<SLOT>{});                                                 \
      }                                                                                                    \
      if constexpr (FN == 4 && (G3B & 3) == (SLOT) && n == NM / 2 + TPOS) {                                \
                                                                 \
        if (__builtin_expect((U) == G3B, 0)) touch8k(smem_base + 131072 + 4096, lane * 128u, rsP, wid * 8192u); \
      }                                                                                                    \
      MFMA_AV(acc[n & 3][n >> 2], bb[n >> 2], AR[n & 3]);                                                  \
      if constexpr (n < NM / 2) {                                                                          \
        if constexpr (n % 3 == 1 && n / 3 < 4) G3_STAGE(((SLOT) + 3) & 3, n / 3, (U) + 3);                 \
      } else {                                                                                             \
        constexpr int r0 = (n - NM / 2) * 2;                                                               \
        if constexpr (r0 < RD) rd(ic<((SLOT) + 1) & 3>{}, ic<(r0 < RD ? r0 : 0)>{}, NAR, NBL, NBH);        \
        if constexpr (r0 + 1 < RD) rd(ic<((SLOT) + 1) & 3>{}, ic<(r0 + 1 < RD ? r0 + 1 : 0)>{}, NAR, NBL, NBH); \
      }                                                                                                    \
    });                                                                                                    \
    if (NBC || FN == 4 || wm < 3) {                                                                        \
      MFMA_AV(accC[0], bb[0], AR[4]);                                                                      \
      MFMA_AV(accC[1], bb[FN], AR[4]);                                                                     \
    }                                                                                                      \
    SCHED0();                                                                                              \
  } while (0)

  asm volatile("s_waitcnt vmcnt(6) lgkmcnt(0)" ::: "memory");
  __builtin_amdgcn_s_barrier();
  STAMPDECL;
  STAMPNW(5, 1);
  cad -= 1280;
  static_for<RD>([&](auto RR) { rd(ic<0>{}, RR, aX, bXl, bXh); });
  for (int u = 0; u < 32; u += 4) {
    if constexpr (FN == 3) {
#pragma unroll
      for (int i = 0; i < 16; ++i) asm volatile("" : "+v"(tv[i]));
      if (__builtin_expect(u == 12, 0)) {
        float s = 0;
#pragma unroll
        for (int i = 0; i < 16; i += 2) s += __uint_as_float(tv[i][0]) + __uint_as_float(tv[i + 1][0]);
        const float mean = s * (1.0f / D_DIM);
        float m2 = 0;
#pragma unroll
        for (int i = 0; i < 16; i += 2) {
          const float d0 = __uint_as_float(tv[i][0]) * (1.0f / 64.0f) - mean, d1 = __uint_as_float(tv[i + 1][0]) * (1.0f / 64.0f) - mean;
          m2 += __uint_as_float(tv[i][1]) + __uint_as_float(tv[i + 1][1]) + 64.0f * (d0 * d0 + d1 * d1);
        }
        const float rstd = rsqrtf(m2 * (1.0f / D_DIM) + 1e-5f);
        reinterpret_cast<f32x2*>(smem + 131072)[wm * 64 + lane] = f32x2{rstd, -mean * rstd};
      }
    }
    G3_UNIT(aX, bXl, bXh, aY, bYl, bYh, 0, u);
    G3_UNIT(aY, bYl, bYh, aX, bXl, bXh, 1, u + 1);
    G3_UNIT(aX, bXl, bXh, aY, bYl, bYh, 2, u + 2);
    G3_UNIT(aY, bYl, bYh, aX, bXl, bXh, 3, u + 3);
  }
  asm volatile("s_waitcnt lgkmcnt(0)\n\ts_nop 15" ::: "memory");
#pragma unroll
  for (int i = 0; i < 5; ++i) asm volatile("" : "+v"(aX[i]));
#pragma unroll
  for (int j = 0; j < NB; ++j) asm volatile("" : "+v"(bXl[j]), "+v"(bXh[j]));
  SCHED0();
  STAMP(5, 2);
  STAMPFLUSH(5, 1);
  const f32x2* rstat = reinterpret_cast<const f32x2*>(smem + 131072);
  float* colc = reinterpret_cast<float*>(smem + 131072 + 2048);
#pragma unroll
  for (int m = 0; m < 2; ++m) {
    f32x4 v;
#pragma unroll
    for (int r = 0; r < 4; ++r) v[r] = accC[m][r] + __shfl_xor(accC[m][r], 1);
    if ((fr == 0 || fr == 2) && wm < FN) *reinterpret_cast<f32x4*>(colc + (m * 2 + (fr >> 1)) * 128 + (wn * 4 + rot) * 16 + fq * 4) = v;
  }
  asm volatile("s_waitcnt lgkmcnt(0)" ::: "memory");
  __builtin_amdgcn_s_barrier();
  asm volatile("" ::: "memory");
  f32x2 rs[4];
#pragma unroll
  for (int i = 0; i < 4; ++i) rs[i] = rstat[wm * 64 + i * 16 + fr];
#pragma unroll
  for (int j = 0; j < FN; ++j) {
    const int jg = (j + rot) % FN;
    const int cl = (wn * 4 + jg) * 16 + fq * 4;
    const f32x4 csg = *reinterpret_cast<const f32x4*>(colc + cl), bg = *reinterpret_cast<const f32x4*>(colc + 128 + cl);
    const f32x4 csu = *reinterpret_cast<const f32x4*>(colc + 256 + cl), bu = *reinterpret_cast<const f32x4*>(colc + 384 + cl);
    u32x2 pk[4];
#pragma unroll
    for (int i = 0; i < 4; ++i) {
#pragma unroll
      for (int hh = 0; hh < 2; ++hh) {
        const f32x2 ag = hh ? hi2(acc[i][j]) : lo2(acc[i][j]), au = hh ? hi2(acc[i][FN + j]) : lo2(acc[i][FN + j]);
        const f32x2 g = rs[i][0] * ag + (rs[i][1] * (hh ? hi2(csg) : lo2(csg)) + (hh ? hi2(bg) : lo2(bg)));
        const f32x2 uu = rs[i][0] * au + (rs[i][1] * (hh ? hi2(csu) : lo2(csu)) + (hh ? hi2(bu) : lo2(bu)));
        const f32x2 ea = g * -1.4426950408889634f;
        f32x2 e = {__builtin_amdgcn_exp2f(ea[0]), __builtin_amdgcn_exp2f(ea[1])};
        e = e + 1.0f;
        const f32x2 rc = {__builtin_amdgcn_rcpf(e[0]), __builtin_amdgcn_rcpf(e[1])};
        const f32x2 o = (g * uu) * rc;
        pk[i][hh] = pk_bf16(o[0], o[1]);
      }
    }
    const int J = bn * 7 + wn * 4 + jg;
    char* dst = (char*)act + ((size_t)((bm * 4 + wm) * (F_DIM / 32) + (J >> 1)) * 4 + (fq & 1)) * 1024 + (((J & 1) * 2 + (fq >> 1)) * 16 + fr) * 16;
#pragma unroll
    for (int i = 0; i < 4; i += 2) {
      const auto s0 = __builtin_amdgcn_permlane16_swap(pk[i][0], pk[i + 1][0], false, false);
      const auto s1 = __builtin_amdgcn_permlane16_swap(pk[i][1], pk[i + 1][1], false, false);
      gst128(dst + i * 1024, u32x4{s0[0], s1[0], s0[1], s1[1]});
    }
  }
}

DI void g3_phase(char* smem, unsigned smem_base, int tid, int wid, const bf16* Y, const bf16* __restrict__ Wg,
                 const bf16* __restrict__ Wu, bf16* __restrict__ act, const float* stat,
                 const bf16* __restrict__ Wd, unsigned* yflags, int own, const char* tabs, unsigned tag) {
  const int lane = tid & 63;
  const int wm = wid & 3, wn = wid >> 2;
  const int xcd = blockIdx.x & 7, idx = blockIdx.x >> 3;
  const int bm = idx & 7, bn = xcd * 4 + (idx >> 3);
  const int n0 = bn * 112, n0a = n0 & ~31;
  const rsrc_t rsA = mk_rsrc((const char*)Y + (size_t)bm * 32 * 16384, 32 * 16384);
  const rsrc_t rsB = mk_rsrc(wn ? Wu : Wg, D_DIM * F_DIM * 2);
  const rsrc_t rsP = mk_rsrc((const char*)Wd + (size_t)blockIdx.x * 28672, 28672);
  const unsigned voffA = wid * 2048 + lane * 16;
  unsigned voffB[2];
#pragma unroll
  for (int i = 0; i < 2; ++i) {
    const int r = (wm * 2 + i) * 4 + (lane >> 4);
    const int sw = (r & 3) | (((r >> 3) & 1) << 2);
    const int c16 = lane & 15;
    voffB[i] = (unsigned)(r * (F_DIM * 2) + (n0a + (((c16 >> 1) ^ sw) * 16) + (c16 & 1) * 8) * 2);
  }
  const unsigned m0w = smem_base + wid * 2048;
  const unsigned voffB0 = voffB[0], voffB1 = voffB[1];
  G3_STAGE(0, 2, 0); G3_STAGE(0, 3, 0); G3_STAGE(1, 2, 1); G3_STAGE(1, 3, 1);
  wait_vm<4>();
  __builtin_amdgcn_s_barrier();
  STAMP(3, 4);
  if (wn == 0) {
    if (wm == 0) {
      asm volatile("buffer_store_dword %0, %1, %2, 0 offen sc1" ::"v"(tag), "v"(lane * (1024 * YFS)), "s"(mk_rsrc(yflags + own * YFS, 7 * 1024 * YFS + 4)) : "memory");
      const rsrc_t rsF = mk_rsrc(yflags + (xcd * 256 + bm * 32) * YFS, 128 * YFS);
      if (YSL) __builtin_amdgcn_s_sleep(YSL);
      unsigned spins = 0, f;
      do {
        asm volatile("buffer_load_dword %0, %1, %2, 0 offen sc1\n\ts_waitcnt vmcnt(0)" : "=v"(f) : "v"((lane & 31) * (4 * YFS)), "s"(rsF) : "memory");
      } while (!__all(f == tag) && ++spins < (1u << 18));
    }
    __builtin_amdgcn_s_barrier();
    STAMP(5, 0);
    G3_STAGE(0, 0, 0); G3_STAGE(0, 1, 0); G3_STAGE(1, 0, 1); G3_STAGE(1, 1, 1);
    G3_STAGE(2, 0, 2); G3_STAGE(2, 1, 2); G3_STAGE(2, 2, 2); G3_STAGE(2, 3, 2);
    g3_body<4>(smem, smem_base, wm, wn, wid, lane, bm, bn, rsA, rsB, rsP, voffA, voffB0, voffB1, act, stat);
  } else {
    const rsrc_t rsT = mk_rsrc(tabs, 32 * 320);
#pragma unroll
    for (int j = 0; j < 3; ++j) bload_lds_sc1<0>(smem_base + G3_CT + (wm * 3 + j) * 1024, lane * 16u, rsT, (wm * 3 + j) * 1024u);
    __builtin_amdgcn_s_barrier();
    G3_STAGE(0, 0, 0); G3_STAGE(0, 1, 0); G3_STAGE(1, 0, 1); G3_STAGE(1, 1, 1);
    G3_STAGE(2, 0, 2); G3_STAGE(2, 1, 2); G3_STAGE(2, 2, 2); G3_STAGE(2, 3, 2);
    g3_body<3>(smem, smem_base, wm, wn, wid, lane, bm, bn, rsA, rsB, rsP, voffA, voffB0, voffB1, act, stat);
  }
}

__global__ __launch_bounds__(512, 2) void block_kernel(const bf16* x_, const bf16* c_, const bf16* wmod_, char* ws_, const bf16* wq_,
                                                       const bf16* wo_, const bf16* wg_, const bf16* wu_, const bf16* __restrict__ wd) {
  __shared__ __attribute__((aligned(16))) char smem[Q_LDS];
  u64x8 ka;
  asm volatile("s_load_dwordx16 %0, %1, 0x0" : "=s"(ka) : "s"(__builtin_amdgcn_kernarg_segment_ptr()));
  STAMP(0, 0);
  const int tid = threadIdx.x;
  const int wid = __builtin_amdgcn_readfirstlane(tid >> 6);
  const unsigned smem_base = (unsigned)(uintptr_t)smem;
  const int xcd = blockIdx.x & 7, idx = blockIdx.x >> 3;
  {
    unsigned long long pc;
    asm volatile("s_getpc_b64 %0" : "=s"(pc));
    touch8k(smem_base + QH_OFF, (tid - 448) * 128u, mk_rsrc((const char*)(uintptr_t)(pc & ~255ull) + idx * 2048, idx < CODEPF / 2 ? 2048 : 0), 0);
  }
  const int bm = (xcd >> 2) * 8 + (idx & 7), bn = (xcd & 3) * 4 + (idx >> 3);
  const int cm = (xcd >> 1) * 4 + (idx & 3), cn = __builtin_amdgcn_readfirstlane((xcd & 1) * 8 + (idx >> 2));
  const int lane = tid & 63;
  Pre p;
  {
    const bool gv = ((bm + bn) & 1) == 0;
    unsigned oa = ((bm >> 2) * 4 + (wid & 3)) * 786432 + (bm & 2) * 1024 + (bn ^ 1) * 128, ob = (bm * 128 + (bn >> 1) * 16 + (wid & 3) * 4) * 2048;
    unsigned va = (lane >> 3) * (8 * 12288) + (lane & 7) * 16, vb = lane * 16;
    asm volatile("" : "+s"(oa), "+s"(ob));
    asm volatile("" : "+v"(va), "+v"(vb));
    p.o1 = gv ? oa : ob; p.n1 = gv ? 63 * 12288 + 128 : 8192; p.vo = gv ? va : vb; p.st = gv ? 12288 : 1024;
    dsw32<0>(smem_base + QT_OFF, 0u);
    dsw32<4>(smem_base + QT_OFF, 0u);
    asm volatile("" : "+v"(p.vo));
    asm volatile("s_waitcnt lgkmcnt(0)" : "+s"(ka), "+s"(p.o1), "+s"(p.n1), "+s"(p.st));
  }
  asm volatile("s_cmp_lt_u32 %0, 4\n\ts_cbranch_scc1 1\n\ts_barrier" ::"s"(wid) : "scc", "memory");
  const Args g = {(const bf16*)ka[0], (const bf16*)ka[1], (const bf16*)ka[2], (const bf16*)ka[4], (const bf16*)ka[5], (const bf16*)ka[6], (const bf16*)ka[7], (char*)ka[3]};
  char* ws = g.ws;
  const bf16 *x = g.x, *wg = g.wg, *wu = g.wu;
  bf16* O = (bf16*)(ws + WS_O);
  bf16* Y = (bf16*)(ws + WS_Y);
  unsigned* oflags = (unsigned*)(ws + WS_FO);
  float* stat = (float*)(ws + WS_STAT);
  unsigned tag = 0;
  if (__builtin_expect(wid < 4, 1)) {
    __builtin_amdgcn_s_setprio(2);
    tag = front1(smem, smem_base, lane, wid, bm, bn, g, p);
    front_h(smem, smem_base, lane, wid, bm, bn, g, tag);
    qkv_mfma(smem, smem_base, tid, wid, bm * 128, bn * 64, O, oflags, tag);
  } else if (wid < 7) {
    qkv_loader(smem, smem_base, lane, wid - 4, bm, bn, cn, g);
  } else {
    tag = qkv_spare(smem, smem_base, lane, bm, bn, cn, g);
  }
  STAMPT(2, 6, 320);
  STAMPT(3, 0, 448);
  o_phase(smem, smem_base, tid & 63, wid, cm, cn, O, oflags, x, Y, stat, ws + WS_TV, tag);
  g3_phase(smem, smem_base, tid, wid, Y, wg, wu, (bf16*)(ws + WS_ACT), stat, wd, (unsigned*)(ws + WS_FY), cm * 16 + cn, ws + WS_TC + cm * 12288, tag);
  STAMP(5, 3);
  STAMP(5, 4);
}

#define XST 0
#define G4NF 11
#define HFS 3
#define HFL 4
#define HCK 5
#define XMAP 1
template <int OFF>
DI void bstore_sc1(u32x4 d, unsigned voff, rsrc_t rs, unsigned soff) {
  asm volatile("buffer_store_dwordx4 %0, %1, %2, %3 offen offset:%4 sc1\n\ts_nop 1" ::"v"(d), "v"(voff), "s"(rs), "s"(soff), "i"(OFF) : "memory");
}
namespace g4 {
constexpr int NF = G4NF, NH = 28 - 2 * NF, NS = NF + NH, HEP = NH - 3;
constexpr int nslot(int g) { return g < NF ? 64 : 32; }
constexpr int wslot(int g) { return g < NF - 1 ? 48 : (g == NF - 1 ? 56 : 24); }
constexpr int ring(int g, int n) {
  if (g < NF - 1) {
    if (n >= 48 || n % 3 != 1) return -1;
    const int l = n / 3;
    if (g + 2 < NF) return (g + 2) * 16 + l;
    return l < 12 ? (g + 2) * 16 + (l < 8 ? l : l + 4) : -1;
  }
  if (g == NF - 1) { if (n >= 48 || n % 4 != 1) return -1; const int l = n / 4; return (g + 2) * 16 + (l < 8 ? l : l + 4); }
  if (g == NF) { if (n >= 24) return -1; const int l = n % 12; return (g + 2 + n / 12) * 16 + (l < 8 ? l : l + 4); }
  if (g + 3 >= NS || n >= 24 || (n & 1)) return -1;
  const int l = n >> 1;
  return (g + 3) * 16 + (l < 8 ? l : l + 4);
}
constexpr int pwrite(int g, int n) {
  const int m = g == NF - 1 ? n : 64 + n;
  for (int e = 0; e < 24; ++e) if (12 + (13 * e) / 4 == m) return e;
  return -1;
}
constexpr int red0(int n) { return n < 8 ? 0 : (64 * (n - 8)) / 24; }
#define SPS 4
constexpr int sslot(int f) {
  const int ready = (f >> 2) * 32 + 8 + (((f & 3) + 1) * 16 * 24 + 63) / 64;
  const int paced = (f / SPS) * 32 + 15 + (f % SPS) * (32 / SPS);
  int m = ready > paced ? ready : paced;
  if ((m & 1) == 0) ++m;
  return m;
}
constexpr int dstore_f(int g, int n) {
  for (int f = 0; f < 8; ++f) if (sslot(f) == (g - NF) * 32 + n) return f;
  return -1;
}
constexpr bool dstore(int g, int n) { return g >= NF && dstore_f(g, n) >= 0; }
constexpr int vmops(int g, int n) {
  int c = (ring(g, n) >= 0 ? 1 : 0) + (dstore(g, n) ? 1 : 0);
  const int h = g - NF;
  if (h == HFS && n == 24) c += 1;
  if (h == HFL && n == 25) c += 1;
  if (h == HCK && n >= 24) c += 1;
  if (h == HEP && n == 1) c += 1;
  if (h == HEP && n == 3) c += 4;
  return c;
}
constexpr int between(int g0, int n0, int g1, int n1) {
  int c = 0;
  for (int g = g0; g <= g1; ++g)
    for (int n = (g == g0 ? n0 + 1 : 0); n < (g == g1 ? n1 : nslot(g)); ++n) c += vmops(g, n);
  return c;
}
constexpr int imin(int a, int b) { return a < b ? a : b; }
constexpr int waitcnt(int g) {
  int c = 63;
  if (g + 1 < NS && g + 1 >= 2) {
    for (int s = 0; s <= g; ++s)
      for (int n = 0; n < nslot(s); ++n)
        if (ring(s, n) == (g + 1) * 16 + 15) c = imin(c, between(s, n, g, wslot(g)) + (dstore(s, n) ? 1 : 0));
  }
  if (g == 0) c = 16;
  if (g - NF == HFS) c = imin(c, between(NF + sslot(7) / 32, sslot(7) % 32, g, 24));
  if (g - NF == HCK) c = imin(c, between(NF + HFL, 25, g, 24));
  return c;
}
constexpr int bslot(int b) { return b < NF + 3 ? b % 3 : ((b - NF) % 4 == 3 ? 3 : (NF + (b - NF) % 4) % 3); }
}
__global__ __launch_bounds__(256, 1) void gemm4_kernel(const bf16* __restrict__ W, bf16* __restrict__ C, char* ws) {
  using namespace g4;
  const char* At = ws + WS_ACT;
  const char* Yt = ws + WS_Y;
  char* xbuf = ws + WS_X;
  unsigned* flags = (unsigned*)(ws + WS_F4);
  constexpr int KB = F_DIM / 32;
  constexpr int GT = 96 * 1024;
  __shared__ __attribute__((aligned(16))) char smem[GT + 1024];
  STAMPDECL;
  STAMP(6, 0);
  const int tid = threadIdx.x, lane = tid & 63;
  const int w = __builtin_amdgcn_readfirstlane(tid >> 6);
  const int xcd = blockIdx.x & 7, idx = blockIdx.x >> 3;
  const int kh = xcd & 1, bm = ((xcd >> 1) & 1) * 8 + (idx & 7), bn = (xcd >> 2) * 4 + (idx >> 3);
  const unsigned partner = blockIdx.x ^ 1;
  const int n0 = bn * 128, bm64 = bm * 2 + kh;
  const int fr = lane & 15, fq = lane >> 4;
  const rsrc_t rsA0 = mk_rsrc((const char*)At + (size_t)(bm64 ^ 1) * KB * 4096, KB * 4096);
  const rsrc_t rsA1 = mk_rsrc((const char*)At + (size_t)bm64 * KB * 4096, KB * 4096);
  const rsrc_t rsB = mk_rsrc((const char*)W + n0 * 2, KB * 65536 - n0 * 2);
  const unsigned voffL = lane * 16;
  unsigned voffA[4];
#pragma unroll
  for (int i = 0; i < 4; ++i) voffA[i] = voffL + ((i + w) & 3) * 1024;
  const unsigned voffB = fq * 8 * 2048 + fr * 16;
  u32x4 braw[4][8], afr[3][8], afa[4][4];
  u32x4 T[2][2];
  u32x4 xv[4], pv[8];
  unsigned gv, flg;
  f32x4 acc[8][8];
#pragma unroll
  for (int r = 0; r < 3; ++r)
#pragma unroll
    for (int j = 0; j < 8; ++j) asm volatile("" : "=v"(braw[r][j]), "=v"(afr[r][j]));
  const int kbF = (kh ? KB - 4 * NF : 0) + w, kbH = w;
#define ELOAD(B, L)                                                                                \
  do {                                                                                             \
    const unsigned kbv = (unsigned)(((B) < NF ? kbF : kbH) + (B) * 4);                             \
    if constexpr ((L) < 8) {                                                                       \
      const unsigned so = kbv * 65536u + ((L) >> 1) * 4096u;                                       \
      if constexpr (bslot(B) == 3 || (B) >= NF + 3) asm volatile("" : "=v"(braw[bslot(B)][(L) & 7]));        \
      if constexpr ((L) & 1) bload_v<2048>(braw[bslot(B)][(L) & 7], voffB, rsB, so);               \
      else bload_v<0>(braw[bslot(B)][(L) & 7], voffB, rsB, so);                                    \
    } else if constexpr ((B) >= NF + 2) {                                                          \
      asm volatile("" : "=a"(afa[((B) - NF) & 3][(L) & 3]));                                       \
      bload_a<0>(afa[((B) - NF) & 3][(L) & 3], voffA[(L) & 3], rsA1, kbv * 4096u);                 \
    } else {                                                                                       \
      bload_v<0>(afr[(B) % 3][(L) & 7], voffA[(L) & 3], ((L) & 4) ? rsA1 : rsA0, kbv * 4096u);      \
    }                                                                                              \
  } while (0)
#define DPERM(TB, Q, SL, PP)                                                                       \
  do {                                                                                             \
    if ((Q) & 1) PERM_HI(T[TB][1][(Q) >> 1], braw[SL][((Q) >> 1) * 2][PP], braw[SL][((Q) >> 1) * 2 + 1][PP]); \
    else PERM_LO(T[TB][0][(Q) >> 1], braw[SL][((Q) >> 1) * 2][PP], braw[SL][((Q) >> 1) * 2 + 1][PP]);         \
  } while (0)

#define MFMA_E(C, B, A) asm volatile("v_mfma_f32_16x16x32_bf16 %0, %1, %2, %0" : "+a"(C) : "v"(B), "v"(A))
  static_for<16>([&](auto L) { ELOAD(0, L.value); });
  static_for<16>([&](auto L) { ELOAD(1, L.value); });
#pragma unroll
  for (int i = 0; i < 8; ++i)
#pragma unroll
    for (int f = 0; f < 8; ++f) { acc[i][f] = f32x4{0, 0, 0, 0}; asm volatile("" : "+a"(acc[i][f])); }
  const unsigned sbase = (unsigned)(uintptr_t)smem + voffL;
  unsigned wlo[3];
#pragma unroll
  for (int i = 1; i < 4; ++i) wlo[i - 1] = sbase + (((w + i) & 3) * 3 + (i - 1)) * 8192;
  const unsigned rlo = sbase + w * 24576;
  const rsrc_t rsEx = mk_rsrc(xbuf, 256 * 32768);
  const unsigned exo = (unsigned)(blockIdx.x * 32768 + w * 8192);
  const rsrc_t rsFo = mk_rsrc(flags + blockIdx.x * 4 + w, 4), rsFi = mk_rsrc(flags + partner * 4 + w, 4);
  wait_vm<16>();
  STAMPNW(6, 1);
#pragma unroll
  for (int q = 0; q < 8; ++q) DPERM(0, q, 0, 0);
  asm volatile("s_nop 1");
  u32x4 pr[3][4];
  f32x4 tt[8], uu;
  static_for<NS>([&](auto G) {
    constexpr int g = G.value, h = g - NF, sl = bslot(g), ns = bslot(g + 1);
    constexpr bool full = g < NF - 1, lastf = g == NF - 1;
    constexpr bool red = (h == 0 || h == 1);
    constexpr int fb = h == 1 ? 4 : 0;
    if constexpr (h == 0 || h == 1) { WAIT_LGKM0(); __builtin_amdgcn_s_barrier(); }
    if constexpr (h == 3) __builtin_amdgcn_s_barrier();
    static_for<nslot(g)>([&](auto N) {
      constexpr int n = N.value;
      if constexpr (n == wslot(g)) {
        if constexpr (waitcnt(g) < 63) {
          if constexpr (h == HCK) asm volatile("s_waitcnt vmcnt(%1)" : "+v"(flg) : "n"(waitcnt(g)) : "memory");
          else wait_vm<waitcnt(g)>();
        }
        if constexpr (h == HFS) asm volatile("buffer_store_dword %0, %1, %2, 0 offen sc1" ::"v"(1u), "v"(voffL >> 2), "s"(rsFo) : "memory");
        if constexpr (h == HCK) {
          unsigned spins = 0;
          while (__builtin_amdgcn_readfirstlane(flg) == 0u && spins < (1u << 20)) {
            __builtin_amdgcn_s_sleep(1);
            bload1_sc1(flg, 0u, rsFi);
            asm volatile("s_waitcnt vmcnt(0)" : "+v"(flg)::"memory");
            ++spins;
          }
        }
      }
      if constexpr (red && n == 8) WAIT_LGKM0();
      if constexpr (full) {
        constexpr int p = n >> 4, q = n & 15;
        MFMA_E(acc[q & 7][2 * p + (q >> 3)], T[p & 1][q >> 3], afr[g % 3][q & 7]);
        if constexpr ((q & 1) == 0) {
          if constexpr (p < 3) DPERM((p + 1) & 1, q >> 1, sl, p + 1);
          else DPERM(0, q >> 1, ns, 0);
        }
      } else {
        constexpr int p = (n >> 3) & 3, q = n & 7, ib = (lastf && n < 32) ? 0 : 4;
        if constexpr (h >= 2) MFMA_A(acc[ib + (q & 3)][2 * p + (q >> 2)], T[p & 1][q >> 2], afa[h & 3][q & 3]);
        else MFMA_E(acc[ib + (q & 3)][2 * p + (q >> 2)], T[p & 1][q >> 2], afr[g % 3][ib + (q & 3)]);
        if constexpr (p < 3) DPERM((p + 1) & 1, q, sl, p + 1);
        else if constexpr (ib == 0) DPERM(0, q, sl, 0);
        else if constexpr (g + 1 < NS) DPERM(0, q, ns, 0);
      }
      if constexpr (ring(g, n) >= 0) ELOAD((ring(g, n) >> 4), (ring(g, n) & 15));
      if constexpr ((lastf || h == 0) && pwrite(g, n) >= 0) {
        constexpr int e = pwrite(g, n), i = 1 + e % 3, f = e / 3;
        dsw128a<f * 1024>(wlo[i - 1], acc[i][f]);
      }
      if constexpr (red) {
        if constexpr (n < 6) dsr128<(n % 3) * 8192 + (fb + n / 3) * 1024>(pr[n % 3][n / 3], rlo);
        if constexpr (n == 6 || n == 7) {
          static_for<3>([&](auto E) { dsr128<E.value * 8192 + (fb + n - 4) * 1024>(pr[E.value][n - 4], rlo); });
        }
        if constexpr (n >= 8) {
          constexpr int o0 = red0(n), o1 = red0(n + 1);
          static_for<o1 - o0>([&](auto O) {
            constexpr int o = o0 + O.value, fl = o / 16, k = o % 16, r = k & 3;
            (void)&uu; (void)&tt; (void)&pr;
            if constexpr (k < 4) asm volatile("v_accvgpr_read_b32 %0, %1" : "=v"(tt[fb + fl][r]) : "a"(acc[0][fb + fl][r]));
            else if constexpr (k < 8) asm volatile("v_add_f32 %0, %0, %1" : "+v"(tt[fb + fl][r]) : "v"(pr[0][fl][r]));
            else if constexpr (k < 12) asm volatile("v_add_f32 %0, %1, %2" : "=v"(uu[r]) : "v"(pr[1][fl][r]), "v"(pr[2][fl][r]));
            else asm volatile("v_add_f32 %0, %0, %1" : "+v"(tt[fb + fl][r]) : "v"(uu[r]));
          });
        }
      }
      if constexpr (h >= 0 && dstore(g, n)) {
        constexpr int f = dstore_f(g, n);
        bstore_sc1<(f & 3) * 1024>(__builtin_bit_cast(u32x4, tt[f]), voffL, rsEx, exo + (f >> 2) * 4096);
      }
      if constexpr (h == HFL && n == 25) {
        asm volatile("" : "=v"(flg));
        bload1_sc1(flg, 0u, rsFi);
      }
      if constexpr (h == HCK && n >= 24) {
        constexpr int f = n - 24;
        asm volatile("" : "=v"(pv[f]));
        bload_sc1<(f & 3) * 1024>(pv[f], voffL, rsEx, partner * 32768u + w * 8192u + (f >> 2) * 4096u);
      }
      if constexpr (h == HEP && n == 1) {
        asm volatile("" : "=v"(gv));
        bload1_v<0>(gv, voffL >> 2, mk_rsrc(ws + WS_TV + (bn * 2 + (w & 1)) * 1024 + 512 + (w >> 1) * 256, 256), 0);
      }
      if constexpr (h == HEP && n == 3) {
        const rsrc_t rsX = mk_rsrc(Yt, S_LEN * D_DIM * 2);
        const unsigned vx = (unsigned)(((bm64 >> 2) * 32 + bn * 4 + fq) * 16384 + ((bm64 & 3) * 4 + w) * 1024 + fr * 16);
#pragma unroll
        for (int r = 0; r < 4; ++r) asm volatile("" : "=v"(xv[r]));
        bload_v<0>(xv[0], vx, rsX, 0); bload_v<256>(xv[1], vx, rsX, 0); bload_v<512>(xv[2], vx, rsX, 0); bload_v<768>(xv[3], vx, rsX, 0);
      }
      if constexpr (g == NS - 1 && n >= 8 && (n & 7) >= 2) {
        constexpr int e = (n & 7) - 2, i = 1 + e % 3, f = 2 * ((n >> 3) - 1) + e / 3;
        dsw128a<f * 1024>(wlo[i - 1], acc[4 + i][f]);
      }
    });
  });
  asm volatile("s_nop 15" ::: "memory");
#pragma unroll
  for (int f = 0; f < 8; ++f) asm volatile("" : "+a"(acc[4][f]));
  static_for<6>([&](auto E) { constexpr int i = 1 + E.value % 3, f = 6 + E.value / 3; dsw128a<f * 1024>(wlo[i - 1], acc[4 + i][f]); });
  asm volatile("s_waitcnt vmcnt(%5)" : "+v"(xv[0]), "+v"(xv[1]), "+v"(xv[2]), "+v"(xv[3]), "+v"(gv) : "n"(between(NF + HEP, 3, NS - 1, 32)) : "memory");
  STAMP(6, 2); STAMPFLUSH(6, 1);
  float* gtab = reinterpret_cast<float*>(smem + GT);
  {
    const int col = tid & 127;
    gtab[(tid & 128) + (col & 96) + (col & 7) * 4 + ((col >> 3) & 3)] = __uint_as_float(gv);
  }
  asm volatile("s_waitcnt lgkmcnt(0)" ::: "memory");
  __builtin_amdgcn_s_barrier();
  u32x4 pq[3][8], gt[8], ct[8];
  f32x4 fin[8];
  {
    const unsigned gsrc = (unsigned)(uintptr_t)smem + GT + fq * 128;
    static_for<24>([&](auto E) { dsr128<E.value * 1024>(pq[E.value >> 3][E.value & 7], rlo); });
    static_for<8>([&](auto F) { dsr128<F.value * 16>(gt[F.value], gsrc); });
    static_for<8>([&](auto F) { dsr128<512 + F.value * 16>(ct[F.value], gsrc); });
#pragma unroll
    for (int f = 0; f < 8; ++f) { fin[f] = acc[4][f]; asm volatile("" : "+v"(fin[f])); }
    asm volatile("s_waitcnt lgkmcnt(0)" ::: "memory");
#pragma unroll
    for (int f = 0; f < 8; ++f) asm volatile("" : "+v"(pq[0][f]), "+v"(pq[1][f]), "+v"(pq[2][f]), "+v"(gt[f]), "+v"(ct[f]));
  }
#pragma unroll
  for (int f = 0; f < 8; ++f) {
    fin[f] = (fin[f] + __builtin_bit_cast(f32x4, pq[0][f])) + (__builtin_bit_cast(f32x4, pq[1][f]) + __builtin_bit_cast(f32x4, pq[2][f]));
    asm volatile("" : "+v"(fin[f]));
  }
  asm volatile("s_waitcnt vmcnt(0)" : "+v"(pv[0]), "+v"(pv[1]), "+v"(pv[2]), "+v"(pv[3]), "+v"(pv[4]), "+v"(pv[5]), "+v"(pv[6]), "+v"(pv[7])::"memory");
  STAMP(6, 6);
#pragma unroll
  for (int f = 0; f < 8; ++f) {
    fin[f] += __builtin_bit_cast(f32x4, pv[f]);
    f32x4 xf;
#pragma unroll
    for (int r = 0; r < 4; ++r) xf[r] = (f & 1) ? bf_hi(xv[r][f >> 1]) : bf_lo(xv[r][f >> 1]);
    xf *= __builtin_bit_cast(f32x4, ct[f]);
    fin[f] = xf + __builtin_bit_cast(f32x4, gt[f]) * fin[f];
  }
  {
    char* tb = smem + w * 24576;
#pragma unroll
    for (int r = 0; r < 4; ++r) {
      const u32x4 ov = {pk_bf16(fin[0][r], fin[1][r]), pk_bf16(fin[2][r], fin[3][r]), pk_bf16(fin[4][r], fin[5][r]), pk_bf16(fin[6][r], fin[7][r])};
      *reinterpret_cast<u32x4*>(tb + fr * 256 + (((fq * 4 + r) ^ (fr & 7)) * 16)) = ov;
    }
#pragma unroll
    for (int j = 0; j < 4; ++j) {
      const int row = j * 4 + fq;
      const u32x4 ov = *reinterpret_cast<const u32x4*>(tb + row * 256 + ((fr ^ (row & 7)) * 16));
      gst128(C + (size_t)(bm64 * 64 + w * 16 + row) * D_DIM + n0 + fr * 8, ov);
    }
  }
  STAMP(6, 3);
  STAMP(6, 4);
}

extern "C" void kernel_launch(void* const* d_in, const int* in_sizes, int n_in, void* d_out, int out_size,
                              void* d_ws, size_t ws_size, hipStream_t stream) {
  const bf16* x = (const bf16*)d_in[0];
  const bf16* wqkv = (const bf16*)d_in[1];
  const bf16* wo = (const bf16*)d_in[2];
  const bf16* wg = (const bf16*)d_in[4];
  const bf16* wu = (const bf16*)d_in[5];
  const bf16* wd = (const bf16*)d_in[6];
  const bf16* c = (const bf16*)d_in[7];
  const bf16* wmod = (const bf16*)d_in[8];
  bf16* out = (bf16*)d_out;

  char* ws = (char*)d_ws;
  block_kernel<<<256, 512, 0, stream>>>(x, c, wmod, ws, wqkv, wo, wg, wu, wd);
  gemm4_kernel<<<256, 256, 0, stream>>>(wd, out, ws);
}
```

```cpp
#include <hip/hip_runtime.h>
#include <hip/hip_bf16.h>
#include <stdint.h>

typedef __hip_bfloat16 bf16;
typedef __attribute__((ext_vector_type(8))) short bf16x8;
typedef __attribute__((ext_vector_type(4))) float f32x4;
typedef __attribute__((ext_vector_type(2))) float f32x2;
typedef __attribute__((ext_vector_type(4))) unsigned u32x4;
typedef __attribute__((ext_vector_type(2))) unsigned u32x2;
typedef __attribute__((ext_vector_type(2))) __bf16 bf16x2_t;
typedef __attribute__((ext_vector_type(8))) unsigned long long u64x8;
typedef __amdgpu_buffer_rsrc_t rsrc_t;

#define DI __device__ __forceinline__
#define QBAR 2
#define TQ 8
#define DWO 6
#define DW0 20
#define DWS 4
#define OFS 4
#define ORP 1
#define YFS 32
#define YSL 6
#define CODESZ 75300

#define PW 0
#define STAMP(k, s) do { } while (0)
#define STAMPT(k, s, T) do { } while (0)
#define STAMPNW(k, s)
#define STAMPFLUSH(k, s)
#define STAMPDECL

constexpr int S_LEN = 2048;
constexpr int D_DIM = 1024;
constexpr int F_DIM = 3584;

template <int V> struct ic { static constexpr int value = V; };
template <int N, int I = 0, class F>
DI void static_for(F&& f) {
  if constexpr (I < N) { f(ic<I>{}); static_for<N, I + 1>(f); }
}

DI float bf_lo(unsigned u) { return __uint_as_float(u << 16); }
DI float bf_hi(unsigned u) { return __uint_as_float(u & 0xffff0000u); }
DI unsigned pk_bf16(float lo, float hi) {
  const f32x2 v = {lo, hi};
  return __builtin_bit_cast(unsigned, __builtin_convertvector(v, bf16x2_t));
}
DI rsrc_t mk_rsrc(const void* p, unsigned bytes) { return __builtin_amdgcn_make_buffer_rsrc((void*)p, (short)0, (int)bytes, 0x00020000); }

template <int N>
DI void wait_vm() { asm volatile("s_waitcnt vmcnt(%0)" ::"n"(N > 63 ? 63 : N) : "memory"); }
#define WAIT_LGKM0() do { asm volatile("s_waitcnt lgkmcnt(0)" ::: "memory"); __builtin_amdgcn_sched_barrier(0); } while (0)
#define SCHED0() __builtin_amdgcn_sched_barrier(0)
#define ALIGN(M) do { if ((M) == 1) asm volatile(".p2align 3"); if ((M) == 2) asm volatile(".p2align 3\n\ts_nop 0"); } while (0)
#define DAL 1
#define GAL 0
#define QAL 0

#define MFMA_A(C, B, A) asm volatile("v_mfma_f32_16x16x32_bf16 %0, %1, %2, %0" : "+a"(C) : "v"(B), "a"(A))
#define MFMA_AV(C, B, A) asm volatile("v_mfma_f32_16x16x32_bf16 %0, %1, %2, %0" : "+v"(C) : "v"(B), "v"(A))
#define PERM_LO(D, X, Y) asm volatile("v_perm_b32 %0, %1, %2, %3" : "=v"(D) : "v"(Y), "v"(X), "s"(0x05040100u))
#define PERM_HI(D, X, Y) asm volatile("v_perm_b32 %0, %1, %2, %3" : "=v"(D) : "v"(Y), "v"(X), "s"(0x07060302u))
template <int OFF>
DI void bload_v(u32x4& d, unsigned voff, rsrc_t rs, unsigned soff) {
  asm volatile("buffer_load_dwordx4 %0, %1, %2, %3 offen offset:%4" : "+v"(d) : "v"(voff), "s"(rs), "s"(soff), "i"(OFF) : "memory");
}
template <int OFF>
DI void bload_a(u32x4& d, unsigned voff, rsrc_t rs, unsigned soff) {
  asm volatile("buffer_load_dwordx4 %0, %1, %2, %3 offen offset:%4" : "+a"(d) : "v"(voff), "s"(rs), "s"(soff), "i"(OFF) : "memory");
}
template <int OFF>
DI void bload2_v(u32x2& d, unsigned voff, rsrc_t rs, unsigned soff) {
  asm volatile("buffer_load_dwordx2 %0, %1, %2, %3 offen offset:%4" : "+v"(d) : "v"(voff), "s"(rs), "s"(soff), "i"(OFF) : "memory");
}
template <int OFF>
DI void bload1_v(unsigned& d, unsigned voff, rsrc_t rs, unsigned soff) {
  asm volatile("buffer_load_dword %0, %1, %2, %3 offen offset:%4" : "+v"(d) : "v"(voff), "s"(rs), "s"(soff), "i"(OFF) : "memory");
}
template <int OFF>
DI void bload_lds(unsigned m0v, unsigned voff, rsrc_t rs, unsigned soff) {
  asm volatile("s_mov_b32 m0, %0\n\ts_nop 0\n\tbuffer_load_dwordx4 %1, %2, %3 offen offset:%4 lds" ::"s"(m0v), "v"(voff), "s"(rs), "s"(soff), "i"(OFF) : "memory");
}
DI void touch8k(unsigned m0v, unsigned voff, rsrc_t rs, unsigned soff) {
  asm volatile("s_mov_b32 m0, %0\n\ts_nop 0\n\tbuffer_load_dword %1, %2, %3 offen lds" ::"s"(m0v), "v"(voff), "s"(rs), "s"(soff) : "memory");
}
template <int OFF>
DI void bload_lds_sc1(unsigned m0v, unsigned voff, rsrc_t rs, unsigned soff) {
  asm volatile("s_mov_b32 m0, %0\n\ts_nop 0\n\tbuffer_load_dwordx4 %1, %2, %3 offen offset:%4 sc1 lds" ::"s"(m0v), "v"(voff), "s"(rs), "s"(soff), "i"(OFF) : "memory");
}
template <int OFF>
DI void dsr128(u32x4& d, unsigned addr) { asm volatile("ds_read_b128 %0, %1 offset:%2" : "=v"(d) : "v"(addr), "i"(OFF) : "memory"); }
template <int OFF>
DI void dsr64(u32x2& d, unsigned addr) { asm volatile("ds_read_b64 %0, %1 offset:%2" : "=v"(d) : "v"(addr), "i"(OFF) : "memory"); }
template <int OFF>
DI void dsr_tr(u32x2& d, unsigned addr) { asm volatile("ds_read_b64_tr_b16 %0, %1 offset:%2" : "=v"(d) : "v"(addr), "i"(OFF) : "memory"); }
template <int OFF>
DI void dsw128(unsigned addr, u32x4 d) { asm volatile("ds_write_b128 %0, %1 offset:%2" ::"v"(addr), "v"(d), "i"(OFF) : "memory"); }
template <int OFF>
DI void dsw128a(unsigned addr, const f32x4& d) { asm volatile("ds_write_b128 %0, %1 offset:%2" ::"v"(addr), "a"(d), "i"(OFF) : "memory"); }
template <int OFF>
DI void dsw32(unsigned addr, unsigned d) { asm volatile("ds_write_b32 %0, %1 offset:%2" ::"v"(addr), "v"(d), "i"(OFF) : "memory"); }

DI void gst128(void* p, u32x4 v) {
  asm volatile("global_store_dwordx4 %0, %1, off sc1\n\ts_nop 1" ::"v"(p), "v"(v) : "memory");
}
DI f32x2 lo2(f32x4 v) { return __builtin_shufflevector(v, v, 0, 1); }
DI f32x2 hi2(f32x4 v) { return __builtin_shufflevector(v, v, 2, 3); }
DI float rowsum4(float v) {
  const unsigned u = __float_as_uint(v);
  const auto a = __builtin_amdgcn_permlane16_swap(u, u, false, false);
  const unsigned t = __float_as_uint(__uint_as_float(a[0]) + __uint_as_float(a[1]));
  const auto b = __builtin_amdgcn_permlane32_swap(t, t, false, false);
  return __uint_as_float(b[0]) + __uint_as_float(b[1]);
}
DI void split4(const f32x4 v, u32x2& hi, u32x2& lo) {
  hi[0] = pk_bf16(v[0], v[1]); hi[1] = pk_bf16(v[2], v[3]);
  lo[0] = pk_bf16(v[0] - bf_lo(hi[0]), v[1] - bf_hi(hi[0]));
  lo[1] = pk_bf16(v[2] - bf_lo(hi[1]), v[3] - bf_hi(hi[1]));
}
DI int slow_col(const void* p) { return (int)((3u - ((unsigned)(uintptr_t)p >> 7)) & 7u); }
DI float silu_f(float v) { return v / (1.0f + __expf(-v)); }

constexpr unsigned WS_G1M = 0x20000;
constexpr unsigned WS_G1S = 0x40000;
constexpr unsigned WS_G2 = 0x60000;
constexpr unsigned WS_STAT = 0x80000;
constexpr unsigned WS_FO = 0x141000;
constexpr unsigned WS_FC = 0x146000;
constexpr unsigned WS_FY = 0x148000;
constexpr unsigned WS_FH = 0x188000;
constexpr unsigned WS_FA = 0x1C8000;
constexpr unsigned WS_WC = 2u << 20;
constexpr unsigned WS_H = 4u << 20, WS_O = 8u << 20, WS_Y = 12u << 20, WS_ACT = 16u << 20, WS_X = 32u << 20, WS_X2 = 36u << 20;

template <int CTRL>
DI float dpp_get(float b) { return __uint_as_float(__builtin_amdgcn_update_dpp(0u, __float_as_uint(b), CTRL, 0xf, 0xf, true)); }
DI float swap32_add(float a, float b) {
  const auto sw = __builtin_amdgcn_permlane32_swap(__float_as_uint(a), __float_as_uint(b), false, false);
  return __uint_as_float(sw[0]) + __uint_as_float(sw[1]);
}
DI float swap16_add(float a, float b) {
  const auto sw = __builtin_amdgcn_permlane16_swap(__float_as_uint(a), __float_as_uint(b), false, false);
  return __uint_as_float(sw[0]) + __uint_as_float(sw[1]);
}
DI float scatter8(const f32x2 (&a)[4], int lane) {
  const float t0 = swap32_add(a[0][0], a[2][0]), t1 = swap32_add(a[0][1], a[2][1]), t2 = swap32_add(a[1][0], a[3][0]), t3 = swap32_add(a[1][1], a[3][1]);
  const float u0 = swap16_add(t0, t2), u1 = swap16_add(t1, t3);
  const bool b3 = lane & 8;
  return (b3 ? u1 : u0) + dpp_get<0x128>(b3 ? u0 : u1);
}
DI float sum4(const f32x2 (&a)[4]) {
  float t = swap16_add(swap32_add(a[0][0] + a[0][1], a[2][0] + a[2][1]), swap32_add(a[1][0] + a[1][1], a[3][0] + a[3][1]));
  t += dpp_get<0x128>(t); t += dpp_get<0x124>(t); t += dpp_get<0x122>(t); t += dpp_get<0x121>(t);
  return t;
}
DI int lane_id() {
  int l;
  asm volatile("v_mbcnt_lo_u32_b32 %0, -1, 0\n\tv_mbcnt_hi_u32_b32 %0, -1, %0" : "=v"(l));
  return l;
}
DI f32x2 unpk(unsigned u) { return f32x2{bf_lo(u), bf_hi(u)}; }
DI f32x2 unpk_v(unsigned u) {
  f32x2 r;
  asm volatile("v_lshlrev_b32 %0, 16, %1" : "=v"(r[0]) : "v"(u));
  asm volatile("v_and_b32 %0, 0xffff0000, %1" : "=v"(r[1]) : "v"(u));
  return r;
}

constexpr int Q_SLOT = 21504, Q_NSL = 7;
constexpr int WO_H0 = 4 * Q_SLOT;
constexpr int QX_OFF = Q_SLOT * Q_NSL + 1024;
constexpr int QH_OFF = QX_OFF + 8192;
constexpr int QT_OFF = QH_OFF + 1024;
constexpr int Q_LDS = 163840;
#define C3Q 1
#define C3O 1
#define POLLW 0
#define OD 3
#define SSL 14
#define SSI 4
#define OMB 6
template <int OFF>
DI void bload_sc1(u32x4& d, unsigned voff, rsrc_t rs, unsigned soff) {
  asm volatile("buffer_load_dwordx4 %0, %1, %2, %3 offen offset:%4 sc1" : "+v"(d) : "v"(voff), "s"(rs), "s"(soff), "i"(OFF) : "memory");
}
DI void bload2_sc1(u32x2& d, unsigned voff, rsrc_t rs, unsigned soff) {
  asm volatile("buffer_load_dwordx2 %0, %1, %2, %3 offen sc1" : "+v"(d) : "v"(voff), "s"(rs), "s"(soff) : "memory");
}
DI void bload1_sc1(unsigned& d, unsigned voff, rsrc_t rs) {
  asm volatile("buffer_load_dword %0, %1, %2, 0 offen sc1" : "+v"(d) : "v"(voff), "s"(rs) : "memory");
}
DI unsigned wo_voff(int lane, int odd, int sh) {
  const int q = (lane >> 3) & 3, h = lane >> 5, ch = lane & 7, g = (q >> 1) | (odd << 1);
  return (unsigned)(((h * 16 + q) << sh) + ((((ch >> 1) ^ g) * 16) + (ch & 1) * 8) * 2);
}
DI rsrc_t wo_rsrc(const bf16* wo, const char* wc, int cn) {
  const bool c3 = C3O && (cn & 7) == slow_col(wo);
  return mk_rsrc(c3 ? wc + 786432 + (cn >> 3) * 131072 : (const char*)wo + cn * 128, c3 ? 131072 : D_DIM * D_DIM * 2 - cn * 128);
}

#define QORD 1
#define DORD 2
#define XCL 2
#define XCM 0x11110u
#define XCS 1
#define XCP 6
#define QY 26
#define QBAR0 0
#define XWO 1
DI unsigned xoff(unsigned b) { return XCL == 1 && XWO ? WS_O + ((b >> 3) & 7) * 524288 + ((b & 7) * 4 + (b >> 6)) * 16384 : WS_X + b * 16384; }
struct Args { const bf16 *x, *c, *wmod, *wq, *wo, *wg, *wu; char* ws; };
#define CK 3
#define TW 8
#define TO 4
#define TT 2
#define PSL 8
#define PSI 4
DI void gstore2_sc1(u32x2 d, unsigned voff, rsrc_t rs) {
  asm volatile("buffer_store_dwordx2 %0, %1, %2, 0 offen sc1" ::"v"(d), "v"(voff), "s"(rs) : "memory");
}
DI void gstore4_sc1(u32x4 d, unsigned voff, rsrc_t rs, unsigned soff) {
  asm volatile("buffer_store_dwordx4 %0, %1, %2, %3 offen sc1\n\ts_nop 1" ::"v"(d), "v"(voff), "s"(rs), "s"(soff) : "memory");
}
DI void bload_us(unsigned& d, unsigned voff, rsrc_t rs, unsigned soff) {
  asm volatile("buffer_load_ushort %0, %1, %2, %3 offen" : "+v"(d) : "v"(voff), "s"(rs), "s"(soff) : "memory");
}
template <int W0, int W1>
DI void gemv8(float* red, int lane, const u32x4 (&d)[8], unsigned cv) {
  wait_vm<W0>();
  const unsigned sv = __float_as_uint(silu_f(unpk_v(cv)[0]));
  f32x2 a[4] = {{0, 0}, {0, 0}, {0, 0}, {0, 0}};
  static_for<8>([&](auto J) {
    constexpr int j = J.value;
    if constexpr (j == 4 && W1 != W0) wait_vm<W1>();
    const float s = __uint_as_float(__builtin_amdgcn_update_dpp(__builtin_amdgcn_update_dpp(0u, sv, 0x150 + j, 0xf, 0x3, false), sv, 0x158 + j, 0xf, 0xc, false));
#pragma unroll
    for (int e = 0; e < 4; ++e) a[e] += s * unpk_v(d[j][e]);
  });
  red[(lane & 7) * 8 + (lane >> 3)] = scatter8(a, lane);
}
struct Pre { unsigned o1, n1, vo, st; };
DI unsigned front1(char* smem, unsigned smem_base, int lane, int w4, int bm, int bn, const Args& g, const Pre& p) {
  const bool gv = ((bm + bn) & 1) == 0;
  u32x4 d[8];
  unsigned tag, cv;
  asm volatile("" : "=v"(d[0]), "=v"(d[1]), "=v"(d[2]), "=v"(d[3]), "=v"(d[4]), "=v"(d[5]), "=v"(d[6]), "=v"(d[7]), "=v"(tag), "=v"(cv));
  {
    const rsrc_t rs = mk_rsrc((const char*)(gv ? g.wmod : g.x) + p.o1, p.n1), rsC = mk_rsrc(g.c, 2048);
#pragma unroll
    for (int i = 0; i < 4; ++i) bload_v<0>(d[i], p.vo, rs, i * p.st);
    bload_us(cv, lane * 2, rsC, (bm >> 2) * 512 + w4 * 128);
    bload1_v<0>(tag, 0u, mk_rsrc(g.ws, 4), 0);
#pragma unroll
    for (int i = 4; i < 8; ++i) bload_v<0>(d[i], p.vo, rs, i * p.st);
  }
  STAMPT(0, 6, 192);
  __builtin_amdgcn_s_barrier();
  float* red = reinterpret_cast<float*>(smem + QX_OFF);
  if (gv) {
    gemv8<4, 0>(red + w4 * 64, lane, d, cv);
  } else {
    f32x2 v[4][8], t[4];
    asm volatile("s_waitcnt vmcnt(4)" ::"v"(cv) : "memory");
#pragma unroll
    for (int r = 0; r < 4; ++r) {
      if (r == 2) wait_vm<0>();
#pragma unroll
      for (int e = 0; e < 8; ++e) v[r][e] = unpk_v(d[r * 2 + (e >> 2)][e & 3]);
      t[r] = ((v[r][0] + v[r][1]) + (v[r][2] + v[r][3])) + ((v[r][4] + v[r][5]) + (v[r][6] + v[r][7]));
    }
    const float mean = sum4(t) * (1.0f / D_DIM);
#pragma unroll
    for (int r = 0; r < 4; ++r) {
      const float m = __uint_as_float(__builtin_amdgcn_readlane(__float_as_uint(mean), r * 16));
      f32x2 q0 = {0, 0}, q1 = {0, 0};
#pragma unroll
      for (int e = 0; e < 8; e += 2) { const f32x2 e0 = v[r][e] - m, e1 = v[r][e + 1] - m; q0 += e0 * e0; q1 += e1 * e1; }
      t[r] = q0 + q1;
    }
    const float rstd = rsqrtf(sum4(t) * (1.0f / D_DIM) + 1e-5f);
    if ((lane & 15) == 0) *reinterpret_cast<f32x2*>(red + w4 * 8 + (lane >> 3)) = f32x2{rstd, -mean * rstd};
  }
  asm volatile("ds_add_u32 %1, %2" : "+v"(tag) : "v"(smem_base + QT_OFF), "v"(1u) : "memory");
  STAMPT(0, 1, 192);
  tag = __builtin_amdgcn_readfirstlane(tag) + 0x9E3779B9u;
  const unsigned o = gv ? WS_G1M + (w4 >> 1) * 65536 + ((bm & 2) * 2 + (bm >> 2)) * 8192 + (bn ^ 1) * 512 + (w4 & 1) * 256 : WS_G1S + w4 * 32768 + (bm * 128 + (bn >> 1) * 16) * 16;
  const rsrc_t rsG = mk_rsrc(g.ws + o, 256);
  const f32x2* rp = reinterpret_cast<const f32x2*>(red) + (lane & 15) + (gv ? (w4 & 1) * 16 : 0);
  unsigned n;
  do {
    asm volatile("ds_read_b32 %0, %1\n\ts_waitcnt lgkmcnt(0)" : "=v"(n) : "v"(smem_base + QT_OFF) : "memory");
  } while (__builtin_amdgcn_readfirstlane(n) != 256u);
  f32x2 m = rp[0];
  const f32x2 m2 = (m + rp[32]) + (rp[64] + rp[96]);
  m = gv ? m2 : m;
  gstore4_sc1(u32x4{__float_as_uint(m[0]), tag, __float_as_uint(m[1]), tag}, lane * 16, rsG, 0);
  STAMPT(0, 2, 192);
  return tag;
}
DI void front_ocld(int lane, int w4, int bm, int bn, const Args& g, u32x4 (&oc)[8], unsigned& c2) {
  const rsrc_t rs = mk_rsrc((const char*)g.wmod + ((bm & 3) * 4 + w4) * 786432 + 4096 + (bm >> 2) * 2048 + (bn ^ 2) * 128, 63 * 12288 + 128);
  asm volatile("" : "=v"(c2));
  bload_us(c2, lane * 2, mk_rsrc(g.c, 2048), (bm & 3) * 512 + w4 * 128);
#pragma unroll
  for (int i = 0; i < 8; ++i) {
    wait_vm<TO - 1>();
    asm volatile("" : "=v"(oc[i]));
    bload_v<0>(oc[i], (lane >> 3) * (8 * 12288) + (lane & 7) * 16, rs, i * 12288);
  }
}
template <int NW>
DI void front_oc(char* smem, int lane, int w4, const u32x4 (&oc)[8], unsigned c2) {
  gemv8<NW, NW>(reinterpret_cast<float*>(smem + QX_OFF + 1024) + w4 * 64, lane, oc, c2);
  asm volatile("s_waitcnt lgkmcnt(0)" ::: "memory");
}

DI void front_h(char* smem, unsigned smem_base, int lane, int wid, int bm, int bn, const Args& g, unsigned tag) {
  const int fr = lane & 15, fq = lane >> 4;
  const int kA = ((bn ^ 3) * 2) ^ (wid & 1), kB = ((bn ^ 3) * 2) ^ (2 + wid), f0 = (wid >> 1) * 4;
  u32x4 xa[5], xb[9];
  const unsigned hc = bm == 0 ? 1u : 0u;
  const unsigned la = smem_base + (wid & 1) * Q_SLOT + f0 * 1024 + lane * 16, lb = smem_base + (2 + wid) * Q_SLOT + lane * 16;
  {
    unsigned n;
    do {
      asm volatile("ds_read_b32 %0, %1 offset:4\n\ts_waitcnt lgkmcnt(0)" : "=v"(n) : "v"(smem_base + QT_OFF) : "memory");
    } while (__builtin_amdgcn_readfirstlane(n) != 192u);
  }
  const unsigned lx = fr * 64 + (fq ^ ((fr >> 1) & 2)) * 16 - lane * 16;
  static_for<5>([&](auto I) { dsr128<I.value * 1024>(xa[I.value], la + lx); });
  static_for<9>([&](auto I) { dsr128<I.value * 1024>(xb[I.value], lb + lx); });
  f32x2 fa[5][4], fb[9][4];
  asm volatile("s_waitcnt lgkmcnt(9)" ::: "memory");
#pragma unroll
  for (int i = 0; i < 5; ++i)
#pragma unroll
    for (int e = 0; e < 4; ++e) fa[i][e] = unpk_v(xa[i][e]);
  asm volatile("s_waitcnt lgkmcnt(0)" ::: "memory");
#pragma unroll
  for (int i = 0; i < 9; ++i)
#pragma unroll
    for (int e = 0; e < 4; ++e) fb[i][e] = unpk_v(xb[i][e]);
  STAMP(0, 4);
  u32x4 pm[4], ps[3];
  {
    const rsrc_t rsM = mk_rsrc(g.ws + WS_G1M + (bm >> 3) * 65536, 65536), rsS = mk_rsrc(g.ws + WS_G1S + (bn >> 2) * 32768, 32768);
    const unsigned vm = ((fr >> 2) & 1) * 32768 + (fr >> 3) * 8192 + fq * 64 + (fr & 3) * 16;
    const unsigned vs0 = (bm * 128 - 16 + lane + (lane < 16 ? hc * 16 : 0)) * 16, vs1 = (bm * 128 + 48 + lane) * 16, vs2 = (bm * 128 + 112 + fr) * 16;
    if (PSL) __builtin_amdgcn_s_sleep(PSL);
    unsigned spins = 0;
    for (;;) {
      asm volatile("buffer_load_dwordx4 %4, %8, %12, 0 offen sc1\n\tbuffer_load_dwordx4 %5, %9, %12, 0 offen sc1\n\tbuffer_load_dwordx4 %6, %10, %12, 0 offen sc1\n\t"
                   "buffer_load_dwordx4 %0, %7, %11, %13 offen sc1\n\tbuffer_load_dwordx4 %1, %7, %11, %14 offen sc1\n\t"
                   "buffer_load_dwordx4 %2, %7, %11, %15 offen sc1\n\tbuffer_load_dwordx4 %3, %7, %11, %16 offen sc1\n\t"
                   "s_waitcnt vmcnt(0)"
                   : "=&v"(pm[0]), "=&v"(pm[1]), "=&v"(pm[2]), "=&v"(pm[3]), "=&v"(ps[0]), "=&v"(ps[1]), "=&v"(ps[2])
                   : "v"(vm), "v"(vs0), "v"(vs1), "v"(vs2), "s"(rsM), "s"(rsS), "s"(kA * 256), "s"(kA * 256 + 16384), "s"(kB * 256), "s"(kB * 256 + 16384) : "memory");
      unsigned bad = 0;
#pragma unroll
      for (int i = 0; i < 4; ++i) bad |= (pm[i][1] ^ tag) | (pm[i][3] ^ tag);
#pragma unroll
      for (int i = 0; i < 3; ++i) bad |= (ps[i][1] ^ tag) | (ps[i][3] ^ tag);
      asm volatile("" : "+v"(bad));
      if (__all(bad == 0u) || ++spins > (1u << 18)) break;
      if (PSI) __builtin_amdgcn_s_sleep(PSI);
    }
  }
  STAMP(0, 3);
  f32x2 rs[9], ra[5];
  {
    u32x2* sp = reinterpret_cast<u32x2*>(smem + QX_OFF + 2048 + wid * 1536);
#pragma unroll
    for (int i = 0; i < 3; ++i) sp[i * 64 + lane] = u32x2{ps[i][0], ps[i][2]};
    __builtin_amdgcn_wave_barrier();
#pragma unroll
    for (int i = 0; i < 5; ++i) ra[i] = __builtin_bit_cast(f32x2, sp[(f0 + i) * 16 + fr]);
#pragma unroll
    for (int i = 0; i < 9; ++i) rs[i] = __builtin_bit_cast(f32x2, sp[i * 16 + fr]);
  }
  auto cols = [&](const u32x4& p0, const u32x4& p1, f32x2 (&sa)[4], f32x2 (&ca)[4]) {
    float m0 = __uint_as_float(p0[0]) + __uint_as_float(p1[0]), m1 = __uint_as_float(p0[2]) + __uint_as_float(p1[2]);
    const float one = (fr & 4) ? 1.0f : 0.0f;
    m0 = (m0 + dpp_get<0x128>(m0)) + one; m1 = (m1 + dpp_get<0x128>(m1)) + one;
    static_for<4>([&](auto E) {
      constexpr int e = E.value;
      sa[e] = f32x2{dpp_get<0x150 + e>(m0), dpp_get<0x150 + e>(m1)};
      ca[e] = f32x2{dpp_get<0x154 + e>(m0), dpp_get<0x154 + e>(m1)};
    });
  };
  auto frag = [&](const f32x2 (&xf)[4], const f32x2 r, const f32x2 (&sa)[4], const f32x2 (&ca)[4]) {
    u32x4 ho;
#pragma unroll
    for (int e = 0; e < 4; ++e) { const f32x2 h = (xf[e] * r[0] + r[1]) * ca[e] + sa[e]; ho[e] = pk_bf16(h[0], h[1]); }
    return ho;
  };
  f32x2 sa[4], ca[4];
  cols(pm[0], pm[1], sa, ca);
  const rsrc_t rsH = mk_rsrc(g.ws + WS_H + ((bm * 8 + f0) * 32 + kA) * 1024, 3 * 32768 + 1024);
  static_for<5>([&](auto I) {
    constexpr int i = (I.value + 1) % 5;
    const u32x4 ho = frag(fa[i], ra[i], sa, ca);
    if constexpr (i > 0) gstore4_sc1(ho, lane * 16, rsH, (i - 1) * 32768);
    dsw128<i * 1024>(i == 0 && f0 ? smem_base + QH_OFF + lane * 16 : la, ho);
  });
  cols(pm[2], pm[3], sa, ca);
  static_for<9>([&](auto I) { dsw128<I.value * 1024>(lb, frag(fb[I.value], rs[I.value], sa, ca)); });
  STAMP(0, 5);
  wait_vm<0>();
  asm volatile("buffer_store_dword %0, %1, %2, 0 offen sc1" ::"v"(tag), "v"(lane * 32768), "s"(mk_rsrc(g.ws + WS_FH + (bm * 16 + bn) * 128 + wid * 4, 7 * 32768 + 4)) : "memory");
  STAMP(1, 1);
}

DI unsigned qkv_loader(char* smem, unsigned smem_base, int lane, int sw, int bm, int bn, int cn, const Args& g) {
  const int m0 = bm * 128, n0 = bn * 64;
  const unsigned u0 = ((bn ^ 3) * 2);
  const char* wc = g.ws + WS_WC;
  unsigned m0a = smem_base + sw * 3072, m0w = smem_base + 9216 + sw * 4096;
  auto wrow = [&](int i, unsigned sec) {
    const int r = i * 8 + (lane >> 3), ch = lane & 7;
    const int gg = ((r >> 1) & 1) | (((r >> 3) & 1) << 1);
    return (unsigned)((r * 3 + sw) * sec + ((((ch >> 1) ^ gg) * 16) + (ch & 1) * 8) * 2);
  };
  int fb0 = (m0 >> 4) - 1 + 3 * sw; fb0 = fb0 < 0 ? 0 : fb0;
  {
    const rsrc_t rsX = mk_rsrc(g.x, S_LEN * D_DIM * 2);
    const unsigned vx = (lane >> 2) * 2048 + ((lane & 3) ^ ((lane >> 3) & 2)) * 16;
#pragma unroll
    for (int i = 0; i < 18; ++i) {
      const int u = (i / 6) * 2 + (i & 1), r = (i >> 1) % 3;
      if (i >= TW) wait_vm<TW - 1>();
      bload_lds<0>(m0a + u * Q_SLOT + r * 1024, vx, rsX, (r ? (m0 >> 4) - 1 + 3 * sw + r : fb0) * 32768 + (u ^ u0) * 64);
    }
    const rsrc_t rsW0 = mk_rsrc((const char*)g.wq + n0 * 2, D_DIM * 3 * D_DIM * 2 - n0 * 2);
    const unsigned voffW0[2] = {wrow(0, 2048), wrow(1, 2048)};
#pragma unroll
    for (int u = 0; u < Q_NSL - 1; ++u)
#pragma unroll
      for (int i = 0; i < 4; ++i) {
        wait_vm<TW - 1>();
        if (u * 4 + i == TW - 1) asm volatile("ds_add_u32 %0, %1 offset:4" ::"v"(smem_base + QT_OFF), "v"(1u) : "memory");
        bload_lds<0>(m0w + u * Q_SLOT + i * 1024, voffW0[i & 1], rsW0, ((u ^ u0) * 2u + (i >> 1)) * 98304u);
      }
  }
  u32x4 oc[8];
  unsigned c2;
  front_ocld(lane, sw, bm, bn, g, oc, c2);
  const rsrc_t rsT = mk_rsrc((const char*)g.wq + (blockIdx.x * 3 + sw) * 8192, 8192);
    wait_vm<TT>();
  touch8k(smem_base + QH_OFF, lane * 128u, rsT, 0);
  const rsrc_t rsH = mk_rsrc(g.ws + WS_H, S_LEN * D_DIM * 2);
  const bool c3 = C3Q && (bn & 7) == slow_col(g.wq);
  const rsrc_t rsW = mk_rsrc(c3 ? wc + (n0 >> 9) * 393216 : (const char*)g.wq + n0 * 2, c3 ? 393216 : D_DIM * 3 * D_DIM * 2 - n0 * 2);
  const unsigned sec = c3 ? 128u : 2048u, p16 = sec * 48u;
  unsigned voffH0 = (unsigned)(fb0 * 32768 + lane * 16);
  unsigned voffH1 = (unsigned)(((m0 >> 4) + 3 * sw) * 32768 + lane * 16);
  unsigned voffW[2] = {wrow(0, sec), wrow(1, sec)};
  asm volatile("" : "+v"(voffH0), "+v"(voffH1), "+v"(voffW[0]), "+v"(voffW[1]));
  asm volatile("" : "+s"(m0a), "+s"(m0w));
#define Q_LOADH(SOFF, U)                                                                                         \
  do {                                                                                                           \
    const unsigned so_ = ((unsigned)(U) ^ u0) * 1024u;                                                           \
    bload_lds_sc1<0>(m0a + (SOFF), voffH0, rsH, so_);                                                            \
    bload_lds_sc1<0>(m0a + (SOFF) + 1024, voffH1, rsH, so_);                                                     \
    bload_lds_sc1<0>(m0a + (SOFF) + 2048, voffH1, rsH, so_ + 32768u);                                            \
  } while (0)
#define Q_LOADW(SOFF, U)                                                                                         \
  do {                                                                                                           \
    _Pragma("unroll") for (int i = 0; i < 4; ++i)                                                                \
      bload_lds_sc1<0>(m0w + (SOFF) + i * 1024, voffW[i & 1], rsW, (((unsigned)(U) ^ u0) * 2u + (i >> 1)) * p16); \
  } while (0)
  front_oc<1>(smem, lane, sw, oc, c2);
  __builtin_amdgcn_s_barrier();
  Q_LOADW(6 * Q_SLOT, 6);
  __builtin_amdgcn_s_barrier();
  Q_LOADW(0, 7); Q_LOADW(Q_SLOT, 8);
  __builtin_amdgcn_s_barrier();
  Q_LOADH(6 * Q_SLOT, 6); Q_LOADH(0, 7); Q_LOADH(Q_SLOT, 8);
  Q_LOADH(2 * Q_SLOT, 9); Q_LOADW(2 * Q_SLOT, 9); Q_LOADH(3 * Q_SLOT, 10); Q_LOADW(3 * Q_SLOT, 10);
  wait_vm<17>();
  __builtin_amdgcn_s_barrier();
  unsigned sb = 4 * Q_SLOT;
  for (int u = 11; u < 31; u += 2) {
#pragma unroll
    for (int t = 0; t < 2; ++t) {
      Q_LOADH(sb, u + t);
      Q_LOADW(sb, u + t);
      sb = (sb == (Q_NSL - 1) * Q_SLOT) ? 0u : sb + Q_SLOT;
    }
    wait_vm<21>();
    __builtin_amdgcn_s_barrier();
  }
  Q_LOADH(3 * Q_SLOT, 31); Q_LOADW(3 * Q_SLOT, 31);
  const rsrc_t rsO = wo_rsrc(g.wo, wc, cn);
  const int sh = (C3O && (cn & 7) == slow_col(g.wo)) ? 7 : 11;
  const unsigned voffO[2] = {wo_voff(lane, sw & 1, sh), wo_voff(lane, (sw & 1) ^ 1, sh)};
  static_for<6>([&](auto U) {
    constexpr int u = U.value;
#pragma unroll
    for (int j = 0; j < 7; ++j) {
      const int pc = (u < 3 ? u * 21 : 64 + (u - 3) * 21) + 3 * j;
      const unsigned p = pc + sw;
      bload_lds_sc1<0>(smem_base + (u < 3 ? WO_H0 : -65536) + p * 1024u, voffO[pc & 1], rsO, ((p >> 2) * 32u + (p & 3) * 4u) << sh);
    }
    if constexpr (u == 0 || u == 2) wait_vm<21>();
    if constexpr (u == 0 || u == 2 || u >= 4) __builtin_amdgcn_s_barrier();
  });
  unsigned tag;
#pragma unroll
  for (int i = 0; i < (XCL == 2 ? 4 : 0); ++i) gstore4_sc1(u32x4{~0u, ~0u, ~0u, ~0u}, lane * 16, mk_rsrc(g.ws + WS_X + blockIdx.x * 16384 + sw * 4096, 4096), i * 1024);
  asm volatile("ds_read_b32 %0, %1 offset:512\n\ts_waitcnt vmcnt(%2) lgkmcnt(0)" : "=v"(tag) : "v"(smem_base + QH_OFF), "n"(XCL == 2 ? 4 : 0) : "memory");
  return __builtin_amdgcn_readfirstlane(tag);
}
namespace d4 {
struct Tile { int kh, bm, bn; };
DI Tile tile() {
  int b = blockIdx.x;
  asm volatile("" : "+s"(b));
  const int xcd = b & 7, idx = b >> 3;
  return Tile{xcd & 1, ((xcd >> 1) & 1) * 8 + (idx & 7), (xcd >> 2) * 4 + (idx >> 3)};
}
}
#define FTO 3
#define CL0 4
#define TLBT 0
#define TLBU -1
namespace w7 {
constexpr int UD = 24;
constexpr int ops(int u) {
  int c = (TLBT && u == TLBU) + (u == 26) + (u == 32) + (u == DWO) + (XCL == 0 && u >= CL0 && u < CL0 + 16) + (u == CL0 + 16);
  for (int j = 0; j < 2 * ((TQ + 7) / 8); ++j) c += DW0 + (j * DWS) / 2 == u;
  return c;
}
constexpr int after(int v, int u) { int c = 0; for (int t = v + 1; t < u; ++t) c += ops(t); return c > 63 ? 63 : c; }
}
DI unsigned qkv_spare(char* smem, unsigned smem_base, int lane, int bm, int bn, int cn, const Args& g, const bf16* wd, const bf16* out) {
  using namespace w7;
  u32x4 cp[4], oc[8];
  unsigned tag, c2;
  const rsrc_t rsP = mk_rsrc((const char*)g.wo + (size_t)blockIdx.x * 8192, 8192);
  auto tlbt = [&]() {
    if (TLBT) {
      const int gq = lane >> 3, i = lane & 7, idx = blockIdx.x >> 3;
      const char* b = gq < 3 ? g.ws : (gq == 3 ? (const char*)out : (gq == 4 ? (const char*)g.wg : (gq == 5 ? (const char*)g.wu : (gq == 6 ? (const char*)wd : (const char*)g.wo))));
      unsigned o;
      if (TLBT == 1) o = gq < 3 ? lane * 2097152u : i * (gq == 3 ? 599040u : (gq == 7 ? 299520u : 1048448u));
      else o = gq < 3 ? (lane * 32u + idx) * 65536u : (i * 32u + idx) * (gq == 3 ? 16384u : (gq == 7 ? 8192u : 28672u));
      asm volatile("s_mov_b32 m0, %0\n\ts_nop 0\n\tglobal_load_lds_dword %1, off" ::"s"(smem_base + QH_OFF), "v"(b + o) : "memory");
    }
  };
  {
    const rsrc_t rsQ = mk_rsrc((const char*)g.wq + (size_t)blockIdx.x * 24576, 24576);
    const unsigned vc = ((lane >> 3) & 3) * 2048 + (lane >> 5) * 1024 + (lane & 7) * 16;
    asm volatile("" : "=v"(cp[0]), "=v"(cp[1]), "=v"(cp[2]), "=v"(cp[3]), "=v"(tag));
#pragma unroll
    for (int i = 0; i < 3; ++i) bload_v<0>(cp[i], vc, rsQ, i * 8192 + slow_col(g.wq) * 128);
    bload_v<0>(cp[3], vc, rsP, slow_col(g.wo) * 128);
    bload1_v<0>(tag, 0u, mk_rsrc(g.ws, 4), 0);
    if (FTO) {
      const unsigned o = lane < 16 ? WS_FY + blockIdx.x * 2048 + lane * 128 : (lane < 18 ? WS_FA + blockIdx.x * 256 + (lane - 16) * 128 : WS_FO + blockIdx.x * 64);
      asm volatile("s_mov_b32 m0, %0\n\ts_nop 0\n\tbuffer_load_dword %1, %2, 0 offen sc1 lds" ::"s"(smem_base + QH_OFF), "v"(lane < ((FTO & 2) ? 0 : 16) || lane > 18 ? 1u << 30 : o), "s"(mk_rsrc(g.ws, 2u << 20)) : "memory");
    }
    if (TLBU < 0) tlbt();
    asm volatile("s_waitcnt vmcnt(%5)" : "+v"(cp[0]), "+v"(cp[1]), "+v"(cp[2]), "+v"(cp[3]), "+v"(tag) : "n"((FTO ? 1 : 0) + (TLBT && TLBU < 0 ? 1 : 0)) : "memory");
    tag = __builtin_amdgcn_readfirstlane(tag) + 0x9E3779B9u;
    const rsrc_t rsC = mk_rsrc(g.ws + WS_WC, 1 << 20);
#pragma unroll
    for (int i = 0; i < 3; ++i) gstore4_sc1(cp[i], (lane >> 5) * 393216 + (lane & 31) * 16, rsC, (blockIdx.x * 3 + i) * 512);
    gstore4_sc1(cp[3], (lane >> 5) * 131072 + (lane & 31) * 16, rsC, 786432 + blockIdx.x * 512);
  }
  wait_vm<0>();
  asm volatile("buffer_store_dword %0, %1, %2, 0 offen sc1" ::"v"(tag), "v"(lane * 4), "s"(mk_rsrc(g.ws + WS_FC + blockIdx.x * 16, 4)) : "memory");
  STAMPT(1, 3, 448);
  front_ocld(lane, 3, bm, bn, g, oc, c2);
  front_oc<0>(smem, lane, 3, oc, c2);
  STAMPT(1, 4, 448);
  if ((C3Q && (bn & 7) == slow_col(g.wq)) || (C3O && (cn & 7) == slow_col(g.wo))) {
    const rsrc_t rsF = mk_rsrc(g.ws + WS_FC, 4096);
    unsigned spins = 0, f0, f1, f2, f3;
    do {
      if (spins) __builtin_amdgcn_s_sleep(8);
      asm volatile("buffer_load_dword %0, %4, %5, 0 offen sc1\n\tbuffer_load_dword %1, %4, %5, 0 offen offset:1024 sc1\n\t"
                   "buffer_load_dword %2, %4, %5, 0 offen offset:2048 sc1\n\tbuffer_load_dword %3, %4, %5, 0 offen offset:3072 sc1\n\ts_waitcnt vmcnt(0)"
                   : "=&v"(f0), "=&v"(f1), "=&v"(f2), "=&v"(f3) : "v"(lane * 16), "s"(rsF) : "memory");
    } while (!__all(((f0 ^ tag) | (f1 ^ tag) | (f2 ^ tag) | (f3 ^ tag)) == 0u) && ++spins < (1u << 16));
  }
  STAMPT(1, 0, 448);
  const rsrc_t rsX = mk_rsrc(g.ws + WS_G2 + (bn ^ 2) * 512, 131072);
  const rsrc_t rsO = wo_rsrc(g.wo, g.ws + WS_WC, cn);
  const int sh = (C3O && (cn & 7) == slow_col(g.wo)) ? 7 : 11;
  const unsigned voffO = wo_voff(lane, 1, sh);
  __builtin_amdgcn_s_barrier();
  const rsrc_t rsG = mk_rsrc((const char*)g.wg + (size_t)blockIdx.x * (TQ * 1024), TQ * 1024);
  const rsrc_t rsU = mk_rsrc((const char*)g.wu + (size_t)blockIdx.x * (TQ * 1024), TQ * 1024);
  static_for<33>([&](auto U) {
    constexpr int u = U.value;
    (void)&rsG; (void)&rsU; (void)&rsP; (void)&rsX; (void)&oc;
    if constexpr (u == 0) {
      dsw32<512>(smem_base + QH_OFF, tag);
      const float* rp = reinterpret_cast<const float*>(smem + QX_OFF + 1024) + lane;
      gstore2_sc1(u32x2{__float_as_uint((rp[0] + rp[64]) + (rp[128] + rp[192])), tag}, bm * 8192 + lane * 8, rsX);
    }
    if constexpr (u == CK) {
      const rsrc_t rsF = mk_rsrc(g.ws + WS_FH + (blockIdx.x & 7) * 32768 + (bm == 0 ? 0 : (bm - 1) * 2048), 4096);
      unsigned spins = 0, f0, f1;
      do {
        asm volatile("buffer_load_dword %0, %2, %3, 0 offen sc1\n\tbuffer_load_dword %1, %2, %3, %4 offen sc1\n\ts_waitcnt vmcnt(0)"
                     : "=&v"(f0), "=&v"(f1) : "v"((lane >> 2) * 128 + (lane & 3) * 4), "s"(rsF), "s"(bm ? 2048 : 0) : "memory");
      } while (!__all(f0 == tag && f1 == tag) && ++spins < (1u << 18));
      STAMPT(1, 2, 448);
    }
    if constexpr (u == UD) wait_vm<after(CL0 + 16, u)>();
    if constexpr (u == 26) bload_lds_sc1<0>(smem_base + WO_H0 + 63 * 1024, voffO, rsO, (15 * 32u + 12u) << sh);
    if constexpr (u == 32) bload_lds_sc1<0>(smem_base + 63 * 1024, voffO, rsO, (31 * 32u + 12u) << sh);
    if constexpr (u == TLBU) tlbt();
    if constexpr (u == DWO) touch8k(smem_base + QH_OFF, lane * 128u, rsP, 0);
#pragma unroll
    for (int j = 0; j < 2 * ((TQ + 7) / 8); ++j)
      if (DW0 + (j * DWS) / 2 == u) touch8k(smem_base + QH_OFF, lane * 128u, (j & 1) ? rsU : rsG, (j >> 1) * 8192u);
    if constexpr (XCL == 0 && u >= CL0 && u < CL0 + 16) gstore4_sc1(u32x4{~0u, ~0u, ~0u, ~0u}, lane * 16, mk_rsrc(g.ws + WS_X + blockIdx.x * 16384, 16384), (u - CL0) * 1024);
    if constexpr (u == CL0 + 16) gstore4_sc1(u32x4{~0u, ~0u, ~0u, ~0u}, lane * 16, mk_rsrc(g.ws + WS_STAT + (bm * 16 + bn) * 1024, 1024), 0);
    if constexpr ((u & 1) || u == 32) __builtin_amdgcn_s_barrier();
  });
  {
    const rsrc_t rsS = mk_rsrc(g.ws + WS_G2, 131072);
    const unsigned vc = cn * 512 + (lane >> 5) * 32768 + (lane & 31) * 16, vd = 98304 + d4::tile().bn * 1024 + lane * 16;
    u32x4 pm[12];
    unsigned spins = 0;
    for (;;) {
      asm volatile("buffer_load_dwordx4 %0, %12, %14, 0 offen sc1\n\tbuffer_load_dwordx4 %1, %12, %14, %15 offen sc1\n\t"
                   "buffer_load_dwordx4 %2, %12, %14, %16 offen sc1\n\tbuffer_load_dwordx4 %3, %12, %14, %17 offen sc1\n\t"
                   "buffer_load_dwordx4 %4, %12, %14, %18 offen sc1\n\tbuffer_load_dwordx4 %5, %12, %14, %19 offen sc1\n\t"
                   "buffer_load_dwordx4 %6, %12, %14, %20 offen sc1\n\tbuffer_load_dwordx4 %7, %12, %14, %21 offen sc1\n\t"
                   "buffer_load_dwordx4 %8, %13, %14, 0 offen sc1\n\tbuffer_load_dwordx4 %9, %13, %14, %15 offen sc1\n\t"
                   "buffer_load_dwordx4 %10, %13, %14, %16 offen sc1\n\tbuffer_load_dwordx4 %11, %13, %14, %17 offen sc1\n\ts_waitcnt vmcnt(0)"
                   : "=&v"(pm[0]), "=&v"(pm[1]), "=&v"(pm[2]), "=&v"(pm[3]), "=&v"(pm[4]), "=&v"(pm[5]), "=&v"(pm[6]), "=&v"(pm[7]),
                     "=&v"(pm[8]), "=&v"(pm[9]), "=&v"(pm[10]), "=&v"(pm[11])
                   : "v"(vc), "v"(vd), "s"(rsS), "s"(8192), "s"(16384), "s"(24576), "s"(65536), "s"(73728), "s"(81920), "s"(90112) : "memory");
      unsigned bad = 0;
#pragma unroll
      for (int i = 0; i < 12; ++i) bad |= (pm[i][1] ^ tag) | (pm[i][3] ^ tag);
      if (__all(bad == 0u) || ++spins > (1u << 16)) break;
    }
#pragma unroll
    for (int i = 0; i < (XCL == 2 ? 4 : 0); ++i) gstore4_sc1(u32x4{~0u, ~0u, ~0u, ~0u}, lane * 16, mk_rsrc(g.ws + WS_X + blockIdx.x * 16384 + 12288, 4096), i * 1024);
    f32x2 m[3];
#pragma unroll
    for (int v = 0; v < 3; ++v)
#pragma unroll
      for (int e = 0; e < 2; ++e)
        m[v][e] = (__uint_as_float(pm[v * 4][2 * e]) + __uint_as_float(pm[v * 4 + 1][2 * e])) + (__uint_as_float(pm[v * 4 + 2][2 * e]) + __uint_as_float(pm[v * 4 + 3][2 * e]));
    f32x2* tp = reinterpret_cast<f32x2*>(smem + QT_OFF);
    tp[(lane >> 5) * 64 + (lane & 31)] = m[0];
    if (lane < 32) tp[32 + lane] = m[1] + 1.0f;
    tp[128 + lane] = m[2];
  }
  asm volatile("s_waitcnt lgkmcnt(0)" ::: "memory");
  return tag;
}

DI void qkv_mfma(char* smem, unsigned smem_base, int tid, int wid, int m0, int n0, bf16* O, unsigned* oflags, unsigned tag) {
  const int lane = tid & 63;
  const int wm = wid & 1, wn = wid >> 1;
  const int fr = lane & 15, fq = lane >> 4;
  unsigned rad[3][4];
  {
    const int q = (lane >> 2) & 3, p = lane & 3;
    const int r = 8 * fq + q;
    const int g = ((r >> 1) & 1) | (((r >> 3) & 1) << 1);
#pragma unroll
    for (int i = 0; i < 3; ++i) {
      rad[i][3] = smem_base + i * (3 * Q_SLOT) + lane * 16; rad[i][2] = rad[i][3] + 1024 + wm * 4096;
#pragma unroll
      for (int jj = 0; jj < 2; ++jj) rad[i][jj] = smem_base + i * (3 * Q_SLOT) + 9216 + r * 128 + (((wn * 2 + (jj ^ wm)) ^ g) * 32) + 8 * p;
      asm volatile("" : "+v"(rad[i][0]), "+v"(rad[i][1]), "+v"(rad[i][2]), "+v"(rad[i][3]));
    }
  }
  f32x4 acc[4][6], acch[2];
#pragma unroll
  for (int i = 0; i < 4; ++i)
#pragma unroll
    for (int j = 0; j < 6; ++j) { acc[i][j] = f32x4{0, 0, 0, 0}; asm volatile("" : "+v"(acc[i][j])); }
#pragma unroll
  for (int j = 0; j < 2; ++j) { acch[j] = f32x4{0, 0, 0, 0}; asm volatile("" : "+v"(acch[j])); }
  asm volatile("s_waitcnt lgkmcnt(0)" ::: "memory");
  __builtin_amdgcn_s_barrier();
  STAMPDECL;
  STAMPNW(2, 1);
  u32x4 a[2][5];
  u32x2 bl[2][6], bh[2][6];
  auto rdb = [&](auto PP, auto GG, auto RR) {
    constexpr int jj = RR.value >> 2, j = GG.value * 2 + jj, k = (RR.value >> 1) & 1, sl = (PP.value * 2 + k) % Q_NSL;
    constexpr int off = (sl % 3) * Q_SLOT + GG.value * 4096 + (RR.value & 1) * 512;
    if constexpr (RR.value & 1) dsr_tr<off>(bh[k][j], rad[sl / 3][jj]);
    else dsr_tr<off>(bl[k][j], rad[sl / 3][jj]);
  };
  auto gap = [&](auto PP, auto NN) {
    constexpr int n = NN.value, k = ~n & 1, sl = (PP.value * 2 + k) % Q_NSL;
    if constexpr (n < QY + 8) rdb(PP, ic<0>{}, ic<(n - QY) & 7>{});
    else if constexpr (n == 34 || n == 35) dsr128<(sl % 3) * Q_SLOT>(a[k][4], rad[sl / 3][3]);
    else if constexpr (n >= 36 && (n & 2)) dsr128<(sl % 3) * Q_SLOT + ((n - 36) >> 2) * 1024>(a[k][((n - 36) >> 2) & 3], rad[sl / 3][2]);
    else if constexpr (n >= 36) rdb(PP, ic<1>{}, ic<((n - 36) >> 2) * 2 + (n & 1)>{});
  };
  static_for<52 - QY>([&](auto N) { gap(ic<0>{}, ic<N.value + QY>{}); });
  static_for<16>([&](auto PP) {
    static_for<52>([&](auto N) {
      constexpr int n = N.value, m = n < 36 ? n : n - 4, k = (n ^ (n >> 1)) & 1;
      constexpr int i = (m & 16) ? 3 - ((m >> 2) & 3) : (m >> 2) & 3, j = (m >> 4) * 2 + (((m + 2) >> 2) & 1);
      (void)&acc; (void)&acch; (void)&a; (void)&bl; (void)&bh;
      if constexpr (n < 16 && n % 4 == 0) asm volatile("s_waitcnt lgkmcnt(%0)" ::"n"(n < 12 ? 12 : 8) : "memory");
      if constexpr (n == QY) {
        asm volatile("s_waitcnt lgkmcnt(0)" ::: "memory");
        __builtin_amdgcn_s_barrier();
      }
      constexpr int jb = n < 32 || n >= 36 ? j : (n < 34 ? 2 : 4);
      const u32x4 b = {bl[k][jb][0], bl[k][jb][1], bh[k][jb][0], bh[k][jb][1]};
      if constexpr (n < 32) MFMA_AV(acc[i][j], b, a[k][i]);
      else if constexpr (n < 34) MFMA_AV(acch[0], b, a[k][4]);
      else if constexpr (n < 36) MFMA_AV(acch[1], a[k][4], b);
      else MFMA_AV(acc[i][j], a[k][i], b);
      if constexpr (n < 8) rdb(PP, ic<2>{}, N);
      if constexpr (n >= QY && PP.value < 15) gap(ic<PP.value + 1>{}, N);
    });
  });
  asm volatile("s_waitcnt vmcnt(0) lgkmcnt(0)\n\ts_nop 15" ::: "memory");
  SCHED0();
  __builtin_amdgcn_s_setprio(0);
  STAMP(2, 2); STAMPFLUSH(2, 1);
#pragma unroll
  for (int i = 0; i < 4; ++i)
#pragma unroll
    for (int j = 0; j < 6; ++j) asm volatile("" : "+v"(acc[i][j]));
  asm volatile("" : "+v"(acch[0]), "+v"(acch[1]));
  auto pk2 = [](const f32x4 v) { return u32x2{pk_bf16(v[0], v[1]), pk_bf16(v[2], v[3])}; };
  auto cat = [](const u32x2 a, const u32x2 b) { return __builtin_bit_cast(bf16x8, u32x4{a[0], a[1], b[0], b[1]}); };
  u32x2 kp[5][2], vp[5][2];
  {
    char* xb = smem + QX_OFF + wn * 4096 + lane * 8;
#pragma unroll
    for (int f = 0; f < 2; ++f) { kp[4][f] = pk2(acc[3][2 + f]); vp[4][f] = pk2(acc[3][4 + f]); }
    *reinterpret_cast<u32x2*>(xb + wm * 512) = pk2(acch[0]);
    *reinterpret_cast<u32x2*>(xb + 1024 + wm * 512) = pk2(acch[1]);
    if (wm == 0) {
#pragma unroll
      for (int f = 0; f < 2; ++f) {
        *reinterpret_cast<u32x2*>(xb + 2048 + f * 512) = kp[4][f];
        *reinterpret_cast<u32x2*>(xb + 2048 + 1024 + f * 512) = vp[4][f];
      }
    }
    asm volatile("s_waitcnt lgkmcnt(0)" ::: "memory");
    __builtin_amdgcn_s_barrier();
    asm volatile("" ::: "memory");
#pragma unroll
    for (int f = 0; f < 2; ++f) {
      kp[0][f] = *reinterpret_cast<const u32x2*>(xb + wm * 2048 + (f ^ wm) * 512);
      vp[0][f] = *reinterpret_cast<const u32x2*>(xb + wm * 2048 + 1024 + (f ^ wm) * 512);
    }
  }
  STAMP(2, 3);
#pragma unroll
  for (int i = 0; i < 3; ++i)
#pragma unroll
    for (int f = 0; f < 2; ++f) { kp[1 + i][f] = pk2(acc[i][2 + f]); vp[1 + i][f] = pk2(acc[i][4 + f]); }
  const bool par = fr & 1;
  const bool cura = fq * 4 + (fr & 1) <= fr, curb = fq * 4 + 2 + (fr & 1) <= fr;
  const unsigned mh = par ? 0xffff0000u : 0x0000ffffu;
  const unsigned mca = cura ? mh : 0u, mpa = cura ? 0u : mh, mcb = curb ? mh : 0u, mpb = curb ? 0u : mh;
  const float cs = 0.17677669529663687f * 1.4426950408889634f;
  char* op = (char*)O + ((size_t)((m0 >> 6) + wm) * (D_DIM / 32) + (n0 >> 5) + wn) * 4096 + lane * 16;
  const f32x4 z4 = {0, 0, 0, 0};
#pragma unroll
  for (int ii = 0; ii < 4; ++ii) {
    const int i = 3 - ii;
    const bf16x8 qf = cat(pk2(acc[i][0]), pk2(acc[i][1]));
    const f32x4 sc = __builtin_amdgcn_mfma_f32_16x16x32_bf16(cat(kp[i + 1][0], kp[i + 1][1]), qf, z4, 0, 0, 0);
    f32x4 sp = __builtin_amdgcn_mfma_f32_16x16x32_bf16(cat(kp[i][0], kp[i][1]), qf, z4, 0, 0, 0);
    if (i == 0 && m0 == 0 && wm == 0) sp = f32x4{-5.656854249e9f, -5.656854249e9f, -5.656854249e9f, -5.656854249e9f};
    const float la = cura ? (par ? sc[1] : sc[0]) : (par ? sp[1] : sp[0]);
    const float lb = curb ? (par ? sc[3] : sc[2]) : (par ? sp[3] : sp[2]);
    float mx = fmaxf(la, lb);
    {
      const auto a = __builtin_amdgcn_permlane16_swap(__float_as_uint(mx), __float_as_uint(mx), false, false);
      mx = fmaxf(__uint_as_float(a[0]), __uint_as_float(a[1]));
      const auto b = __builtin_amdgcn_permlane32_swap(__float_as_uint(mx), __float_as_uint(mx), false, false);
      mx = fmaxf(__uint_as_float(b[0]), __uint_as_float(b[1]));
    }
    const float ea = __builtin_amdgcn_exp2f((la - mx) * cs), eb = __builtin_amdgcn_exp2f((lb - mx) * cs);
    const float inv = __builtin_amdgcn_rcpf(rowsum4(ea + eb));
    const unsigned wa = pk_bf16(ea, ea), wb = pk_bf16(eb, eb);
    const bf16x8 pf = __builtin_bit_cast(bf16x8, u32x4{wa & mpa, wb & mpb, wa & mca, wb & mcb});
    const f32x4 o0 = __builtin_amdgcn_mfma_f32_16x16x32_bf16(cat(vp[i][0], vp[i + 1][0]), pf, z4, 0, 0, 0) * inv;
    const f32x4 o1 = __builtin_amdgcn_mfma_f32_16x16x32_bf16(cat(vp[i][1], vp[i + 1][1]), pf, z4, 0, 0, 0) * inv;
    const u32x2 u0 = pk2(o0), u1 = pk2(o1);
    gst128(op + i * 1024, wm ? u32x4{u1[0], u1[1], u0[0], u0[1]} : u32x4{u0[0], u0[1], u1[0], u1[1]});
  }
  STAMP(2, 4);
  wait_vm<0>();
  asm volatile("buffer_store_dword %0, %1, %2, 0 offen sc1" ::"v"(tag), "v"(lane * (4096 * OFS)), "s"(mk_rsrc(oflags + ((m0 >> 1) + wm * 32 + (n0 >> 5) + wn) * OFS, (ORP - 1) * 4096 * OFS + 4)) : "memory");
  STAMP(2, 5);
}

#define X2S 2
#define X2P 0
DI void x2_store(const u32x4 (&x2p)[2], const char* ws, int lane, int w, int own) {
  const rsrc_t rsR = mk_rsrc(ws + WS_X2 + (((own >> 4) * 8 + (w & 3) * 2 + (w >> 2)) * 16 + (own & 15)) * 2048, 2048);
  gstore4_sc1(x2p[0], lane * 16, rsR, 0); gstore4_sc1(x2p[1], lane * 16, rsR, 1024);
}
DI void o_phase(char* smem, unsigned smem_base, int lane, int w, int cm, int cn, const bf16* O, const unsigned* oflags,
                const bf16* __restrict__ x, bf16* Y, float* stat, const char* ws, u32x4 (&x2p)[2], unsigned tag) {
  const int wm = w & 3, kh = w >> 2;
  const int fr = lane & 15, fq = lane >> 4;
  const int R = cm * 8 + wm * 2 + kh;
  u32x2 xv[4];
  {
    const rsrc_t rsX = mk_rsrc(x, S_LEN * D_DIM * 2);
    const unsigned vx = (unsigned)(((R * 16 + fr) * D_DIM + cn * 64 + fq * 4) * 2);
#pragma unroll
    for (int f = 0; f < 4; ++f) asm volatile("" : "=v"(xv[f]));
    bload2_v<0>(xv[0], vx, rsX, 0); bload2_v<32>(xv[1], vx, rsX, 0); bload2_v<64>(xv[2], vx, rsX, 0); bload2_v<96>(xv[3], vx, rsX, 0);
  }
  const unsigned hb = smem_base + (kh ? 0 : WO_H0);
  unsigned brd[4];
  {
    const int q = (lane >> 2) & 3, p = lane & 3;
    const int r = 8 * fq + q;
    const int g = ((r >> 1) & 1) | (((r >> 3) & 1) << 1);
#pragma unroll
    for (int f = 0; f < 4; ++f) brd[f] = hb + r * 128 + ((f ^ g) * 32) + 8 * p;
  }
  const rsrc_t rsA = mk_rsrc((const char*)O + (size_t)(cm * 2 + (wm >> 1)) * 131072 + kh * 65536, 65536);
  unsigned va0 = lane * 16 + ((wm & 1) * 2 + kh) * 1024, va1 = va0 ^ 1024;
  unsigned tsrc = smem_base + QT_OFF + fq * 16, xw = hb + lane * 16 + wm * 4096, xr = smem_base + (kh ? WO_H0 : 0) + lane * 16 + wm * 4096;
  char* yb = (char*)Y + ((size_t)(R >> 4) * 32 + cn * 2 + (fq & 1)) * 16384 + (R & 15) * 1024 + ((fq >> 1) * 16 + fr) * 16;
  asm volatile("" : "+v"(tsrc), "+v"(xw), "+v"(xr), "+v"(yb), "+v"(va0), "+v"(va1), "+v"(brd[0]), "+v"(brd[1]), "+v"(brd[2]), "+v"(brd[3]));
  f32x4 acc[2][4];
#pragma unroll
  for (int i = 0; i < 2; ++i)
#pragma unroll
    for (int f = 0; f < 4; ++f) { acc[i][f] = f32x4{0, 0, 0, 0}; asm volatile("" : "+v"(acc[i][f])); }
  if (w == POLLW) {
    const rsrc_t rsF = mk_rsrc(oflags + ((blockIdx.x & (ORP - 1)) * 1024 + cm * 64) * OFS, 256 * OFS);
    unsigned spins = 0, f;
    do {
      asm volatile("buffer_load_dword %0, %1, %2, 0 offen sc1\n\ts_waitcnt vmcnt(0)" : "=v"(f) : "v"(lane * (4 * OFS)), "s"(rsF) : "memory");
    } while (!__all(f == tag) && ++spins < (1u << 18));
  }
  __builtin_amdgcn_s_barrier();
  STAMPDECL;
  STAMPNW(3, 1);
  u32x4 a[16][2], gt[4], ct[4], st[4];
  u32x2 bl[2][4], bh[2][4];
  auto rdb = [&](auto S) {
    constexpr int s = S.value;
#pragma unroll
    for (int f = 0; f < 4; ++f) { dsr_tr<s * 4096>(bl[s & 1][f], brd[f]); dsr_tr<s * 4096 + 512>(bh[s & 1][f], brd[f]); }
  };
  auto lda = [&](auto S) {
    constexpr int s = S.value;
    asm volatile("" : "=v"(a[s][0]), "=v"(a[s][1]));
    bload_sc1<0>(a[s][0], va0, rsA, s * 4096);
    bload_sc1<0>(a[s][1], va1, rsA, s * 4096);
  };
  static_for<OD>([&](auto S) {
    lda(S);
    if constexpr (S.value == 0) rdb(ic<0>{});
    if constexpr (S.value == 1) rdb(ic<1>{});
  });
  static_for<16>([&](auto S) {
    constexpr int s = S.value;
    asm volatile("s_waitcnt vmcnt(%0) lgkmcnt(8)" ::"n"(2 * ((s + OD - 1 < 15 ? s + OD - 1 : 15) - s)) : "memory");
    SCHED0();
    u32x4 bb[4];
#pragma unroll
    for (int f = 0; f < 4; ++f) bb[f] = u32x4{bl[s & 1][f][0], bl[s & 1][f][1], bh[s & 1][f][0], bh[s & 1][f][1]};
    SCHED0();
    if constexpr (s + OD < 16) lda(ic<(s + OD < 16 ? s + OD : 0)>{});
    if constexpr (s == OMB) __builtin_amdgcn_s_barrier();
#pragma unroll
    for (int n = 0; n < 8; ++n) MFMA_AV(acc[1 - (n >> 2)][n & 3], bb[n & 3], a[s][1 - (n >> 2)]);
    if constexpr (s < 14) rdb(ic<(s < 14 ? s + 2 : 0)>{});
    if constexpr (s == 14) {
      static_for<4>([&](auto F) { dsr128<F.value * 64>(gt[F.value], tsrc); dsr128<256 + F.value * 64>(ct[F.value], tsrc); });
    }
    SCHED0();
  });
  STAMP(3, 2); STAMPFLUSH(3, 1);
  asm volatile("s_nop 7" ::: "memory");
  static_for<4>([&](auto F) { dsw128<F.value * 1024>(xw, __builtin_bit_cast(u32x4, acc[1][F.value])); });
  asm volatile("s_waitcnt lgkmcnt(0)" ::: "memory");
  __builtin_amdgcn_s_barrier();
  u32x4 pr[4];
  static_for<4>([&](auto F) { dsr128<F.value * 1024>(pr[F.value], xr); });
  static_for<4>([&](auto F) { dsr128<512 + F.value * 64>(st[F.value], tsrc); });
#pragma unroll
  for (int f = 0; f < 4; ++f) asm volatile("" : "+v"(acc[0][f]), "+v"(xv[f]), "+v"(gt[f]), "+v"(ct[f]));
  asm volatile("s_waitcnt lgkmcnt(4)" : "+v"(pr[0]), "+v"(pr[1]), "+v"(pr[2]), "+v"(pr[3])::"memory");
  f32x4 fin[4];
#pragma unroll
  for (int f = 0; f < 4; ++f) {
    const f32x4 xf = {bf_lo(xv[f][0]), bf_hi(xv[f][0]), bf_lo(xv[f][1]), bf_hi(xv[f][1])};
    fin[f] = xf + __builtin_bit_cast(f32x4, gt[f]) * (acc[0][f] + __builtin_bit_cast(f32x4, pr[f]));
  }
  const rsrc_t rsS = mk_rsrc((char*)stat + R * 2048, 2048);
  {
    const f32x4 s4 = (fin[0] + fin[1]) + (fin[2] + fin[3]);
    const float sm = rowsum4((s4[0] + s4[1]) + (s4[2] + s4[3]));
    const float mb = sm * (1.0f / 64.0f);
    f32x4 q4 = {0, 0, 0, 0};
#pragma unroll
    for (int f = 0; f < 4; ++f) { const f32x4 d = fin[f] - mb; q4 += d * d; }
    const float m2 = rowsum4((q4[0] + q4[1]) + (q4[2] + q4[3]));
    gstore2_sc1(u32x2{min(__float_as_uint(sm), ~1u), min(__float_as_uint(m2), ~1u)}, lane < 16 ? cn * 128 + lane * 8 : 1u << 30, rsS);
  }
  STAMP(3, 3);
#pragma unroll
  for (int j = 0; j < 2; ++j) {
    x2p[j] = u32x4{pk_bf16(fin[2 * j][0], fin[2 * j][1]), pk_bf16(fin[2 * j][2], fin[2 * j][3]), pk_bf16(fin[2 * j + 1][0], fin[2 * j + 1][1]), pk_bf16(fin[2 * j + 1][2], fin[2 * j + 1][3])};
    asm volatile("" : "+v"(x2p[j]));
  }
  if (X2P == 0) x2_store(x2p, ws, lane, w, cm * 16 + cn);
  u32x2 tv[4];
  {
    if (SSL) __builtin_amdgcn_s_sleep(SSL);
    unsigned spins = 0;
    for (;;) {
      asm volatile("buffer_load_dwordx2 %0, %4, %5, 0 offen sc1\n\tbuffer_load_dwordx2 %1, %4, %5, 0 offen offset:512 sc1\n\t"
                   "buffer_load_dwordx2 %2, %4, %5, 0 offen offset:1024 sc1\n\tbuffer_load_dwordx2 %3, %4, %5, 0 offen offset:1536 sc1\n\ts_waitcnt vmcnt(0) lgkmcnt(0)"
                   : "=&v"(tv[0]), "=&v"(tv[1]), "=&v"(tv[2]), "=&v"(tv[3]) : "v"(lane * 8), "s"(rsS) : "memory");
      const unsigned mx = max(max(max(tv[0][0], tv[0][1]), max(tv[1][0], tv[1][1])), max(max(tv[2][0], tv[2][1]), max(tv[3][0], tv[3][1])));
      if (!__any(mx == ~0u) || ++spins > (1u << 18)) break;
      if (SSI) __builtin_amdgcn_s_sleep(SSI);
    }
  }
  STAMP(3, 6);
  const float mean = rowsum4((__uint_as_float(tv[0][0]) + __uint_as_float(tv[1][0])) + (__uint_as_float(tv[2][0]) + __uint_as_float(tv[3][0]))) * (1.0f / D_DIM);
  float m2 = 0;
#pragma unroll
  for (int i = 0; i < 4; ++i) { const float d = __uint_as_float(tv[i][0]) * (1.0f / 64.0f) - mean; m2 += __uint_as_float(tv[i][1]) + 64.0f * d * d; }
  const float rstd = rsqrtf(rowsum4(m2) * (1.0f / D_DIM) + 1e-5f), rm = -mean * rstd;
  u32x2 pk[4];
#pragma unroll
  for (int f = 0; f < 4; ++f) {
    asm volatile("" : "+v"(st[f]));
    const f32x4 yv = (fin[f] * rstd + rm) * __builtin_bit_cast(f32x4, ct[f]) + __builtin_bit_cast(f32x4, st[f]);
    pk[f] = u32x2{pk_bf16(yv[0], yv[1]), pk_bf16(yv[2], yv[3])};
  }
#pragma unroll
  for (int j = 0; j < 2; ++j) {
    const auto s0 = __builtin_amdgcn_permlane16_swap(pk[j][0], pk[j + 2][0], false, false);
    const auto s1 = __builtin_amdgcn_permlane16_swap(pk[j][1], pk[j + 2][1], false, false);
    gst128(yb + j * 512, u32x4{s0[0], s1[0], s0[1], s1[1]});
  }
  if (X2P == 1) x2_store(x2p, ws, lane, w, cm * 16 + cn);
}

#define MORD 1
#define EPR 3
#define TPOS 62
#define TUN 0
#define TSH 1
#define GSC 1
#define GD4 4
#define GD3 3
#define GY4 0
#define GY3 0
struct Stg { rsrc_t rA, rB; unsigned vA, vB, sA, sB, dA, dB, e1, e3; };
#define G3_STAGE(SLOT, P)                                                                                  \
  do {                                                                                                     \
    if ((P) == 0) bload_lds_sc1<0>(m0w + (SLOT) * 32768, st.vA, st.rA, st.sA);                             \
    else if ((P) == 1) { bload_lds_sc1<1024>(m0w + (SLOT) * 32768, st.vA, st.rA, st.sA + st.e1); st.sA += st.dA; } \
    else if ((P) == 2) bload_lds<0>(m0w + (SLOT) * 32768 + 16384, st.vB, st.rB, st.sB);                    \
    else { bload_lds<0>(m0w + (SLOT) * 32768 + 16384 + 1024, st.vB, st.rB, st.sB + st.e3); st.sB += st.dB; } \
  } while (0)

#define DNF 24
namespace d4 {
constexpr int NF = DNF, NH = 56 - 2 * NF, N = NF + NH, NE = 12, FR = 32768;
constexpr int eg(int s) { return (s / 6) * 2 + (s & 1); }
constexpr int ef(int s) { return (s % 6) >> 1; }
constexpr int apos(int s) { return s < NE ? eg(s) * 3 + ef(s) : s + NE; }
constexpr int wpos(int s, int k) { return s < NE ? (eg(s) * 4 + k) * 7 + 4 + ef(s) : (s - NE) * 7 + k; }
struct WSrc { rsrc_t rs; unsigned v, f[3]; };
DI WSrc wsrc(const bf16* Wd, int j) {
  const Tile t = tile();
  const int lane = lane_id(), grp = t.bn * 2 + (j >> 1), ch = lane & 7, gg = ((lane >> 4) & 1) | ((j & 1) << 1);
  WSrc w;
  w.rs = mk_rsrc((const char*)Wd + grp * 128, F_DIM * D_DIM * 2 - grp * 128);
  w.v = (unsigned)(((j & 1) * 8 + (lane >> 3)) * 2048 + ((ch >> 1) ^ gg) * 32 + (ch & 1) * 16);
  w.f[0] = t.kh * (112 * FR); w.f[1] = t.kh * ((44 - NF) * 7 * FR); w.f[2] = 0;
  asm volatile("" : "+v"(w.v), "+s"(w.f[0]), "+s"(w.f[1]), "+s"(w.f[2]));
  return w;
}
constexpr int cls(int s) { return s < NE ? 0 : (s < NF ? 1 : 2); }
}

template <int FN>
DI void g3_body(char* smem, unsigned smem_base, int wm, int wn, int wid, int lane, int bm, int bn, Stg st,
                rsrc_t rsW, const bf16* Wd, char* ws) {
  const unsigned tmask = wm < 2 ? 0x1111111u : 0x1100000u;
  bf16* act = (bf16*)(ws + WS_ACT);
  constexpr int NB = 2 * FN, NM = 8 * NB;
  const int fr = lane & 15, fq = lane >> 4;
  f32x4 acc[4][NB];
#pragma unroll
  for (int i = 0; i < 4; ++i)
#pragma unroll
    for (int j = 0; j < NB; ++j) { acc[i][j] = f32x4{0, 0, 0, 0}; asm volatile("" : "+v"(acc[i][j])); }
  unsigned aad[2], bad[2][FN];
  aad[0] = smem_base + wm * 4096 + lane * 16; aad[1] = aad[0] + 65536;
  {
    const int q = (lane >> 2) & 3, p = lane & 3;
    const int r = 8 * fq + q;
    const int sw = (r & 3) | (((r >> 3) & 1) << 2);
#pragma unroll
    for (int j = 0; j < FN; ++j) {
      const int gi = wn * 4 + j + (bn & 1);
      bad[0][j] = smem_base + 16384 + r * 256 + ((gi ^ sw) * 32) + 8 * p;
      bad[1][j] = bad[0][j] + 65536;
    }
  }
  u32x4 a[2][4];
  u32x2 bl[2][NB], bh[2][NB];
  const unsigned m0w = smem_base + wid * 2048;
  constexpr int GDS = FN == 4 ? GD4 : GD3;
  constexpr int L = NM - 16, Y = GSC == 1 ? L : L - 8 * (FN - 2);
  auto rdb = [&](auto HH, auto GG, auto RR) {
    constexpr int j = GG.value * 2 + (RR.value >> 2), k = (RR.value >> 1) & 1, off = k * 32768 + (j / FN) * 8192 + (RR.value & 1) * 1024;
    if constexpr (RR.value & 1) dsr_tr<off>(bh[k][j], bad[HH.value][j % FN]);
    else dsr_tr<off>(bl[k][j], bad[HH.value][j % FN]);
  };
  auto row = [](int h, int n) constexpr { return ((n >> 4) ^ (FN == 4 ? h : 0)) & 1 ? 3 - ((n >> 2) & 3) : (n >> 2) & 3; };
  auto tail = [&](auto HH, auto NN) {
    constexpr int h = HH.value, n = NN.value;
    if constexpr (n & 2) dsr128<(~n & 1) * 32768 + row(h, n) * 1024>(a[~n & 1][row(h, n)], aad[h]);
    else rdb(HH, ic<FN - 2>{}, ic<(n >> 2) * 2 + (n & 1)>{});
  };
  unsigned long long em = ~0ull;
  unsigned vt = (lane & 31) * 128u + (lane >> 5) * 0x40000000u;
  const rsrc_t rsD = mk_rsrc((const char*)Wd + blockIdx.x * 28672, 28672);
  rsrc_t rsT = rsW;
  unsigned sT = 8u << 18, dT = 4u << 18;
  if (TUN && wm >= 2) vt = 1u << 30;
  auto period = [&](auto HH, int u) {
    constexpr int h = HH.value;
    static_for<NM>([&](auto N) {
      constexpr int n = N.value, p = n >> 1, k = (n ^ p) & 1, i = row(h, n), j = (n >> 4) * 2 + (((p + 1) >> 1) & 1);
      (void)&acc; (void)&a; (void)&bl; (void)&bh; (void)&st; (void)&em; (void)&rsW; (void)&rsD; (void)&vt; (void)&rsT; (void)&sT; (void)&dT;
      if constexpr (n < 16 && n % 4 == 0) asm volatile("s_waitcnt lgkmcnt(%0)" ::"n"(n < 12 ? 12 : 8) : "memory");
      if constexpr (n == 16) {
        asm volatile("s_waitcnt vmcnt(%0) lgkmcnt(0)" ::"n"(GSC == 1 ? 4 + (TUN && FN == 4 && h == 0) : 63) : "memory");
        __builtin_amdgcn_s_barrier();
      }
      if constexpr (n == Y) {
        wait_vm<GSC == 1 ? 4 + (TUN && FN == 4 && h == 0) : ((Y - 17 + GDS - 1) / GDS < 8 ? (Y - 17 + GDS - 1) / GDS : 8)>();
        __builtin_amdgcn_s_barrier();
      }
      if constexpr (!TUN && FN == 4 && h == 1 && n == TPOS) {
        if (__builtin_expect((tmask >> u) & 1u, 0))
          touch8k(smem_base + QH_OFF, vt, u >= 20 ? rsD : rsW, u >= 20 ? (u - 20 + wm) * 4096u : (u + 8u) << 18);
      }
      if constexpr (TUN && FN == 4 && h == 1 && n == 63) { touch8k(smem_base + QH_OFF, vt, rsT, sT); sT += dT; }
      if constexpr (h == 0 && n == 10) {
        if (__builtin_expect(u == 28 || (TUN && FN == 4 && u == 20 - 4 * TSH), 0)) {
          if (u == 28) {
            const d4::WSrc w = d4::wsrc(Wd, wm);
            st.rA = st.rB = w.rs; st.vA = st.vB = w.v;
            st.sA = w.f[0] + (4 + wn * 14) * d4::FR; st.sB = st.sA + 28 * d4::FR; st.dA = st.dB = d4::FR;
            st.e3 = 7 * d4::FR; st.e1 = st.e3 - 1024;
            em = 0; vt = 1u << 30;
          } else {
            rsT = rsD; sT = wm * 4096; dT = 16384; vt = (lane & 31) * 128u + (lane >> 5) * 0x40000000u;
          }
        }
      }
      MFMA_AV(acc[i][j], (u32x4{bl[k][j][0], bl[k][j][1], bh[k][j][0], bh[k][j][1]}), a[k][i]);
      if constexpr (n < 8) rdb(HH, ic<FN - 1>{}, ic<n & 7>{});
      if constexpr (n >= L) tail(ic<h ^ 1>{}, ic<n & 15>{});
      if constexpr (GSC == 1 && n >= 16 && n < L && (n & 15) < 8) rdb(ic<h ^ 1>{}, ic<(n >> 4) - 1>{}, ic<n & 7>{});
      if constexpr (GSC != 1 && n >= Y && n < L) rdb(ic<h ^ 1>{}, ic<(n - Y) / 8>{}, ic<n & 7>{});
      constexpr int d = GSC == 1 ? (n >= L ? 4 * GDS + n - L - 1 : (n - 17 < 4 * GDS ? n - 17 : -1)) : n - 17;
      if constexpr (d >= 0 && d % GDS == 0 && d / GDS < 8) {
        constexpr int q = d / GDS, sl = 2 * h + ((q >> 1) & 1);
        if constexpr (sl == 3) asm volatile("s_mov_b64 exec, %0" ::"s"(em));
        G3_STAGE(sl, (q & 1) + (q < 4 ? 2 : 0));
        if constexpr (sl == 3) asm volatile("s_mov_b64 exec, -1");
      }
    });
  };
  static_for<8 * (FN - 2)>([&](auto RR) { rdb(ic<0>{}, ic<RR.value / 8>{}, ic<RR.value & 7>{}); });
#pragma unroll
  for (int j = 0; j < FN; ++j) asm volatile("" : "+v"(bad[1][j]));
  asm volatile("" : "+v"(aad[0]), "+v"(aad[1]), "+v"(vt), "+s"(em), "+s"(st.sA), "+s"(st.sB), "+s"(st.dA), "+s"(st.dB), "+s"(st.e1), "+s"(st.e3) : "s"(rsD));
  if constexpr (TUN && FN == 4) { touch8k(smem_base + QH_OFF, TSH ? vt : 1u << 30, rsT, sT); sT += TSH * dT; }
  wait_vm<4 + (TUN && FN == 4)>();
  __builtin_amdgcn_s_barrier();
  STAMPDECL;
  STAMPNW(5, 1);
  static_for<16>([&](auto RR) { tail(ic<0>{}, RR); });
  for (int u = 0; u < 32; u += 4) {
    period(ic<0>{}, u);
    period(ic<1>{}, u);
  }
  asm volatile("s_waitcnt lgkmcnt(0)\n\ts_nop 15" ::: "memory");
#pragma unroll
  for (int i = 0; i < 8; ++i) asm volatile("" : "+v"(a[i >> 2][i & 3]));
#pragma unroll
  for (int j = 0; j < NB - 2; ++j) asm volatile("" : "+v"(bl[0][j]), "+v"(bh[0][j]), "+v"(bl[1][j]), "+v"(bh[1][j]));
#pragma unroll
  for (int j = 0; j < NB; ++j) asm volatile("" : "+v"(acc[0][j]), "+v"(acc[1][j]), "+v"(acc[2][j]), "+v"(acc[3][j]));
  SCHED0();
  STAMP(5, 2);
  STAMPFLUSH(5, 1);
  if constexpr (FN == 3) __builtin_amdgcn_s_setprio(EPR);
  static_for<FN>([&](auto JJ) {
    constexpr int j = JJ.value;
    u32x2 pk[4];
    static_for<8>([&](auto G) {
      constexpr int i = G.value >> 1, hh = G.value & 1;
      const f32x2 ag = hh ? hi2(acc[i][j]) : lo2(acc[i][j]), au = hh ? hi2(acc[i][FN + j]) : lo2(acc[i][FN + j]);
      const f32x2 ea = ag * -1.4426950408889634f;
      f32x2 e = {__builtin_amdgcn_exp2f(ea[0]), __builtin_amdgcn_exp2f(ea[1])};
      e = e + 1.0f;
      const f32x2 rc = {__builtin_amdgcn_rcpf(e[0]), __builtin_amdgcn_rcpf(e[1])};
      const f32x2 o = (ag * au) * rc;
      pk[i][hh] = pk_bf16(o[0], o[1]);
    });
    const int J = FN == 4 ? (24 + bn) * 4 + j : ((bn >> 2) * 3 + j) * 4 + (bn & 3);
    char* dst = (char*)act + ((size_t)((bm * 4 + wm) * (F_DIM / 32) + (J >> 1)) * 4 + (fq & 1)) * 1024 + (((J & 1) * 2 + (fq >> 1)) * 16 + fr) * 16;
#pragma unroll
    for (int i = 0; i < 4; i += 2) {
      const auto s0 = __builtin_amdgcn_permlane16_swap(pk[i][0], pk[i + 1][0], false, false);
      const auto s1 = __builtin_amdgcn_permlane16_swap(pk[i][1], pk[i + 1][1], false, false);
      gst128(dst + i * 1024, u32x4{s0[0], s1[0], s0[1], s1[1]});
    }
  });
  STAMP(5, 3);
  STAMPT(5, 5, 256);
}
DI unsigned flag_off(unsigned b, unsigned wid) {
  const unsigned xcd = b & 7, idx = b >> 3;
  return ((idx & 7) * 2 + ((wid >> 1) & 1)) * 512 + (xcd * 4 + (idx >> 3)) * 16 + (wid & 1) * 8 + (wid >> 2) * 4;
}
DI void act_flag(char* ws, int lane, int wid, unsigned tag) {
  asm volatile("buffer_store_dword %0, %1, %2, 0 offen sc1" ::"v"(tag), "v"(lane * 8192), "s"(mk_rsrc(ws + WS_FA + flag_off(blockIdx.x, wid), 7 * 8192 + 4)) : "memory");
}

#define XRD 1
#define DSI 0
#define DPS (N - 3)
#define DFS 0
#define DSL 4
#define DLA 4
#define DMX 3
#define LPR 0
namespace d4 {
struct Sched { int tA[N], tW[N], eA[N], eW[N], eB, cnt[N + 2], last[N], tL; };
constexpr Sched sched() {
  Sched q = {};
  unsigned fr = 7u << 6;
  int nA = 0, nW = 6, c = 0;
  auto take = [&]() { int e = 0; while (!((fr >> e) & 1)) ++e; fr &= ~(1u << e); return e; };
  for (int s = 0; s < 6; ++s) { q.tW[s] = -1; q.eW[s] = s; }
  for (int t = 0; t <= N; ++t) {
    q.cnt[t] = c;
    if (nA >= NE - 2 && !q.tL) { q.tL = t; c += 1; }
    if (t == 1) fr |= 1u << 9;
    if (t >= 2) fr |= (1u << q.eA[t - 2]) | (1u << q.eW[t - 2]);
    for (int m = 0; m < DMX && fr; ++m) {
      const bool a = nA <= nW;
      const int s = a ? nA : nW;
      if (s >= N || s > t + DLA) break;
      if (a) { q.tA[s] = t; q.eA[s] = take(); c += s < NF ? 4 : 2; ++nA; } else { q.tW[s] = t; q.eW[s] = take(); c += 4; ++nW; }
      q.last[s] = c;
    }
    if (t == N - 1) q.eB = take();
  }
  q.cnt[N + 1] = c;
  return q;
}
constexpr Sched Q = sched();
template <int M, int S>
DI void dma(unsigned m0b, unsigned voff, rsrc_t rs, unsigned so) {
  unsigned t;
  asm volatile("s_add_u32 m0, %1, %5\n\ts_add_u32 %0, %4, %6\n\tbuffer_load_dwordx4 %2, %3, %0 offen sc1 lds"
               : "=&s"(t) : "s"(m0b), "v"(voff), "s"(rs), "s"(so), "n"(M), "n"(S) : "memory", "scc");
}
}
DI void d4_loader(char* smem, unsigned smem_base, int lane, int j, const bf16* Wd, char* ws, unsigned tag) {
  using namespace d4;
  wait_vm<0>();
  act_flag(ws, lane, 4 + j, tag);
  if (blockIdx.x == 0 && j == 3) asm volatile("buffer_store_dword %0, %1, %2, 0 offen sc1" ::"v"(tag), "v"(lane * 4), "s"(mk_rsrc(ws, 4)) : "memory");
  STAMPT(5, 6, 256);
  __builtin_amdgcn_s_setprio(LPR);
  const Tile t = tile();
  const rsrc_t rsA = mk_rsrc(ws + WS_ACT + t.bm * 917504, 917504);
  const WSrc w = wsrc(Wd, j);
  unsigned vA = lane * 16 + j * 1024;
  const unsigned mA = smem_base + j * 1024, mW = smem_base + j * 2048;
  unsigned sO[3] = {t.kh * (458752u + NE * 8192), t.kh * (458752u + (44 - NF) * 8192), t.kh * 458752u};
  unsigned sP[2] = {458752u - t.kh * (458752u - NE * 8192), 458752u - t.kh * (458752u - (44 - NF) * 8192)};
  asm volatile("" : "+v"(vA));
  asm volatile("" : "+s"(sO[0]), "+s"(sO[1]), "+s"(sO[2]), "+s"(sP[0]), "+s"(sP[1]));
  const rsrc_t rsF = mk_rsrc(ws + WS_FA + t.bn * 8192, 8192);
  if (j == 0) {
    if (DSL) __builtin_amdgcn_s_sleep(DSL);
    unsigned spins = 0, f;
    do {
      asm volatile("buffer_load_dword %0, %1, %2, %3 offen offset:4 sc1\n\ts_waitcnt vmcnt(0)" : "=v"(f) : "v"((lane & 31) * 8), "s"(rsF), "s"(t.bm * 512 + t.kh * 256) : "memory");
    } while (!__all(f == tag) && ++spins < (1u << 18));
    dsw32<768>(smem_base + QT_OFF, 1u);
  } else {
    unsigned n;
    do {
      asm volatile("s_sleep 1\n\tds_read_b32 %0, %1 offset:768\n\ts_waitcnt lgkmcnt(0)" : "=v"(n) : "v"(smem_base + QT_OFF) : "memory");
    } while (__builtin_amdgcn_readfirstlane(n) == 0u);
  }
  STAMPT(6, 0, 256);
  unsigned fl;
  const int cn = t.bn * 2 + ((lane >> 2) & 1);
  const unsigned vF = lane < 2 * (44 - NF) ? t.bm * 512 + t.kh * ((NF - NE) * 16) + lane * 8
                    : (lane < 56 ? flag_off(blockIdx.x ^ 1, 4 + (lane & 3)) : flag_off(((cn & 7) * 4 + (t.bm & 3)) * 8 + (t.bm >> 2) * 2 + (cn >> 3), t.kh * 2 + (lane & 1) + (lane & 2) * 2));
  static_for<N>([&](auto T) {
    constexpr int i = T.value;
    (void)&fl;
    if constexpr (i == Q.tL) { asm volatile("" : "=v"(fl)); bload1_sc1(fl, vF, rsF); }
    if constexpr (i == Q.tA[NE]) {
      asm volatile("s_waitcnt vmcnt(%1)" : "+v"(fl) : "n"(Q.cnt[i] - Q.cnt[Q.tL] - 1) : "memory");
      if (__builtin_expect(!__all(fl == tag), 0)) {
        unsigned spins = 0, f;
        do {
          asm volatile("buffer_load_dword %0, %1, %2, 0 offen sc1\n\ts_waitcnt vmcnt(0)" : "=v"(f) : "v"(vF), "s"(rsF) : "memory");
        } while (!__all(f == tag) && ++spins < (1u << 18));
      }
    }
    static_for<N>([&](auto S) {
      constexpr int s = S.value;
      if constexpr (Q.tA[s] == i) {
        static_for<2>([&](auto K) {
          constexpr int k = K.value, m = Q.eA[s] * 16384 + k * 8192;
          if constexpr (s < NF) dma<m, (apos(s) * 2 + k) * 4096>(mA, vA, rsA, sP[cls(s) & 1]);
          dma<m + 4096, (apos(s) * 2 + k) * 4096>(mA, vA, rsA, sO[cls(s)]);
        });
      }
      if constexpr (Q.tW[s] == i) {
        static_for<4>([&](auto K) {
          constexpr int k = K.value;
          dma<Q.eW[s] * 16384 + (k >> 1) * 8192 + (k & 1) * 1024, wpos(s, k) * FR>(mW, w.v, w.rs, w.f[cls(s)]);
        });
      }
    });
    wait_vm<Q.cnt[i + 1] - Q.last[i]>();
    __builtin_amdgcn_s_barrier();
  });
}
DI bool missing(const u32x4 (&pv)[4]) {
  unsigned m = 0;
#pragma unroll
  for (int f = 0; f < 4; ++f) m = max(max(m, max(pv[f][0], pv[f][1])), max(pv[f][2], pv[f][3]));
  return __any(m == ~0u);
}
DI void d4_mfma(char* smem, unsigned smem_base, int lane, int w, bf16* out, char* ws, unsigned tag) {
  using namespace d4;
  const Tile t = tile();
  const int fr = lane & 15, fq = lane >> 4, wr = w & 1, wc = w >> 1;
  unsigned brd[3][4], ard[3], vL = lane * 16;
  {
    const int q = (lane >> 2) & 3, p = lane & 3, g = (q >> 1) | ((fq & 1) << 1);
#pragma unroll
    for (int i = 0; i < 3; ++i) {
#pragma unroll
      for (int f = 0; f < 4; ++f) {
        brd[i][f] = smem_base + i * 65536 + wc * 4096 + (fq & 1) * 2048 + (fq >> 1) * 1024 + q * 128 + ((f ^ g) * 32) + 8 * p;
        asm volatile("" : "+v"(brd[i][f]));
      }
      ard[i] = smem_base + i * 65536 + wr * 2048 + lane * 16;
      asm volatile("" : "+v"(ard[i]));
    }
  }
  asm volatile("" : "+v"(vL));
  f32x4 acc[4][4];
#pragma unroll
  for (int i = 0; i < 4; ++i)
#pragma unroll
    for (int f = 0; f < 4; ++f) { acc[i][f] = f32x4{0, 0, 0, 0}; asm volatile("" : "+v"(acc[i][f])); }
  const rsrc_t rsX = mk_rsrc(ws + xoff(blockIdx.x) + w * 4096, 4096), rsP = mk_rsrc(ws + xoff(blockIdx.x ^ 1) + w * 4096, 4096);
  const unsigned bo = Q.eB * 16384 + w * 4096;
  unsigned tw[4], tr = smem_base + bo + (lane >> 3) * 128 + (((lane & 7) ^ (lane >> 3)) * 16);
  const rsrc_t rsO = mk_rsrc((char*)out + ((t.bm * 2 + t.kh) * 64 + wr * 32) * (D_DIM * 2) + t.bn * 256 + wc * 128, 31 * D_DIM * 2 + 128);
  unsigned vO = (lane >> 3) * (D_DIM * 2) + (lane & 7) * 16;
#pragma unroll
  for (int f = 0; f < 4; ++f) tw[f] = smem_base + bo + fr * 128 + (((f * 2 + (fq >> 1)) ^ (fr & 7)) * 16) + (fq & 1) * 8;
  asm volatile("" : "+v"(tw[0]), "+v"(tw[1]), "+v"(tw[2]), "+v"(tw[3]), "+v"(tr), "+v"(vO) : "s"(rsO));
  __builtin_amdgcn_s_setprio(2);
  u32x4 gt[4], xq[4], ex, pv[4];
  f32x2 base[16];
  const rsrc_t rsR = mk_rsrc(ws + WS_X2 + (((t.bm * 2 + t.kh) * 4 + wr * 2) * 16 + t.bn * 2 + wc) * 2048, 32768 + 2048);
  unsigned k32 = 32768;
  asm volatile("" : "+s"(k32) : "s"(rsR));
  static_for<4>([&](auto F) { dsr128<1024 + F.value * 64>(gt[F.value], smem_base + QT_OFF + wc * 256 + fq * 16); });
  asm volatile("s_waitcnt vmcnt(8) lgkmcnt(0)" ::: "memory");
  STAMP(5, 4);
  __builtin_amdgcn_s_barrier();
  STAMPDECL;
  STAMPNW(6, 1);
  u32x4 a[2][2][4];
  u32x2 bl[2][2][4], bh[2][2][4];
  auto rd = [&](auto S, auto RR) {
    constexpr int s = S.value, per = s < NF ? 12 : 10, k = RR.value / per, r = RR.value % per;
    if constexpr (r < 8) {
      constexpr int e = Q.eW[s], off = (e & 3) * 16384 + k * 8192 + (r & 1) * 512;
      if constexpr (r & 1) dsr_tr<off>(bh[s & 1][k][r >> 1], brd[e >> 2][r >> 1]);
      else dsr_tr<off>(bl[s & 1][k][r >> 1], brd[e >> 2][r >> 1]);
    } else {
      constexpr int e = Q.eA[s], i = s < NF ? r - 8 : r - 6;
      dsr128<(e & 3) * 16384 + k * 8192 + (i >> 1) * 4096 + (i & 1) * 1024>(a[s & 1][k][i], ard[e >> 2]);
    }
  };
  static_for<24>([&](auto RR) { rd(ic<0>{}, RR); });
  static_for<N>([&](auto S) {
    constexpr int s = S.value, nm = s < NF ? 32 : 16, nr = s + 1 == N ? 0 : (s + 1 < NF ? 24 : 20), pg = s < NF ? 1 : 2;
    WAIT_LGKM0();
    u32x4 bb[2][4];
#pragma unroll
    for (int k = 0; k < 2; ++k)
#pragma unroll
      for (int f = 0; f < 4; ++f) bb[k][f] = u32x4{bl[s & 1][k][f][0], bl[s & 1][k][f][1], bh[s & 1][k][f][0], bh[s & 1][k][f][1]};
    SCHED0();
    static_for<nm>([&](auto M) {
      constexpr int n = M.value;
      (void)&base; (void)&xq; (void)&rsR; (void)&k32; (void)&rsX; (void)&vL; (void)&ex; (void)&pv; (void)&rsP;
      if constexpr (n == 2 && s + 1 < N) { __builtin_amdgcn_s_barrier(); ALIGN(DAL); }
      if constexpr (DORD == 2) {
        constexpr int p = n >> 1, k = (n ^ p) & 1, f = s < NF ? p >> 2 : p >> 1, i = s < NF ? ((f & 1) ? 3 - (p & 3) : p & 3) : 2 + ((p ^ f) & 1);
        MFMA_AV(acc[i][f], bb[k][f], a[s & 1][k][i]);
      } else {
        constexpr int mi = s < NF ? (DORD && (n & 4) ? 3 - (n & 3) : n & 3) : 2 + ((n ^ (DORD ? n >> 1 : 0)) & 1);
        if constexpr (s < NF) MFMA_AV(acc[mi][(n >> 2) & 3], bb[n >> 4][(n >> 2) & 3], a[s & 1][n >> 4][mi]);
        else MFMA_AV(acc[mi][(n >> 1) & 3], bb[n >> 3][(n >> 1) & 3], a[s & 1][n >> 3][mi]);
      }
      static_for<pg>([&](auto G) {
        constexpr int r = (n - 2) * pg + G.value;
        if constexpr (n >= 2 && r < nr && XRD) rd(ic<(s + 1 < N ? s + 1 : 0)>{}, ic<(r >= 0 && r < nr ? r : 0)>{});
      });
      if constexpr (s == DFS && n == 28) { wait_vm<0>(); act_flag(ws, lane, w, tag); }
      if constexpr (s == NE + 1 && n >= 28) { asm volatile("" : "=v"(xq[n & 3])); bload_sc1<(n & 1) * 1024>(xq[n & 3], vL, rsR, (n & 2) ? k32 : 0u); }
      if constexpr (s == NF - 1 && n == 28) wait_vm<0>();
      if constexpr ((s == NF || s == NF + 1) && n >= 4 && n < 14) {
        constexpr int i = s - NF, p = (n - 4) / 5, h = (n - 4) % 5;
        if constexpr (h < 4) {
          asm volatile("v_cvt_pk_bf16_f32 %0, %1, %2\n\tv_min_u32 %0, -2, %0" : "=v"(ex[h]) : "v"(acc[i][2 * p + (h >> 1)][2 * (h & 1)]), "v"(acc[i][2 * p + (h >> 1)][2 * (h & 1) + 1]));
        } else {
          asm volatile("buffer_store_dwordx4 %0, %1, %2, 0 offen offset:%3 sc1" ::"v"(ex), "v"(vL), "s"(rsX), "i"(i * 2048 + p * 1024) : "memory");
        }
      }
      if constexpr (s == DPS && n >= 12) { asm volatile("" : "=v"(pv[n & 3])); bload_sc1<(n & 3) * 1024>(pv[n & 3], vL, rsP, 0); }
      if constexpr (s + 1 == N) base[n] = unpk_v(xq[n >> 2][n & 3]);
    });
    SCHED0();
  });
  asm volatile("s_nop 7\n\ts_waitcnt vmcnt(0) lgkmcnt(0)" : "+v"(gt[0]), "+v"(gt[1]), "+v"(gt[2]), "+v"(gt[3]), "+v"(pv[0]), "+v"(pv[1]), "+v"(pv[2]), "+v"(pv[3])::"memory");
  if (__builtin_expect(missing(pv), 0)) {
    unsigned spins = 0;
    do {
      asm volatile("buffer_load_dwordx4 %0, %4, %5, 0 offen sc1\n\tbuffer_load_dwordx4 %1, %4, %5, 0 offen offset:1024 sc1\n\t"
                   "buffer_load_dwordx4 %2, %4, %5, 0 offen offset:2048 sc1\n\tbuffer_load_dwordx4 %3, %4, %5, 0 offen offset:3072 sc1\n\t"
                   "s_waitcnt vmcnt(0)"
                   : "=&v"(pv[0]), "=&v"(pv[1]), "=&v"(pv[2]), "=&v"(pv[3]) : "v"(vL), "s"(rsP) : "memory");
    } while (missing(pv) && ++spins < (1u << 18));
  }
  STAMP(6, 2); STAMPFLUSH(6, 1);
  u32x4 ov[4];
  static_for<8>([&](auto E) {
    constexpr int e = E.value, i = e >> 2, f = e & 3;
    asm volatile("" : "+v"(acc[2 + i][f]));
    const unsigned p0 = pv[e >> 1][(e & 1) * 2], p1 = pv[e >> 1][(e & 1) * 2 + 1];
    const f32x4 v = f32x4{base[2 * e][0], base[2 * e][1], base[2 * e + 1][0], base[2 * e + 1][1]} + __builtin_bit_cast(f32x4, gt[f]) * (acc[2 + i][f] + f32x4{bf_lo(p0), bf_hi(p0), bf_lo(p1), bf_hi(p1)});
    asm volatile("ds_write_b64 %0, %1 offset:%2" ::"v"(tw[f]), "v"(u32x2{pk_bf16(v[0], v[1]), pk_bf16(v[2], v[3])}), "i"(i * 2048) : "memory");
    if constexpr (f == 3) { dsr128<i * 2048>(ov[2 * i], tr); dsr128<i * 2048 + 1024>(ov[2 * i + 1], tr); }
    if constexpr (e == 6) {
      asm volatile("s_waitcnt lgkmcnt(3)" : "+v"(ov[0]), "+v"(ov[1])::"memory");
      gstore4_sc1(ov[0], vO, rsO, 0); gstore4_sc1(ov[1], vO, rsO, 8 * D_DIM * 2);
    }
  });
  asm volatile("s_waitcnt lgkmcnt(0)" : "+v"(ov[2]), "+v"(ov[3])::"memory");
  gstore4_sc1(ov[2], vO, rsO, 16 * D_DIM * 2); gstore4_sc1(ov[3], vO, rsO, 24 * D_DIM * 2);
  STAMP(6, 3);
  STAMP(6, 4);
}

DI void g3_phase(char* smem, unsigned smem_base, int tid, int wid, const bf16* __restrict__ Wg, const bf16* __restrict__ Wu,
                 const bf16* __restrict__ Wd, bf16* out, char* ws, int own, unsigned tag, const u32x4 (&x2p)[2]) {
  const int lane = tid & 63;
  const int wm = wid & 3, wn = wid >> 2;
  const int xcd = blockIdx.x & 7, idx = blockIdx.x >> 3;
  const int bm = idx & 7, bn = xcd * 4 + (idx >> 3);
  const int n0 = bn * 112, n0a = n0 & ~31;
  unsigned* yflags = (unsigned*)(ws + WS_FY);
  const rsrc_t rsW = mk_rsrc((const char*)(wm & 1 ? Wu : Wg) + blockIdx.x * 4096, D_DIM * F_DIM * 2 - blockIdx.x * 4096);
  Stg st;
  st.rA = mk_rsrc(ws + WS_Y + (size_t)bm * 32 * 16384, 32 * 16384);
  st.rB = mk_rsrc(wn ? Wu : Wg, D_DIM * F_DIM * 2);
  st.vA = wid * 2048 + lane * 16;
  {
    const int r = wm * 8 + (lane >> 4);
    const int sw = (r & 3) | (((r >> 3) & 1) << 2);
    const int c16 = lane & 15;
    st.vB = (unsigned)(r * (F_DIM * 2) + (n0a + (((c16 >> 1) ^ sw) * 16) + (c16 & 1) * 8) * 2);
  }
  st.sA = 0; st.sB = 0; st.dA = 16384; st.dB = 229376; st.e1 = 0; st.e3 = 4 * F_DIM * 2;
  const unsigned m0w = smem_base + wid * 2048;
  asm volatile("" : "+s"(out), "+s"(Wd));
  G3_STAGE(0, 2); G3_STAGE(0, 3); G3_STAGE(1, 2); G3_STAGE(1, 3);
  wait_vm<4>();
  __builtin_amdgcn_s_barrier();
  STAMP(3, 4);
  if (wid == 0) asm volatile("buffer_store_dword %0, %1, %2, 0 offen sc1" ::"v"(tag), "v"(lane * (1024 * YFS)), "s"(mk_rsrc(yflags + own * YFS, 7 * 1024 * YFS + 4)) : "memory");
  G3_STAGE(2, 2); G3_STAGE(2, 3); G3_STAGE(3, 2); G3_STAGE(3, 3);
  if (wn == 0) {
    if (wm == 0) {
      const rsrc_t rsF = mk_rsrc(yflags + (xcd * 256 + bm * 32) * YFS, 128 * YFS);
      if (YSL) __builtin_amdgcn_s_sleep(YSL);
      unsigned spins = 0, f;
      do {
        asm volatile("buffer_load_dword %0, %1, %2, 0 offen sc1\n\ts_waitcnt vmcnt(0)" : "=v"(f) : "v"((lane & 31) * (4 * YFS)), "s"(rsF) : "memory");
      } while (!__all(f == tag) && ++spins < (1u << 18));
    }
    wait_vm<4>();
    __builtin_amdgcn_s_barrier();
    STAMP(5, 0);
    G3_STAGE(0, 0); G3_STAGE(0, 1); G3_STAGE(1, 0); G3_STAGE(1, 1);
    G3_STAGE(2, 0); G3_STAGE(2, 1); G3_STAGE(3, 0); G3_STAGE(3, 1);
    g3_body<4>(smem, smem_base, wm, wn, wid, lane, bm, bn, st, rsW, Wd, ws);
    d4_mfma(smem, smem_base, lane_id(), wm, out, ws, tag);
  } else {
    wait_vm<4>();
    __builtin_amdgcn_s_barrier();
    G3_STAGE(0, 0); G3_STAGE(0, 1); G3_STAGE(1, 0); G3_STAGE(1, 1);
    G3_STAGE(2, 0); G3_STAGE(2, 1); G3_STAGE(3, 0); G3_STAGE(3, 1);
    g3_body<3>(smem, smem_base, wm, wn, wid, lane, bm, bn, st, rsW, Wd, ws);
    d4_loader(smem, smem_base, lane_id(), wm, Wd, ws, tag);
  }
}

__global__ __launch_bounds__(512, 2) void block_kernel(const bf16* x_, const bf16* c_, const bf16* wmod_, char* ws_, const bf16* wq_,
                                                       const bf16* wo_, const bf16* wg_, const bf16* wu_, const bf16* __restrict__ wd, bf16* out) {
  __shared__ __attribute__((aligned(16))) char smem[Q_LDS];
  u64x8 ka;
  asm volatile("s_load_dwordx16 %0, %1, 0x0" : "=s"(ka) : "s"(__builtin_amdgcn_kernarg_segment_ptr()));
  STAMP(0, 0);
  const int tid = threadIdx.x;
  const int wid = __builtin_amdgcn_readfirstlane(tid >> 6);
  const unsigned smem_base = (unsigned)(uintptr_t)smem;
  const int xcd = blockIdx.x & 7, idx = blockIdx.x >> 3;
  {
    unsigned long long pc;
    asm volatile("s_getpc_b64 %0" : "=s"(pc));
    constexpr int CH = (CODESZ + 4095) / 4096 * 128;
    touch8k(smem_base + QH_OFF, tid - 448u < CH / 128 ? (tid - 448) * 128u : 1u << 30, mk_rsrc((const char*)(uintptr_t)(pc & ~255ull), CODESZ), idx * CH);
  }
  const int bm = (xcd >> 2) * 8 + (idx & 7), bn = (xcd & 3) * 4 + (idx >> 3);
  const int cm = (xcd >> 1) * 4 + (idx & 3), cn = __builtin_amdgcn_readfirstlane((xcd & 1) * 8 + (idx >> 2));
  const int lane = tid & 63;
  Pre p;
  {
    const bool gv = ((bm + bn) & 1) == 0;
    unsigned oa = ((bm >> 2) * 4 + (wid & 3)) * 786432 + (bm & 2) * 1024 + (bn ^ 1) * 128, ob = (bm * 128 + (bn >> 1) * 16 + (wid & 3) * 4) * 2048;
    unsigned va = (lane >> 3) * (8 * 12288) + (lane & 7) * 16, vb = lane * 16;
    asm volatile("" : "+s"(oa), "+s"(ob));
    asm volatile("" : "+v"(va), "+v"(vb));
    p.o1 = gv ? oa : ob; p.n1 = gv ? 63 * 12288 + 128 : 8192; p.vo = gv ? va : vb; p.st = gv ? 12288 : 1024;
    dsw32<0>(smem_base + QT_OFF, 0u);
    dsw32<4>(smem_base + QT_OFF, 0u);
    dsw32<768>(smem_base + QT_OFF, 0u);
    asm volatile("" : "+v"(p.vo));
    asm volatile("s_waitcnt lgkmcnt(0)" : "+s"(ka), "+s"(p.o1), "+s"(p.n1), "+s"(p.st));
  }
  asm volatile("s_cmp_lt_u32 %0, 4\n\ts_cbranch_scc1 1\n\ts_barrier" ::"s"(wid) : "scc", "memory");
  const Args g = {(const bf16*)ka[0], (const bf16*)ka[1], (const bf16*)ka[2], (const bf16*)ka[4], (const bf16*)ka[5], (const bf16*)ka[6], (const bf16*)ka[7], (char*)ka[3]};
  char* ws = g.ws;
  const bf16 *x = g.x, *wg = g.wg, *wu = g.wu;
  bf16* O = (bf16*)(ws + WS_O);
  bf16* Y = (bf16*)(ws + WS_Y);
  unsigned* oflags = (unsigned*)(ws + WS_FO);
  float* stat = (float*)(ws + WS_STAT);
  unsigned tag = 0;
  if (__builtin_expect(wid < 4, 1)) {
    __builtin_amdgcn_s_setprio(2);
    tag = front1(smem, smem_base, lane, wid, bm, bn, g, p);
    front_h(smem, smem_base, lane, wid, bm, bn, g, tag);
    qkv_mfma(smem, smem_base, tid, wid, bm * 128, bn * 64, O, oflags, tag);
  } else if (wid < 7) {
    tag = qkv_loader(smem, smem_base, lane, wid - 4, bm, bn, cn, g);
  } else {
    tag = qkv_spare(smem, smem_base, lane, bm, bn, cn, g, wd, out);
  }
  STAMPT(2, 6, 320);
  STAMPT(3, 0, 448);
  u32x4 x2p[2];
  o_phase(smem, smem_base, tid & 63, wid, cm, cn, O, oflags, x, Y, stat, ws, x2p, tag);
  g3_phase(smem, smem_base, tid, wid, wg, wu, wd, out, ws, cm * 16 + cn, tag, x2p);
}

extern "C" void kernel_launch(void* const* d_in, const int* in_sizes, int n_in, void* d_out, int out_size,
                              void* d_ws, size_t ws_size, hipStream_t stream) {
  const bf16* x = (const bf16*)d_in[0];
  const bf16* wqkv = (const bf16*)d_in[1];
  const bf16* wo = (const bf16*)d_in[2];
  const bf16* wg = (const bf16*)d_in[4];
  const bf16* wu = (const bf16*)d_in[5];
  const bf16* wd = (const bf16*)d_in[6];
  const bf16* c = (const bf16*)d_in[7];
  const bf16* wmod = (const bf16*)d_in[8];
  bf16* out = (bf16*)d_out;

  char* ws = (char*)d_ws;
  block_kernel<<<256, 512, 0, stream>>>(x, c, wmod, ws, wqkv, wo, wg, wu, wd, out);
}
```

```cpp
#include <hip/hip_runtime.h>
#include <hip/hip_bf16.h>
#include <stdint.h>

typedef __hip_bfloat16 bf16;
typedef __attribute__((ext_vector_type(8))) short bf16x8;
typedef __attribute__((ext_vector_type(4))) float f32x4;
typedef __attribute__((ext_vector_type(2))) float f32x2;
typedef __attribute__((ext_vector_type(4))) unsigned u32x4;
typedef __attribute__((ext_vector_type(2))) unsigned u32x2;
typedef __attribute__((ext_vector_type(2))) __bf16 bf16x2_t;
typedef __attribute__((ext_vector_type(8))) unsigned long long u64x8;
typedef __amdgpu_buffer_rsrc_t rsrc_t;

#define DI __device__ __forceinline__
#define QBAR 2
#define TQ 8
#define DWO 6
#define DW0 20
#define DWS 4
#define OFS 4
#define ORP 1
#define YFS 32
#define YSL 6
#define CODESZ 78200

#define PW 0
#define STAMP(k, s) do { } while (0)
#define STAMPT(k, s, T) do { } while (0)
#define STAMPNW(k, s)
#define STAMPFLUSH(k, s)
#define STAMPDECL

constexpr int S_LEN = 2048;
constexpr int D_DIM = 1024;
constexpr int F_DIM = 3584;

template <int V> struct ic { static constexpr int value = V; };
template <int N, int I = 0, class F>
DI void static_for(F&& f) {
  if constexpr (I < N) { f(ic<I>{}); static_for<N, I + 1>(f); }
}

DI float bf_lo(unsigned u) { return __uint_as_float(u << 16); }
DI float bf_hi(unsigned u) { return __uint_as_float(u & 0xffff0000u); }
DI unsigned pk_bf16(float lo, float hi) {
  const f32x2 v = {lo, hi};
  return __builtin_bit_cast(unsigned, __builtin_convertvector(v, bf16x2_t));
}
DI rsrc_t mk_rsrc(const void* p, unsigned bytes) { return __builtin_amdgcn_make_buffer_rsrc((void*)p, (short)0, (int)bytes, 0x00020000); }

template <int N>
DI void wait_vm() { asm volatile("s_waitcnt vmcnt(%0)" ::"n"(N > 63 ? 63 : N) : "memory"); }
#define WAIT_LGKM0() do { asm volatile("s_waitcnt lgkmcnt(0)" ::: "memory"); __builtin_amdgcn_sched_barrier(0); } while (0)
#define SCHED0() __builtin_amdgcn_sched_barrier(0)
#define ALIGN(M) do { if ((M) == 1) asm volatile(".p2align 3"); if ((M) == 2) asm volatile(".p2align 3\n\ts_nop 0"); } while (0)
#define DAL 1
#define GAL 0
#define QAL 0

#define MFMA_A(C, B, A) asm volatile("v_mfma_f32_16x16x32_bf16 %0, %1, %2, %0" : "+a"(C) : "v"(B), "a"(A))
#define MFMA_AV(C, B, A) asm volatile("v_mfma_f32_16x16x32_bf16 %0, %1, %2, %0" : "+v"(C) : "v"(B), "v"(A))
#define PERM_LO(D, X, Y) asm volatile("v_perm_b32 %0, %1, %2, %3" : "=v"(D) : "v"(Y), "v"(X), "s"(0x05040100u))
#define PERM_HI(D, X, Y) asm volatile("v_perm_b32 %0, %1, %2, %3" : "=v"(D) : "v"(Y), "v"(X), "s"(0x07060302u))
template <int OFF>
DI void bload_v(u32x4& d, unsigned voff, rsrc_t rs, unsigned soff) {
  asm volatile("buffer_load_dwordx4 %0, %1, %2, %3 offen offset:%4" : "+v"(d) : "v"(voff), "s"(rs), "s"(soff), "i"(OFF) : "memory");
}
template <int OFF>
DI void bload_a(u32x4& d, unsigned voff, rsrc_t rs, unsigned soff) {
  asm volatile("buffer_load_dwordx4 %0, %1, %2, %3 offen offset:%4" : "+a"(d) : "v"(voff), "s"(rs), "s"(soff), "i"(OFF) : "memory");
}
template <int OFF>
DI void bload2_v(u32x2& d, unsigned voff, rsrc_t rs, unsigned soff) {
  asm volatile("buffer_load_dwordx2 %0, %1, %2, %3 offen offset:%4" : "+v"(d) : "v"(voff), "s"(rs), "s"(soff), "i"(OFF) : "memory");
}
template <int OFF>
DI void bload1_v(unsigned& d, unsigned voff, rsrc_t rs, unsigned soff) {
  asm volatile("buffer_load_dword %0, %1, %2, %3 offen offset:%4" : "+v"(d) : "v"(voff), "s"(rs), "s"(soff), "i"(OFF) : "memory");
}
template <int OFF>
DI void bload_lds(unsigned m0v, unsigned voff, rsrc_t rs, unsigned soff) {
  asm volatile("s_mov_b32 m0, %0\n\ts_nop 0\n\tbuffer_load_dwordx4 %1, %2, %3 offen offset:%4 lds" ::"s"(m0v), "v"(voff), "s"(rs), "s"(soff), "i"(OFF) : "memory");
}
DI void touch8k(unsigned m0v, unsigned voff, rsrc_t rs, unsigned soff) {
  asm volatile("s_mov_b32 m0, %0\n\ts_nop 0\n\tbuffer_load_dword %1, %2, %3 offen lds" ::"s"(m0v), "v"(voff), "s"(rs), "s"(soff) : "memory");
}
template <int OFF>
DI void bload_lds_sc1(unsigned m0v, unsigned voff, rsrc_t rs, unsigned soff) {
  asm volatile("s_mov_b32 m0, %0\n\ts_nop 0\n\tbuffer_load_dwordx4 %1, %2, %3 offen offset:%4 sc1 lds" ::"s"(m0v), "v"(voff), "s"(rs), "s"(soff), "i"(OFF) : "memory");
}
#define GPB ""
#define GPA "sc1"
#define DPW "sc1"
#define DPA "sc1"
template <int OFF>
DI void bload_lds_b(unsigned m0v, unsigned voff, rsrc_t rs, unsigned soff) {
  asm volatile("s_mov_b32 m0, %0\n\ts_nop 0\n\tbuffer_load_dwordx4 %1, %2, %3 offen offset:%4 " GPB " lds" ::"s"(m0v), "v"(voff), "s"(rs), "s"(soff), "i"(OFF) : "memory");
}
template <int OFF>
DI void bload_lds_a(unsigned m0v, unsigned voff, rsrc_t rs, unsigned soff) {
  asm volatile("s_mov_b32 m0, %0\n\ts_nop 0\n\tbuffer_load_dwordx4 %1, %2, %3 offen offset:%4 " GPA " lds" ::"s"(m0v), "v"(voff), "s"(rs), "s"(soff), "i"(OFF) : "memory");
}
template <int OFF>
DI void dsr128(u32x4& d, unsigned addr) { asm volatile("ds_read_b128 %0, %1 offset:%2" : "=v"(d) : "v"(addr), "i"(OFF) : "memory"); }
template <int OFF>
DI void dsr64(u32x2& d, unsigned addr) { asm volatile("ds_read_b64 %0, %1 offset:%2" : "=v"(d) : "v"(addr), "i"(OFF) : "memory"); }
template <int OFF>
DI void dsr_tr(u32x2& d, unsigned addr) { asm volatile("ds_read_b64_tr_b16 %0, %1 offset:%2" : "=v"(d) : "v"(addr), "i"(OFF) : "memory"); }
template <int OFF>
DI void dsw128(unsigned addr, u32x4 d) { asm volatile("ds_write_b128 %0, %1 offset:%2" ::"v"(addr), "v"(d), "i"(OFF) : "memory"); }
template <int OFF>
DI void dsw128a(unsigned addr, const f32x4& d) { asm volatile("ds_write_b128 %0, %1 offset:%2" ::"v"(addr), "a"(d), "i"(OFF) : "memory"); }
template <int OFF>
DI void dsw32(unsigned addr, unsigned d) { asm volatile("ds_write_b32 %0, %1 offset:%2" ::"v"(addr), "v"(d), "i"(OFF) : "memory"); }

DI void gst128(void* p, u32x4 v) {
  asm volatile("global_store_dwordx4 %0, %1, off sc1\n\ts_nop 1" ::"v"(p), "v"(v) : "memory");
}
DI f32x2 lo2(f32x4 v) { return __builtin_shufflevector(v, v, 0, 1); }
DI f32x2 hi2(f32x4 v) { return __builtin_shufflevector(v, v, 2, 3); }
DI float rowsum4(float v) {
  const unsigned u = __float_as_uint(v);
  const auto a = __builtin_amdgcn_permlane16_swap(u, u, false, false);
  const unsigned t = __float_as_uint(__uint_as_float(a[0]) + __uint_as_float(a[1]));
  const auto b = __builtin_amdgcn_permlane32_swap(t, t, false, false);
  return __uint_as_float(b[0]) + __uint_as_float(b[1]);
}
DI void split4(const f32x4 v, u32x2& hi, u32x2& lo) {
  hi[0] = pk_bf16(v[0], v[1]); hi[1] = pk_bf16(v[2], v[3]);
  lo[0] = pk_bf16(v[0] - bf_lo(hi[0]), v[1] - bf_hi(hi[0]));
  lo[1] = pk_bf16(v[2] - bf_lo(hi[1]), v[3] - bf_hi(hi[1]));
}
DI int slow_col(const void* p) { return (int)((3u - ((unsigned)(uintptr_t)p >> 7)) & 7u); }
DI float silu_f(float v) { return v / (1.0f + __expf(-v)); }

constexpr unsigned WS_G1M = 0x20000;
constexpr unsigned WS_G1S = 0x40000;
constexpr unsigned WS_G2 = 0x60000;
constexpr unsigned WS_STAT = 0x80000;
constexpr unsigned WS_FO = 0x141000;
constexpr unsigned WS_FC = 0x146000;
constexpr unsigned WS_FY = 0x148000;
constexpr unsigned WS_FH = 0x188000;
constexpr unsigned WS_FA = 0x1C8000;
constexpr unsigned WS_WC = 2u << 20;
constexpr unsigned WS_H = 4u << 20, WS_O = 8u << 20, WS_Y = 12u << 20, WS_ACT = 16u << 20, WS_X = 32u << 20, WS_X2 = 36u << 20;

template <int CTRL>
DI float dpp_get(float b) { return __uint_as_float(__builtin_amdgcn_update_dpp(0u, __float_as_uint(b), CTRL, 0xf, 0xf, true)); }
DI float swap32_add(float a, float b) {
  const auto sw = __builtin_amdgcn_permlane32_swap(__float_as_uint(a), __float_as_uint(b), false, false);
  return __uint_as_float(sw[0]) + __uint_as_float(sw[1]);
}
DI float swap16_add(float a, float b) {
  const auto sw = __builtin_amdgcn_permlane16_swap(__float_as_uint(a), __float_as_uint(b), false, false);
  return __uint_as_float(sw[0]) + __uint_as_float(sw[1]);
}
DI float scatter8(const f32x2 (&a)[4], int lane) {
  const float t0 = swap32_add(a[0][0], a[2][0]), t1 = swap32_add(a[0][1], a[2][1]), t2 = swap32_add(a[1][0], a[3][0]), t3 = swap32_add(a[1][1], a[3][1]);
  const float u0 = swap16_add(t0, t2), u1 = swap16_add(t1, t3);
  const bool b3 = lane & 8;
  return (b3 ? u1 : u0) + dpp_get<0x128>(b3 ? u0 : u1);
}
DI float sum4(const f32x2 (&a)[4]) {
  float t = swap16_add(swap32_add(a[0][0] + a[0][1], a[2][0] + a[2][1]), swap32_add(a[1][0] + a[1][1], a[3][0] + a[3][1]));
  t += dpp_get<0x128>(t); t += dpp_get<0x124>(t); t += dpp_get<0x122>(t); t += dpp_get<0x121>(t);
  return t;
}
DI int lane_id() {
  int l;
  asm volatile("v_mbcnt_lo_u32_b32 %0, -1, 0\n\tv_mbcnt_hi_u32_b32 %0, -1, %0" : "=v"(l));
  return l;
}
DI f32x2 unpk(unsigned u) { return f32x2{bf_lo(u), bf_hi(u)}; }
DI f32x2 unpk_v(unsigned u) {
  f32x2 r;
  asm volatile("v_lshlrev_b32 %0, 16, %1" : "=v"(r[0]) : "v"(u));
  asm volatile("v_and_b32 %0, 0xffff0000, %1" : "=v"(r[1]) : "v"(u));
  return r;
}

constexpr int Q_SLOT = 21504, Q_NSL = 7;
constexpr int WO_H0 = 4 * Q_SLOT;
constexpr int QX_OFF = Q_SLOT * Q_NSL + 1024;
constexpr int QH_OFF = QX_OFF + 8192;
constexpr int QT_OFF = QH_OFF + 1024;
constexpr int Q_LDS = 163840;
#define C3Q 1
#define C3O 1
#define POLLW 0
#define OD 3
#define SSL 14
#define SSI 4
#define OMB 6
template <int OFF>
DI void bload_sc1(u32x4& d, unsigned voff, rsrc_t rs, unsigned soff) {
  asm volatile("buffer_load_dwordx4 %0, %1, %2, %3 offen offset:%4 sc1" : "+v"(d) : "v"(voff), "s"(rs), "s"(soff), "i"(OFF) : "memory");
}
DI void bload2_sc1(u32x2& d, unsigned voff, rsrc_t rs, unsigned soff) {
  asm volatile("buffer_load_dwordx2 %0, %1, %2, %3 offen sc1" : "+v"(d) : "v"(voff), "s"(rs), "s"(soff) : "memory");
}
DI void bload1_sc1(unsigned& d, unsigned voff, rsrc_t rs) {
  asm volatile("buffer_load_dword %0, %1, %2, 0 offen sc1" : "+v"(d) : "v"(voff), "s"(rs) : "memory");
}
DI unsigned wo_voff(int lane, int odd, int sh) {
  const int q = (lane >> 3) & 3, h = lane >> 5, ch = lane & 7, g = (q >> 1) | (odd << 1);
  return (unsigned)(((h * 16 + q) << sh) + ((((ch >> 1) ^ g) * 16) + (ch & 1) * 8) * 2);
}
DI rsrc_t wo_rsrc(const bf16* wo, const char* wc, int cn) {
  const bool c3 = C3O && (cn & 7) == slow_col(wo);
  return mk_rsrc(c3 ? wc + 786432 + (cn >> 3) * 131072 : (const char*)wo + cn * 128, c3 ? 131072 : D_DIM * D_DIM * 2 - cn * 128);
}

#define QORD 1
#define DORD 2
#define XCL 2
#define XCM 0x11110u
#define XCS 1
#define XCP 6
#define QY 26
#define QBAR0 0
#define XWO 1
DI unsigned xoff(unsigned b) { return XCL == 1 && XWO ? WS_O + ((b >> 3) & 7) * 524288 + ((b & 7) * 4 + (b >> 6)) * 16384 : WS_X + b * 16384; }
struct Args { const bf16 *x, *c, *wmod, *wq, *wo, *wg, *wu; char* ws; };
#define CK 3
#define TW 8
#define TO 4
#define TT 2
#define PSL 8
#define PSI 4
DI void gstore2_sc1(u32x2 d, unsigned voff, rsrc_t rs) {
  asm volatile("buffer_store_dwordx2 %0, %1, %2, 0 offen sc1" ::"v"(d), "v"(voff), "s"(rs) : "memory");
}
DI void gstore4_sc1(u32x4 d, unsigned voff, rsrc_t rs, unsigned soff) {
  asm volatile("buffer_store_dwordx4 %0, %1, %2, %3 offen sc1\n\ts_nop 1" ::"v"(d), "v"(voff), "s"(rs), "s"(soff) : "memory");
}
DI void bload_us(unsigned& d, unsigned voff, rsrc_t rs, unsigned soff) {
  asm volatile("buffer_load_ushort %0, %1, %2, %3 offen" : "+v"(d) : "v"(voff), "s"(rs), "s"(soff) : "memory");
}
template <int W0, int W1>
DI void gemv8(float* red, int lane, const u32x4 (&d)[8], unsigned cv) {
  wait_vm<W0>();
  const unsigned sv = __float_as_uint(silu_f(unpk_v(cv)[0]));
  f32x2 a[4] = {{0, 0}, {0, 0}, {0, 0}, {0, 0}};
  static_for<8>([&](auto J) {
    constexpr int j = J.value;
    if constexpr (j == 4 && W1 != W0) wait_vm<W1>();
    const float s = __uint_as_float(__builtin_amdgcn_update_dpp(__builtin_amdgcn_update_dpp(0u, sv, 0x150 + j, 0xf, 0x3, false), sv, 0x158 + j, 0xf, 0xc, false));
#pragma unroll
    for (int e = 0; e < 4; ++e) a[e] += s * unpk_v(d[j][e]);
  });
  red[(lane & 7) * 8 + (lane >> 3)] = scatter8(a, lane);
}
struct Pre { unsigned o1, n1, vo, st; };
DI unsigned front1(char* smem, unsigned smem_base, int lane, int w4, int bm, int bn, const Args& g, const Pre& p) {
  const bool gv = ((bm + bn) & 1) == 0;
  u32x4 d[8];
  unsigned tag, cv;
  asm volatile("" : "=v"(d[0]), "=v"(d[1]), "=v"(d[2]), "=v"(d[3]), "=v"(d[4]), "=v"(d[5]), "=v"(d[6]), "=v"(d[7]), "=v"(tag), "=v"(cv));
  {
    const rsrc_t rs = mk_rsrc((const char*)(gv ? g.wmod : g.x) + p.o1, p.n1), rsC = mk_rsrc(g.c, 2048);
#pragma unroll
    for (int i = 0; i < 4; ++i) bload_v<0>(d[i], p.vo, rs, i * p.st);
    bload_us(cv, lane * 2, rsC, (bm >> 2) * 512 + w4 * 128);
    bload1_v<0>(tag, 0u, mk_rsrc(g.ws, 4), 0);
#pragma unroll
    for (int i = 4; i < 8; ++i) bload_v<0>(d[i], p.vo, rs, i * p.st);
  }
  STAMPT(0, 6, 192);
  __builtin_amdgcn_s_barrier();
  float* red = reinterpret_cast<float*>(smem + QX_OFF);
  if (gv) {
    gemv8<4, 0>(red + w4 * 64, lane, d, cv);
  } else {
    f32x2 v[4][8], t[4];
    asm volatile("s_waitcnt vmcnt(4)" ::"v"(cv) : "memory");
#pragma unroll
    for (int r = 0; r < 4; ++r) {
      if (r == 2) wait_vm<0>();
#pragma unroll
      for (int e = 0; e < 8; ++e) v[r][e] = unpk_v(d[r * 2 + (e >> 2)][e & 3]);
      t[r] = ((v[r][0] + v[r][1]) + (v[r][2] + v[r][3])) + ((v[r][4] + v[r][5]) + (v[r][6] + v[r][7]));
    }
    const float mean = sum4(t) * (1.0f / D_DIM);
#pragma unroll
    for (int r = 0; r < 4; ++r) {
      const float m = __uint_as_float(__builtin_amdgcn_readlane(__float_as_uint(mean), r * 16));
      f32x2 q0 = {0, 0}, q1 = {0, 0};
#pragma unroll
      for (int e = 0; e < 8; e += 2) { const f32x2 e0 = v[r][e] - m, e1 = v[r][e + 1] - m; q0 += e0 * e0; q1 += e1 * e1; }
      t[r] = q0 + q1;
    }
    const float rstd = rsqrtf(sum4(t) * (1.0f / D_DIM) + 1e-5f);
    if ((lane & 15) == 0) *reinterpret_cast<f32x2*>(red + w4 * 8 + (lane >> 3)) = f32x2{rstd, -mean * rstd};
  }
  asm volatile("ds_add_u32 %1, %2" : "+v"(tag) : "v"(smem_base + QT_OFF), "v"(1u) : "memory");
  STAMPT(0, 1, 192);
  tag = __builtin_amdgcn_readfirstlane(tag) + 0x9E3779B9u;
  const unsigned o = gv ? WS_G1M + (w4 >> 1) * 65536 + ((bm & 2) * 2 + (bm >> 2)) * 8192 + (bn ^ 1) * 512 + (w4 & 1) * 256 : WS_G1S + w4 * 32768 + (bm * 128 + (bn >> 1) * 16) * 16;
  const rsrc_t rsG = mk_rsrc(g.ws + o, 256);
  const f32x2* rp = reinterpret_cast<const f32x2*>(red) + (lane & 15) + (gv ? (w4 & 1) * 16 : 0);
  unsigned n;
  do {
    asm volatile("ds_read_b32 %0, %1\n\ts_waitcnt lgkmcnt(0)" : "=v"(n) : "v"(smem_base + QT_OFF) : "memory");
  } while (__builtin_amdgcn_readfirstlane(n) != 256u);
  f32x2 m = rp[0];
  const f32x2 m2 = (m + rp[32]) + (rp[64] + rp[96]);
  m = gv ? m2 : m;
  gstore4_sc1(u32x4{__float_as_uint(m[0]), tag, __float_as_uint(m[1]), tag}, lane * 16, rsG, 0);
  STAMPT(0, 2, 192);
  return tag;
}
DI void front_ocld(int lane, int w4, int bm, int bn, const Args& g, u32x4 (&oc)[8], unsigned& c2) {
  const rsrc_t rs = mk_rsrc((const char*)g.wmod + ((bm & 3) * 4 + w4) * 786432 + 4096 + (bm >> 2) * 2048 + (bn ^ 2) * 128, 63 * 12288 + 128);
  asm volatile("" : "=v"(c2));
  bload_us(c2, lane * 2, mk_rsrc(g.c, 2048), (bm & 3) * 512 + w4 * 128);
#pragma unroll
  for (int i = 0; i < 8; ++i) {
    wait_vm<TO - 1>();
    asm volatile("" : "=v"(oc[i]));
    bload_v<0>(oc[i], (lane >> 3) * (8 * 12288) + (lane & 7) * 16, rs, i * 12288);
  }
}
template <int NW>
DI void front_oc(char* smem, int lane, int w4, const u32x4 (&oc)[8], unsigned c2) {
  gemv8<NW, NW>(reinterpret_cast<float*>(smem + QX_OFF + 1024) + w4 * 64, lane, oc, c2);
  asm volatile("s_waitcnt lgkmcnt(0)" ::: "memory");
}

DI void front_h(char* smem, unsigned smem_base, int lane, int wid, int bm, int bn, const Args& g, unsigned tag) {
  const int fr = lane & 15, fq = lane >> 4;
  const int kA = ((bn ^ 3) * 2) ^ (wid & 1), kB = ((bn ^ 3) * 2) ^ (2 + wid), f0 = (wid >> 1) * 4;
  u32x4 xa[5], xb[9];
  const unsigned hc = bm == 0 ? 1u : 0u;
  const unsigned la = smem_base + (wid & 1) * Q_SLOT + f0 * 1024 + lane * 16, lb = smem_base + (2 + wid) * Q_SLOT + lane * 16;
  {
    unsigned n;
    do {
      asm volatile("ds_read_b32 %0, %1 offset:4\n\ts_waitcnt lgkmcnt(0)" : "=v"(n) : "v"(smem_base + QT_OFF) : "memory");
    } while (__builtin_amdgcn_readfirstlane(n) != 192u);
  }
  const unsigned lx = fr * 64 + (fq ^ ((fr >> 1) & 2)) * 16 - lane * 16;
  static_for<5>([&](auto I) { dsr128<I.value * 1024>(xa[I.value], la + lx); });
  static_for<9>([&](auto I) { dsr128<I.value * 1024>(xb[I.value], lb + lx); });
  f32x2 fa[5][4], fb[9][4];
  asm volatile("s_waitcnt lgkmcnt(9)" ::: "memory");
#pragma unroll
  for (int i = 0; i < 5; ++i)
#pragma unroll
    for (int e = 0; e < 4; ++e) fa[i][e] = unpk_v(xa[i][e]);
  asm volatile("s_waitcnt lgkmcnt(0)" ::: "memory");
#pragma unroll
  for (int i = 0; i < 9; ++i)
#pragma unroll
    for (int e = 0; e < 4; ++e) fb[i][e] = unpk_v(xb[i][e]);
  STAMP(0, 4);
  u32x4 pm[4], ps[3];
  {
    const rsrc_t rsM = mk_rsrc(g.ws + WS_G1M + (bm >> 3) * 65536, 65536), rsS = mk_rsrc(g.ws + WS_G1S + (bn >> 2) * 32768, 32768);
    const unsigned vm = ((fr >> 2) & 1) * 32768 + (fr >> 3) * 8192 + fq * 64 + (fr & 3) * 16;
    const unsigned vs0 = (bm * 128 - 16 + lane + (lane < 16 ? hc * 16 : 0)) * 16, vs1 = (bm * 128 + 48 + lane) * 16, vs2 = (bm * 128 + 112 + fr) * 16;
    if (PSL) __builtin_amdgcn_s_sleep(PSL);
    unsigned spins = 0;
    for (;;) {
      asm volatile("buffer_load_dwordx4 %4, %8, %12, 0 offen sc1\n\tbuffer_load_dwordx4 %5, %9, %12, 0 offen sc1\n\tbuffer_load_dwordx4 %6, %10, %12, 0 offen sc1\n\t"
                   "buffer_load_dwordx4 %0, %7, %11, %13 offen sc1\n\tbuffer_load_dwordx4 %1, %7, %11, %14 offen sc1\n\t"
                   "buffer_load_dwordx4 %2, %7, %11, %15 offen sc1\n\tbuffer_load_dwordx4 %3, %7, %11, %16 offen sc1\n\t"
                   "s_waitcnt vmcnt(0)"
                   : "=&v"(pm[0]), "=&v"(pm[1]), "=&v"(pm[2]), "=&v"(pm[3]), "=&v"(ps[0]), "=&v"(ps[1]), "=&v"(ps[2])
                   : "v"(vm), "v"(vs0), "v"(vs1), "v"(vs2), "s"(rsM), "s"(rsS), "s"(kA * 256), "s"(kA * 256 + 16384), "s"(kB * 256), "s"(kB * 256 + 16384) : "memory");
      unsigned bad = 0;
#pragma unroll
      for (int i = 0; i < 4; ++i) bad |= (pm[i][1] ^ tag) | (pm[i][3] ^ tag);
#pragma unroll
      for (int i = 0; i < 3; ++i) bad |= (ps[i][1] ^ tag) | (ps[i][3] ^ tag);
      asm volatile("" : "+v"(bad));
      if (__all(bad == 0u) || ++spins > (1u << 18)) break;
      if (PSI) __builtin_amdgcn_s_sleep(PSI);
    }
  }
  STAMP(0, 3);
  f32x2 rs[9], ra[5];
  {
    u32x2* sp = reinterpret_cast<u32x2*>(smem + QX_OFF + 2048 + wid * 1536);
#pragma unroll
    for (int i = 0; i < 3; ++i) sp[i * 64 + lane] = u32x2{ps[i][0], ps[i][2]};
    __builtin_amdgcn_wave_barrier();
#pragma unroll
    for (int i = 0; i < 5; ++i) ra[i] = __builtin_bit_cast(f32x2, sp[(f0 + i) * 16 + fr]);
#pragma unroll
    for (int i = 0; i < 9; ++i) rs[i] = __builtin_bit_cast(f32x2, sp[i * 16 + fr]);
  }
  auto cols = [&](const u32x4& p0, const u32x4& p1, f32x2 (&sa)[4], f32x2 (&ca)[4]) {
    float m0 = __uint_as_float(p0[0]) + __uint_as_float(p1[0]), m1 = __uint_as_float(p0[2]) + __uint_as_float(p1[2]);
    const float one = (fr & 4) ? 1.0f : 0.0f;
    m0 = (m0 + dpp_get<0x128>(m0)) + one; m1 = (m1 + dpp_get<0x128>(m1)) + one;
    static_for<4>([&](auto E) {
      constexpr int e = E.value;
      sa[e] = f32x2{dpp_get<0x150 + e>(m0), dpp_get<0x150 + e>(m1)};
      ca[e] = f32x2{dpp_get<0x154 + e>(m0), dpp_get<0x154 + e>(m1)};
    });
  };
  auto frag = [&](const f32x2 (&xf)[4], const f32x2 r, const f32x2 (&sa)[4], const f32x2 (&ca)[4]) {
    u32x4 ho;
#pragma unroll
    for (int e = 0; e < 4; ++e) { const f32x2 h = (xf[e] * r[0] + r[1]) * ca[e] + sa[e]; ho[e] = pk_bf16(h[0], h[1]); }
    return ho;
  };
  f32x2 sa[4], ca[4];
  cols(pm[0], pm[1], sa, ca);
  const rsrc_t rsH = mk_rsrc(g.ws + WS_H + ((bm * 8 + f0) * 32 + kA) * 1024, 3 * 32768 + 1024);
  static_for<5>([&](auto I) {
    constexpr int i = (I.value + 1) % 5;
    const u32x4 ho = frag(fa[i], ra[i], sa, ca);
    if constexpr (i > 0) gstore4_sc1(ho, lane * 16, rsH, (i - 1) * 32768);
    dsw128<i * 1024>(i == 0 && f0 ? smem_base + QH_OFF + lane * 16 : la, ho);
  });
  cols(pm[2], pm[3], sa, ca);
  static_for<9>([&](auto I) { dsw128<I.value * 1024>(lb, frag(fb[I.value], rs[I.value], sa, ca)); });
  STAMP(0, 5);
  wait_vm<0>();
  asm volatile("buffer_store_dword %0, %1, %2, 0 offen sc1" ::"v"(tag), "v"(lane * 32768), "s"(mk_rsrc(g.ws + WS_FH + (bm * 16 + bn) * 128 + wid * 4, 7 * 32768 + 4)) : "memory");
  STAMP(1, 1);
}

DI unsigned qkv_loader(char* smem, unsigned smem_base, int lane, int sw, int bm, int bn, int cn, const Args& g) {
  const int m0 = bm * 128, n0 = bn * 64;
  const unsigned u0 = ((bn ^ 3) * 2);
  const char* wc = g.ws + WS_WC;
  unsigned m0a = smem_base + sw * 3072, m0w = smem_base + 9216 + sw * 4096;
  auto wrow = [&](int i, unsigned sec) {
    const int r = i * 8 + (lane >> 3), ch = lane & 7;
    const int gg = ((r >> 1) & 1) | (((r >> 3) & 1) << 1);
    return (unsigned)((r * 3 + sw) * sec + ((((ch >> 1) ^ gg) * 16) + (ch & 1) * 8) * 2);
  };
  int fb0 = (m0 >> 4) - 1 + 3 * sw; fb0 = fb0 < 0 ? 0 : fb0;
  {
    const rsrc_t rsX = mk_rsrc(g.x, S_LEN * D_DIM * 2);
    const unsigned vx = (lane >> 2) * 2048 + ((lane & 3) ^ ((lane >> 3) & 2)) * 16;
#pragma unroll
    for (int i = 0; i < 18; ++i) {
      const int u = (i / 6) * 2 + (i & 1), r = (i >> 1) % 3;
      if (i >= TW) wait_vm<TW - 1>();
      bload_lds<0>(m0a + u * Q_SLOT + r * 1024, vx, rsX, (r ? (m0 >> 4) - 1 + 3 * sw + r : fb0) * 32768 + (u ^ u0) * 64);
    }
    const rsrc_t rsW0 = mk_rsrc((const char*)g.wq + n0 * 2, D_DIM * 3 * D_DIM * 2 - n0 * 2);
    const unsigned voffW0[2] = {wrow(0, 2048), wrow(1, 2048)};
#pragma unroll
    for (int u = 0; u < Q_NSL - 1; ++u)
#pragma unroll
      for (int i = 0; i < 4; ++i) {
        wait_vm<TW - 1>();
        if (u * 4 + i == TW - 1) asm volatile("ds_add_u32 %0, %1 offset:4" ::"v"(smem_base + QT_OFF), "v"(1u) : "memory");
        bload_lds<0>(m0w + u * Q_SLOT + i * 1024, voffW0[i & 1], rsW0, ((u ^ u0) * 2u + (i >> 1)) * 98304u);
      }
  }
  u32x4 oc[8];
  unsigned c2;
  front_ocld(lane, sw, bm, bn, g, oc, c2);
  const rsrc_t rsT = mk_rsrc((const char*)g.wq + (blockIdx.x * 3 + sw) * 8192, 8192);
    wait_vm<TT>();
  touch8k(smem_base + QH_OFF, lane * 128u, rsT, 0);
  const rsrc_t rsH = mk_rsrc(g.ws + WS_H, S_LEN * D_DIM * 2);
  const bool c3 = C3Q && (bn & 7) == slow_col(g.wq);
  const rsrc_t rsW = mk_rsrc(c3 ? wc + (n0 >> 9) * 393216 : (const char*)g.wq + n0 * 2, c3 ? 393216 : D_DIM * 3 * D_DIM * 2 - n0 * 2);
  const unsigned sec = c3 ? 128u : 2048u, p16 = sec * 48u;
  unsigned voffH0 = (unsigned)(fb0 * 32768 + lane * 16);
  unsigned voffH1 = (unsigned)(((m0 >> 4) + 3 * sw) * 32768 + lane * 16);
  unsigned voffW[2] = {wrow(0, sec), wrow(1, sec)};
  asm volatile("" : "+v"(voffH0), "+v"(voffH1), "+v"(voffW[0]), "+v"(voffW[1]));
  asm volatile("" : "+s"(m0a), "+s"(m0w));
#define Q_LOADH(SOFF, U)                                                                                         \
  do {                                                                                                           \
    const unsigned so_ = ((unsigned)(U) ^ u0) * 1024u;                                                           \
    bload_lds_sc1<0>(m0a + (SOFF), voffH0, rsH, so_);                                                            \
    bload_lds_sc1<0>(m0a + (SOFF) + 1024, voffH1, rsH, so_);                                                     \
    bload_lds_sc1<0>(m0a + (SOFF) + 2048, voffH1, rsH, so_ + 32768u);                                            \
  } while (0)
#define Q_LOADW(SOFF, U)                                                                                         \
  do {                                                                                                           \
    _Pragma("unroll") for (int i = 0; i < 4; ++i)                                                                \
      bload_lds_sc1<0>(m0w + (SOFF) + i * 1024, voffW[i & 1], rsW, (((unsigned)(U) ^ u0) * 2u + (i >> 1)) * p16); \
  } while (0)
  front_oc<1>(smem, lane, sw, oc, c2);
  __builtin_amdgcn_s_barrier();
  Q_LOADW(6 * Q_SLOT, 6);
  __builtin_amdgcn_s_barrier();
  Q_LOADW(0, 7); Q_LOADW(Q_SLOT, 8);
  __builtin_amdgcn_s_barrier();
  Q_LOADH(6 * Q_SLOT, 6); Q_LOADH(0, 7); Q_LOADH(Q_SLOT, 8);
  Q_LOADH(2 * Q_SLOT, 9); Q_LOADW(2 * Q_SLOT, 9); Q_LOADH(3 * Q_SLOT, 10); Q_LOADW(3 * Q_SLOT, 10);
  wait_vm<17>();
  __builtin_amdgcn_s_barrier();
  unsigned sb = 4 * Q_SLOT;
  for (int u = 11; u < 31; u += 2) {
#pragma unroll
    for (int t = 0; t < 2; ++t) {
      Q_LOADH(sb, u + t);
      Q_LOADW(sb, u + t);
      sb = (sb == (Q_NSL - 1) * Q_SLOT) ? 0u : sb + Q_SLOT;
    }
    wait_vm<21>();
    __builtin_amdgcn_s_barrier();
  }
  Q_LOADH(3 * Q_SLOT, 31); Q_LOADW(3 * Q_SLOT, 31);
  const rsrc_t rsO = wo_rsrc(g.wo, wc, cn);
  const int sh = (C3O && (cn & 7) == slow_col(g.wo)) ? 7 : 11;
  const unsigned voffO[2] = {wo_voff(lane, sw & 1, sh), wo_voff(lane, (sw & 1) ^ 1, sh)};
  static_for<6>([&](auto U) {
    constexpr int u = U.value;
#pragma unroll
    for (int j = 0; j < 7; ++j) {
      const int pc = (u < 3 ? u * 21 : 64 + (u - 3) * 21) + 3 * j;
      const unsigned p = pc + sw;
      bload_lds_sc1<0>(smem_base + (u < 3 ? WO_H0 : -65536) + p * 1024u, voffO[pc & 1], rsO, ((p >> 2) * 32u + (p & 3) * 4u) << sh);
    }
    if constexpr (u == 0 || u == 2) wait_vm<21>();
    if constexpr (u == 0 || u == 2 || u >= 4) __builtin_amdgcn_s_barrier();
  });
  unsigned tag;
#pragma unroll
  for (int i = 0; i < (XCL == 2 ? 4 : 0); ++i) gstore4_sc1(u32x4{~0u, ~0u, ~0u, ~0u}, lane * 16, mk_rsrc(g.ws + WS_X + blockIdx.x * 16384 + sw * 4096, 4096), i * 1024);
  asm volatile("ds_read_b32 %0, %1 offset:512\n\ts_waitcnt vmcnt(%2) lgkmcnt(0)" : "=v"(tag) : "v"(smem_base + QH_OFF), "n"(XCL == 2 ? 4 : 0) : "memory");
  return __builtin_amdgcn_readfirstlane(tag);
}
namespace d4 {
struct Tile { int kh, bm, bn; };
DI Tile tile() {
  int b = blockIdx.x;
  asm volatile("" : "+s"(b));
  const int xcd = b & 7, idx = b >> 3;
  return Tile{xcd & 1, ((xcd >> 1) & 1) * 8 + (idx & 7), (xcd >> 2) * 4 + (idx >> 3)};
}
}
#define FTO 3
#define CL0 4
#define TLBT 0
#define TLBU -1
namespace w7 {
constexpr int UD = 24;
constexpr int ops(int u) {
  int c = (TLBT && u == TLBU) + (u == 26) + (u == 32) + (u == DWO) + (XCL == 0 && u >= CL0 && u < CL0 + 16) + (u == CL0 + 16);
  for (int j = 0; j < 2 * ((TQ + 7) / 8); ++j) c += DW0 + (j * DWS) / 2 == u;
  return c;
}
constexpr int after(int v, int u) { int c = 0; for (int t = v + 1; t < u; ++t) c += ops(t); return c > 63 ? 63 : c; }
}
DI unsigned qkv_spare(char* smem, unsigned smem_base, int lane, int bm, int bn, int cn, const Args& g, const bf16* wd, const bf16* out) {
  using namespace w7;
  u32x4 cp[4], oc[8];
  unsigned tag, c2;
  const rsrc_t rsP = mk_rsrc((const char*)g.wo + (size_t)blockIdx.x * 8192, 8192);
  auto tlbt = [&]() {
    if (TLBT) {
      const int gq = lane >> 3, i = lane & 7, idx = blockIdx.x >> 3;
      const char* b = gq < 3 ? g.ws : (gq == 3 ? (const char*)out : (gq == 4 ? (const char*)g.wg : (gq == 5 ? (const char*)g.wu : (gq == 6 ? (const char*)wd : (const char*)g.wo))));
      unsigned o;
      if (TLBT == 1) o = gq < 3 ? lane * 2097152u : i * (gq == 3 ? 599040u : (gq == 7 ? 299520u : 1048448u));
      else o = gq < 3 ? (lane * 32u + idx) * 65536u : (i * 32u + idx) * (gq == 3 ? 16384u : (gq == 7 ? 8192u : 28672u));
      asm volatile("s_mov_b32 m0, %0\n\ts_nop 0\n\tglobal_load_lds_dword %1, off" ::"s"(smem_base + QH_OFF), "v"(b + o) : "memory");
    }
  };
  {
    const rsrc_t rsQ = mk_rsrc((const char*)g.wq + (size_t)blockIdx.x * 24576, 24576);
    const unsigned vc = ((lane >> 3) & 3) * 2048 + (lane >> 5) * 1024 + (lane & 7) * 16;
    asm volatile("" : "=v"(cp[0]), "=v"(cp[1]), "=v"(cp[2]), "=v"(cp[3]), "=v"(tag));
#pragma unroll
    for (int i = 0; i < 3; ++i) bload_v<0>(cp[i], vc, rsQ, i * 8192 + slow_col(g.wq) * 128);
    bload_v<0>(cp[3], vc, rsP, slow_col(g.wo) * 128);
    bload1_v<0>(tag, 0u, mk_rsrc(g.ws, 4), 0);
    if (FTO) {
      const unsigned o = lane < 16 ? WS_FY + blockIdx.x * 2048 + lane * 128 : (lane < 18 ? WS_FA + blockIdx.x * 256 + (lane - 16) * 128 : WS_FO + blockIdx.x * 64);
      asm volatile("s_mov_b32 m0, %0\n\ts_nop 0\n\tbuffer_load_dword %1, %2, 0 offen sc1 lds" ::"s"(smem_base + QH_OFF), "v"(lane < ((FTO & 2) ? 0 : 16) || lane > 18 ? 1u << 30 : o), "s"(mk_rsrc(g.ws, 2u << 20)) : "memory");
    }
    if (TLBU < 0) tlbt();
    asm volatile("s_waitcnt vmcnt(%5)" : "+v"(cp[0]), "+v"(cp[1]), "+v"(cp[2]), "+v"(cp[3]), "+v"(tag) : "n"((FTO ? 1 : 0) + (TLBT && TLBU < 0 ? 1 : 0)) : "memory");
    tag = __builtin_amdgcn_readfirstlane(tag) + 0x9E3779B9u;
    const rsrc_t rsC = mk_rsrc(g.ws + WS_WC, 1 << 20);
#pragma unroll
    for (int i = 0; i < 3; ++i) gstore4_sc1(cp[i], (lane >> 5) * 393216 + (lane & 31) * 16, rsC, (blockIdx.x * 3 + i) * 512);
    gstore4_sc1(cp[3], (lane >> 5) * 131072 + (lane & 31) * 16, rsC, 786432 + blockIdx.x * 512);
  }
  wait_vm<0>();
  asm volatile("buffer_store_dword %0, %1, %2, 0 offen sc1" ::"v"(tag), "v"(lane * 4), "s"(mk_rsrc(g.ws + WS_FC + blockIdx.x * 16, 4)) : "memory");
  STAMPT(1, 3, 448);
  front_ocld(lane, 3, bm, bn, g, oc, c2);
  front_oc<0>(smem, lane, 3, oc, c2);
  STAMPT(1, 4, 448);
  if ((C3Q && (bn & 7) == slow_col(g.wq)) || (C3O && (cn & 7) == slow_col(g.wo))) {
    const rsrc_t rsF = mk_rsrc(g.ws + WS_FC, 4096);
    unsigned spins = 0, f0, f1, f2, f3;
    do {
      if (spins) __builtin_amdgcn_s_sleep(8);
      asm volatile("buffer_load_dword %0, %4, %5, 0 offen sc1\n\tbuffer_load_dword %1, %4, %5, 0 offen offset:1024 sc1\n\t"
                   "buffer_load_dword %2, %4, %5, 0 offen offset:2048 sc1\n\tbuffer_load_dword %3, %4, %5, 0 offen offset:3072 sc1\n\ts_waitcnt vmcnt(0)"
                   : "=&v"(f0), "=&v"(f1), "=&v"(f2), "=&v"(f3) : "v"(lane * 16), "s"(rsF) : "memory");
    } while (!__all(((f0 ^ tag) | (f1 ^ tag) | (f2 ^ tag) | (f3 ^ tag)) == 0u) && ++spins < (1u << 16));
  }
  STAMPT(1, 0, 448);
  const rsrc_t rsX = mk_rsrc(g.ws + WS_G2 + (bn ^ 2) * 512, 131072);
  const rsrc_t rsO = wo_rsrc(g.wo, g.ws + WS_WC, cn);
  const int sh = (C3O && (cn & 7) == slow_col(g.wo)) ? 7 : 11;
  const unsigned voffO = wo_voff(lane, 1, sh);
  __builtin_amdgcn_s_barrier();
  const rsrc_t rsG = mk_rsrc((const char*)g.wg + (size_t)blockIdx.x * (TQ * 1024), TQ * 1024);
  const rsrc_t rsU = mk_rsrc((const char*)g.wu + (size_t)blockIdx.x * (TQ * 1024), TQ * 1024);
  static_for<33>([&](auto U) {
    constexpr int u = U.value;
    (void)&rsG; (void)&rsU; (void)&rsP; (void)&rsX; (void)&oc;
    if constexpr (u == 0) {
      dsw32<512>(smem_base + QH_OFF, tag);
      const float* rp = reinterpret_cast<const float*>(smem + QX_OFF + 1024) + lane;
      gstore2_sc1(u32x2{__float_as_uint((rp[0] + rp[64]) + (rp[128] + rp[192])), tag}, bm * 8192 + lane * 8, rsX);
    }
    if constexpr (u == CK) {
      const rsrc_t rsF = mk_rsrc(g.ws + WS_FH + (blockIdx.x & 7) * 32768 + (bm == 0 ? 0 : (bm - 1) * 2048), 4096);
      unsigned spins = 0, f0, f1;
      do {
        asm volatile("buffer_load_dword %0, %2, %3, 0 offen sc1\n\tbuffer_load_dword %1, %2, %3, %4 offen sc1\n\ts_waitcnt vmcnt(0)"
                     : "=&v"(f0), "=&v"(f1) : "v"((lane >> 2) * 128 + (lane & 3) * 4), "s"(rsF), "s"(bm ? 2048 : 0) : "memory");
      } while (!__all(f0 == tag && f1 == tag) && ++spins < (1u << 18));
      STAMPT(1, 2, 448);
    }
    if constexpr (u == UD) wait_vm<after(CL0 + 16, u)>();
    if constexpr (u == 26) bload_lds_sc1<0>(smem_base + WO_H0 + 63 * 1024, voffO, rsO, (15 * 32u + 12u) << sh);
    if constexpr (u == 32) bload_lds_sc1<0>(smem_base + 63 * 1024, voffO, rsO, (31 * 32u + 12u) << sh);
    if constexpr (u == TLBU) tlbt();
    if constexpr (u == DWO) touch8k(smem_base + QH_OFF, lane * 128u, rsP, 0);
#pragma unroll
    for (int j = 0; j < 2 * ((TQ + 7) / 8); ++j)
      if (DW0 + (j * DWS) / 2 == u) touch8k(smem_base + QH_OFF, lane * 128u, (j & 1) ? rsU : rsG, (j >> 1) * 8192u);
    if constexpr (XCL == 0 && u >= CL0 && u < CL0 + 16) gstore4_sc1(u32x4{~0u, ~0u, ~0u, ~0u}, lane * 16, mk_rsrc(g.ws + WS_X + blockIdx.x * 16384, 16384), (u - CL0) * 1024);
    if constexpr (u == CL0 + 16) gstore4_sc1(u32x4{~0u, ~0u, ~0u, ~0u}, lane * 16, mk_rsrc(g.ws + WS_STAT + (bm * 16 + bn) * 1024, 1024), 0);
    if constexpr ((u & 1) || u == 32) __builtin_amdgcn_s_barrier();
  });
  {
    const rsrc_t rsS = mk_rsrc(g.ws + WS_G2, 131072);
    const unsigned vc = cn * 512 + (lane >> 5) * 32768 + (lane & 31) * 16, vd = 98304 + d4::tile().bn * 1024 + lane * 16;
    u32x4 pm[12];
    unsigned spins = 0;
    for (;;) {
      asm volatile("buffer_load_dwordx4 %0, %12, %14, 0 offen sc1\n\tbuffer_load_dwordx4 %1, %12, %14, %15 offen sc1\n\t"
                   "buffer_load_dwordx4 %2, %12, %14, %16 offen sc1\n\tbuffer_load_dwordx4 %3, %12, %14, %17 offen sc1\n\t"
                   "buffer_load_dwordx4 %4, %12, %14, %18 offen sc1\n\tbuffer_load_dwordx4 %5, %12, %14, %19 offen sc1\n\t"
                   "buffer_load_dwordx4 %6, %12, %14, %20 offen sc1\n\tbuffer_load_dwordx4 %7, %12, %14, %21 offen sc1\n\t"
                   "buffer_load_dwordx4 %8, %13, %14, 0 offen sc1\n\tbuffer_load_dwordx4 %9, %13, %14, %15 offen sc1\n\t"
                   "buffer_load_dwordx4 %10, %13, %14, %16 offen sc1\n\tbuffer_load_dwordx4 %11, %13, %14, %17 offen sc1\n\ts_waitcnt vmcnt(0)"
                   : "=&v"(pm[0]), "=&v"(pm[1]), "=&v"(pm[2]), "=&v"(pm[3]), "=&v"(pm[4]), "=&v"(pm[5]), "=&v"(pm[6]), "=&v"(pm[7]),
                     "=&v"(pm[8]), "=&v"(pm[9]), "=&v"(pm[10]), "=&v"(pm[11])
                   : "v"(vc), "v"(vd), "s"(rsS), "s"(8192), "s"(16384), "s"(24576), "s"(65536), "s"(73728), "s"(81920), "s"(90112) : "memory");
      unsigned bad = 0;
#pragma unroll
      for (int i = 0; i < 12; ++i) bad |= (pm[i][1] ^ tag) | (pm[i][3] ^ tag);
      if (__all(bad == 0u) || ++spins > (1u << 16)) break;
    }
#pragma unroll
    for (int i = 0; i < (XCL == 2 ? 4 : 0); ++i) gstore4_sc1(u32x4{~0u, ~0u, ~0u, ~0u}, lane * 16, mk_rsrc(g.ws + WS_X + blockIdx.x * 16384 + 12288, 4096), i * 1024);
    f32x2 m[3];
#pragma unroll
    for (int v = 0; v < 3; ++v)
#pragma unroll
      for (int e = 0; e < 2; ++e)
        m[v][e] = (__uint_as_float(pm[v * 4][2 * e]) + __uint_as_float(pm[v * 4 + 1][2 * e])) + (__uint_as_float(pm[v * 4 + 2][2 * e]) + __uint_as_float(pm[v * 4 + 3][2 * e]));
    f32x2* tp = reinterpret_cast<f32x2*>(smem + QT_OFF);
    tp[(lane >> 5) * 64 + (lane & 31)] = m[0];
    if (lane < 32) tp[32 + lane] = m[1] + 1.0f;
    tp[128 + lane] = m[2];
  }
  asm volatile("s_waitcnt lgkmcnt(0)" ::: "memory");
  return tag;
}

DI void qkv_mfma(char* smem, unsigned smem_base, int tid, int wid, int m0, int n0, bf16* O, unsigned* oflags, unsigned tag) {
  const int lane = tid & 63;
  const int wm = wid & 1, wn = wid >> 1;
  const int fr = lane & 15, fq = lane >> 4;
  unsigned rad[3][4];
  {
    const int q = (lane >> 2) & 3, p = lane & 3;
    const int r = 8 * fq + q;
    const int g = ((r >> 1) & 1) | (((r >> 3) & 1) << 1);
#pragma unroll
    for (int i = 0; i < 3; ++i) {
      rad[i][3] = smem_base + i * (3 * Q_SLOT) + lane * 16; rad[i][2] = rad[i][3] + 1024 + wm * 4096;
#pragma unroll
      for (int jj = 0; jj < 2; ++jj) rad[i][jj] = smem_base + i * (3 * Q_SLOT) + 9216 + r * 128 + (((wn * 2 + (jj ^ wm)) ^ g) * 32) + 8 * p;
      asm volatile("" : "+v"(rad[i][0]), "+v"(rad[i][1]), "+v"(rad[i][2]), "+v"(rad[i][3]));
    }
  }
  f32x4 acc[4][6], acch[2];
#pragma unroll
  for (int i = 0; i < 4; ++i)
#pragma unroll
    for (int j = 0; j < 6; ++j) { acc[i][j] = f32x4{0, 0, 0, 0}; asm volatile("" : "+v"(acc[i][j])); }
#pragma unroll
  for (int j = 0; j < 2; ++j) { acch[j] = f32x4{0, 0, 0, 0}; asm volatile("" : "+v"(acch[j])); }
  asm volatile("s_waitcnt lgkmcnt(0)" ::: "memory");
  __builtin_amdgcn_s_barrier();
  STAMPDECL;
  STAMPNW(2, 1);
  u32x4 a[2][5];
  u32x2 bl[2][6], bh[2][6];
  auto rdb = [&](auto PP, auto GG, auto RR) {
    constexpr int jj = RR.value >> 2, j = GG.value * 2 + jj, k = (RR.value >> 1) & 1, sl = (PP.value * 2 + k) % Q_NSL;
    constexpr int off = (sl % 3) * Q_SLOT + GG.value * 4096 + (RR.value & 1) * 512;
    if constexpr (RR.value & 1) dsr_tr<off>(bh[k][j], rad[sl / 3][jj]);
    else dsr_tr<off>(bl[k][j], rad[sl / 3][jj]);
  };
  auto gap = [&](auto PP, auto NN) {
    constexpr int n = NN.value, k = ~n & 1, sl = (PP.value * 2 + k) % Q_NSL;
    if constexpr (n < QY + 8) rdb(PP, ic<0>{}, ic<(n - QY) & 7>{});
    else if constexpr (n == 34 || n == 35) dsr128<(sl % 3) * Q_SLOT>(a[k][4], rad[sl / 3][3]);
    else if constexpr (n >= 36 && (n & 2)) dsr128<(sl % 3) * Q_SLOT + ((n - 36) >> 2) * 1024>(a[k][((n - 36) >> 2) & 3], rad[sl / 3][2]);
    else if constexpr (n >= 36) rdb(PP, ic<1>{}, ic<((n - 36) >> 2) * 2 + (n & 1)>{});
  };
  static_for<52 - QY>([&](auto N) { gap(ic<0>{}, ic<N.value + QY>{}); });
  static_for<16>([&](auto PP) {
    static_for<52>([&](auto N) {
      constexpr int n = N.value, m = n < 36 ? n : n - 4, k = (n ^ (n >> 1)) & 1;
      constexpr int i = (m & 16) ? 3 - ((m >> 2) & 3) : (m >> 2) & 3, j = (m >> 4) * 2 + (((m + 2) >> 2) & 1);
      (void)&acc; (void)&acch; (void)&a; (void)&bl; (void)&bh;
      if constexpr (n < 16 && n % 4 == 0) asm volatile("s_waitcnt lgkmcnt(%0)" ::"n"(n < 12 ? 12 : 8) : "memory");
      if constexpr (n == QY) {
        asm volatile("s_waitcnt lgkmcnt(0)" ::: "memory");
        __builtin_amdgcn_s_barrier();
      }
      constexpr int jb = n < 32 || n >= 36 ? j : (n < 34 ? 2 : 4);
      const u32x4 b = {bl[k][jb][0], bl[k][jb][1], bh[k][jb][0], bh[k][jb][1]};
      if constexpr (n < 32) MFMA_AV(acc[i][j], b, a[k][i]);
      else if constexpr (n < 34) MFMA_AV(acch[0], b, a[k][4]);
      else if constexpr (n < 36) MFMA_AV(acch[1], a[k][4], b);
      else MFMA_AV(acc[i][j], a[k][i], b);
      if constexpr (n < 8) rdb(PP, ic<2>{}, N);
      if constexpr (n >= QY && PP.value < 15) gap(ic<PP.value + 1>{}, N);
    });
  });
  asm volatile("s_waitcnt vmcnt(0) lgkmcnt(0)\n\ts_nop 15" ::: "memory");
  SCHED0();
  __builtin_amdgcn_s_setprio(0);
  STAMP(2, 2); STAMPFLUSH(2, 1);
#pragma unroll
  for (int i = 0; i < 4; ++i)
#pragma unroll
    for (int j = 0; j < 6; ++j) asm volatile("" : "+v"(acc[i][j]));
  asm volatile("" : "+v"(acch[0]), "+v"(acch[1]));
  auto pk2 = [](const f32x4 v) { return u32x2{pk_bf16(v[0], v[1]), pk_bf16(v[2], v[3])}; };
  auto cat = [](const u32x2 a, const u32x2 b) { return __builtin_bit_cast(bf16x8, u32x4{a[0], a[1], b[0], b[1]}); };
  u32x2 kp[5][2], vp[5][2];
  {
    char* xb = smem + QX_OFF + wn * 4096 + lane * 8;
#pragma unroll
    for (int f = 0; f < 2; ++f) { kp[4][f] = pk2(acc[3][2 + f]); vp[4][f] = pk2(acc[3][4 + f]); }
    *reinterpret_cast<u32x2*>(xb + wm * 512) = pk2(acch[0]);
    *reinterpret_cast<u32x2*>(xb + 1024 + wm * 512) = pk2(acch[1]);
    if (wm == 0) {
#pragma unroll
      for (int f = 0; f < 2; ++f) {
        *reinterpret_cast<u32x2*>(xb + 2048 + f * 512) = kp[4][f];
        *reinterpret_cast<u32x2*>(xb + 2048 + 1024 + f * 512) = vp[4][f];
      }
    }
    asm volatile("s_waitcnt lgkmcnt(0)" ::: "memory");
    __builtin_amdgcn_s_barrier();
    asm volatile("" ::: "memory");
#pragma unroll
    for (int f = 0; f < 2; ++f) {
      kp[0][f] = *reinterpret_cast<const u32x2*>(xb + wm * 2048 + (f ^ wm) * 512);
      vp[0][f] = *reinterpret_cast<const u32x2*>(xb + wm * 2048 + 1024 + (f ^ wm) * 512);
    }
  }
  STAMP(2, 3);
#pragma unroll
  for (int i = 0; i < 3; ++i)
#pragma unroll
    for (int f = 0; f < 2; ++f) { kp[1 + i][f] = pk2(acc[i][2 + f]); vp[1 + i][f] = pk2(acc[i][4 + f]); }
  const bool par = fr & 1;
  const bool cura = fq * 4 + (fr & 1) <= fr, curb = fq * 4 + 2 + (fr & 1) <= fr;
  const unsigned mh = par ? 0xffff0000u : 0x0000ffffu;
  const unsigned mca = cura ? mh : 0u, mpa = cura ? 0u : mh, mcb = curb ? mh : 0u, mpb = curb ? 0u : mh;
  const float cs = 0.17677669529663687f * 1.4426950408889634f;
  char* op = (char*)O + ((size_t)((m0 >> 6) + wm) * (D_DIM / 32) + (n0 >> 5) + wn) * 4096 + lane * 16;
  const f32x4 z4 = {0, 0, 0, 0};
#pragma unroll
  for (int ii = 0; ii < 4; ++ii) {
    const int i = 3 - ii;
    const bf16x8 qf = cat(pk2(acc[i][0]), pk2(acc[i][1]));
    const f32x4 sc = __builtin_amdgcn_mfma_f32_16x16x32_bf16(cat(kp[i + 1][0], kp[i + 1][1]), qf, z4, 0, 0, 0);
    f32x4 sp = __builtin_amdgcn_mfma_f32_16x16x32_bf16(cat(kp[i][0], kp[i][1]), qf, z4, 0, 0, 0);
    if (i == 0 && m0 == 0 && wm == 0) sp = f32x4{-5.656854249e9f, -5.656854249e9f, -5.656854249e9f, -5.656854249e9f};
    const float la = cura ? (par ? sc[1] : sc[0]) : (par ? sp[1] : sp[0]);
    const float lb = curb ? (par ? sc[3] : sc[2]) : (par ? sp[3] : sp[2]);
    float mx = fmaxf(la, lb);
    {
      const auto a = __builtin_amdgcn_permlane16_swap(__float_as_uint(mx), __float_as_uint(mx), false, false);
      mx = fmaxf(__uint_as_float(a[0]), __uint_as_float(a[1]));
      const auto b = __builtin_amdgcn_permlane32_swap(__float_as_uint(mx), __float_as_uint(mx), false, false);
      mx = fmaxf(__uint_as_float(b[0]), __uint_as_float(b[1]));
    }
    const float ea = __builtin_amdgcn_exp2f((la - mx) * cs), eb = __builtin_amdgcn_exp2f((lb - mx) * cs);
    const float inv = __builtin_amdgcn_rcpf(rowsum4(ea + eb));
    const unsigned wa = pk_bf16(ea, ea), wb = pk_bf16(eb, eb);
    const bf16x8 pf = __builtin_bit_cast(bf16x8, u32x4{wa & mpa, wb & mpb, wa & mca, wb & mcb});
    const f32x4 o0 = __builtin_amdgcn_mfma_f32_16x16x32_bf16(cat(vp[i][0], vp[i + 1][0]), pf, z4, 0, 0, 0) * inv;
    const f32x4 o1 = __builtin_amdgcn_mfma_f32_16x16x32_bf16(cat(vp[i][1], vp[i + 1][1]), pf, z4, 0, 0, 0) * inv;
    const u32x2 u0 = pk2(o0), u1 = pk2(o1);
    gst128(op + i * 1024, wm ? u32x4{u1[0], u1[1], u0[0], u0[1]} : u32x4{u0[0], u0[1], u1[0], u1[1]});
  }
  STAMP(2, 4);
  wait_vm<0>();
  asm volatile("buffer_store_dword %0, %1, %2, 0 offen sc1" ::"v"(tag), "v"(lane * (4096 * OFS)), "s"(mk_rsrc(oflags + ((m0 >> 1) + wm * 32 + (n0 >> 5) + wn) * OFS, (ORP - 1) * 4096 * OFS + 4)) : "memory");
  STAMP(2, 5);
}

#define X2S 2
#define X2P 0
DI void x2_store(const u32x4 (&x2p)[2], const char* ws, int lane, int w, int own) {
  const rsrc_t rsR = mk_rsrc(ws + WS_X2 + (((own >> 4) * 8 + (w & 3) * 2 + (w >> 2)) * 16 + (own & 15)) * 2048, 2048);
  gstore4_sc1(x2p[0], lane * 16, rsR, 0); gstore4_sc1(x2p[1], lane * 16, rsR, 1024);
}
DI void o_phase(char* smem, unsigned smem_base, int lane, int w, int cm, int cn, const bf16* O, const unsigned* oflags,
                const bf16* __restrict__ x, bf16* Y, float* stat, const char* ws, u32x4 (&x2p)[2], unsigned tag) {
  const int wm = w & 3, kh = w >> 2;
  const int fr = lane & 15, fq = lane >> 4;
  const int R = cm * 8 + wm * 2 + kh;
  u32x2 xv[4];
  {
    const rsrc_t rsX = mk_rsrc(x, S_LEN * D_DIM * 2);
    const unsigned vx = (unsigned)(((R * 16 + fr) * D_DIM + cn * 64 + fq * 4) * 2);
#pragma unroll
    for (int f = 0; f < 4; ++f) asm volatile("" : "=v"(xv[f]));
    bload2_v<0>(xv[0], vx, rsX, 0); bload2_v<32>(xv[1], vx, rsX, 0); bload2_v<64>(xv[2], vx, rsX, 0); bload2_v<96>(xv[3], vx, rsX, 0);
  }
  const unsigned hb = smem_base + (kh ? 0 : WO_H0);
  unsigned brd[4];
  {
    const int q = (lane >> 2) & 3, p = lane & 3;
    const int r = 8 * fq + q;
    const int g = ((r >> 1) & 1) | (((r >> 3) & 1) << 1);
#pragma unroll
    for (int f = 0; f < 4; ++f) brd[f] = hb + r * 128 + ((f ^ g) * 32) + 8 * p;
  }
  const rsrc_t rsA = mk_rsrc((const char*)O + (size_t)(cm * 2 + (wm >> 1)) * 131072 + kh * 65536, 65536);
  unsigned va0 = lane * 16 + ((wm & 1) * 2 + kh) * 1024, va1 = va0 ^ 1024;
  unsigned tsrc = smem_base + QT_OFF + fq * 16, xw = hb + lane * 16 + wm * 4096, xr = smem_base + (kh ? WO_H0 : 0) + lane * 16 + wm * 4096;
  char* yb = (char*)Y + ((size_t)(R >> 4) * 32 + cn * 2 + (fq & 1)) * 16384 + (R & 15) * 1024 + ((fq >> 1) * 16 + fr) * 16;
  asm volatile("" : "+v"(tsrc), "+v"(xw), "+v"(xr), "+v"(yb), "+v"(va0), "+v"(va1), "+v"(brd[0]), "+v"(brd[1]), "+v"(brd[2]), "+v"(brd[3]));
  f32x4 acc[2][4];
#pragma unroll
  for (int i = 0; i < 2; ++i)
#pragma unroll
    for (int f = 0; f < 4; ++f) { acc[i][f] = f32x4{0, 0, 0, 0}; asm volatile("" : "+v"(acc[i][f])); }
  if (w == POLLW) {
    const rsrc_t rsF = mk_rsrc(oflags + ((blockIdx.x & (ORP - 1)) * 1024 + cm * 64) * OFS, 256 * OFS);
    unsigned spins = 0, f;
    do {
      asm volatile("buffer_load_dword %0, %1, %2, 0 offen sc1\n\ts_waitcnt vmcnt(0)" : "=v"(f) : "v"(lane * (4 * OFS)), "s"(rsF) : "memory");
    } while (!__all(f == tag) && ++spins < (1u << 18));
  }
  __builtin_amdgcn_s_barrier();
  STAMPDECL;
  STAMPNW(3, 1);
  u32x4 a[16][2], gt[4], ct[4], st[4];
  u32x2 bl[2][4], bh[2][4];
  auto rdb = [&](auto S) {
    constexpr int s = S.value;
#pragma unroll
    for (int f = 0; f < 4; ++f) { dsr_tr<s * 4096>(bl[s & 1][f], brd[f]); dsr_tr<s * 4096 + 512>(bh[s & 1][f], brd[f]); }
  };
  auto lda = [&](auto S) {
    constexpr int s = S.value;
    asm volatile("" : "=v"(a[s][0]), "=v"(a[s][1]));
    bload_sc1<0>(a[s][0], va0, rsA, s * 4096);
    bload_sc1<0>(a[s][1], va1, rsA, s * 4096);
  };
  static_for<OD>([&](auto S) {
    lda(S);
    if constexpr (S.value == 0) rdb(ic<0>{});
    if constexpr (S.value == 1) rdb(ic<1>{});
  });
  static_for<16>([&](auto S) {
    constexpr int s = S.value;
    asm volatile("s_waitcnt vmcnt(%0) lgkmcnt(8)" ::"n"(2 * ((s + OD - 1 < 15 ? s + OD - 1 : 15) - s)) : "memory");
    SCHED0();
    u32x4 bb[4];
#pragma unroll
    for (int f = 0; f < 4; ++f) bb[f] = u32x4{bl[s & 1][f][0], bl[s & 1][f][1], bh[s & 1][f][0], bh[s & 1][f][1]};
    SCHED0();
    if constexpr (s + OD < 16) lda(ic<(s + OD < 16 ? s + OD : 0)>{});
    if constexpr (s == OMB) __builtin_amdgcn_s_barrier();
#pragma unroll
    for (int n = 0; n < 8; ++n) MFMA_AV(acc[1 - (n >> 2)][n & 3], bb[n & 3], a[s][1 - (n >> 2)]);
    if constexpr (s < 14) rdb(ic<(s < 14 ? s + 2 : 0)>{});
    if constexpr (s == 14) {
      static_for<4>([&](auto F) { dsr128<F.value * 64>(gt[F.value], tsrc); dsr128<256 + F.value * 64>(ct[F.value], tsrc); });
    }
    SCHED0();
  });
  STAMP(3, 2); STAMPFLUSH(3, 1);
  asm volatile("s_nop 7" ::: "memory");
  static_for<4>([&](auto F) { dsw128<F.value * 1024>(xw, __builtin_bit_cast(u32x4, acc[1][F.value])); });
  asm volatile("s_waitcnt lgkmcnt(0)" ::: "memory");
  __builtin_amdgcn_s_barrier();
  u32x4 pr[4];
  static_for<4>([&](auto F) { dsr128<F.value * 1024>(pr[F.value], xr); });
  static_for<4>([&](auto F) { dsr128<512 + F.value * 64>(st[F.value], tsrc); });
#pragma unroll
  for (int f = 0; f < 4; ++f) asm volatile("" : "+v"(acc[0][f]), "+v"(xv[f]), "+v"(gt[f]), "+v"(ct[f]));
  asm volatile("s_waitcnt lgkmcnt(4)" : "+v"(pr[0]), "+v"(pr[1]), "+v"(pr[2]), "+v"(pr[3])::"memory");
  f32x4 fin[4];
#pragma unroll
  for (int f = 0; f < 4; ++f) {
    const f32x4 xf = {bf_lo(xv[f][0]), bf_hi(xv[f][0]), bf_lo(xv[f][1]), bf_hi(xv[f][1])};
    fin[f] = xf + __builtin_bit_cast(f32x4, gt[f]) * (acc[0][f] + __builtin_bit_cast(f32x4, pr[f]));
  }
  const rsrc_t rsS = mk_rsrc((char*)stat + R * 2048, 2048);
  {
    const f32x4 s4 = (fin[0] + fin[1]) + (fin[2] + fin[3]);
    const float sm = rowsum4((s4[0] + s4[1]) + (s4[2] + s4[3]));
    const float mb = sm * (1.0f / 64.0f);
    f32x4 q4 = {0, 0, 0, 0};
#pragma unroll
    for (int f = 0; f < 4; ++f) { const f32x4 d = fin[f] - mb; q4 += d * d; }
    const float m2 = rowsum4((q4[0] + q4[1]) + (q4[2] + q4[3]));
    gstore2_sc1(u32x2{min(__float_as_uint(sm), ~1u), min(__float_as_uint(m2), ~1u)}, lane < 16 ? cn * 128 + lane * 8 : 1u << 30, rsS);
  }
  STAMP(3, 3);
#pragma unroll
  for (int j = 0; j < 2; ++j) {
    x2p[j] = u32x4{pk_bf16(fin[2 * j][0], fin[2 * j][1]), pk_bf16(fin[2 * j][2], fin[2 * j][3]), pk_bf16(fin[2 * j + 1][0], fin[2 * j + 1][1]), pk_bf16(fin[2 * j + 1][2], fin[2 * j + 1][3])};
    asm volatile("" : "+v"(x2p[j]));
  }
  if (X2P == 0) x2_store(x2p, ws, lane, w, cm * 16 + cn);
  u32x2 tv[4];
  {
    if (SSL) __builtin_amdgcn_s_sleep(SSL);
    unsigned spins = 0;
    for (;;) {
      asm volatile("buffer_load_dwordx2 %0, %4, %5, 0 offen sc1\n\tbuffer_load_dwordx2 %1, %4, %5, 0 offen offset:512 sc1\n\t"
                   "buffer_load_dwordx2 %2, %4, %5, 0 offen offset:1024 sc1\n\tbuffer_load_dwordx2 %3, %4, %5, 0 offen offset:1536 sc1\n\ts_waitcnt vmcnt(0) lgkmcnt(0)"
                   : "=&v"(tv[0]), "=&v"(tv[1]), "=&v"(tv[2]), "=&v"(tv[3]) : "v"(lane * 8), "s"(rsS) : "memory");
      const unsigned mx = max(max(max(tv[0][0], tv[0][1]), max(tv[1][0], tv[1][1])), max(max(tv[2][0], tv[2][1]), max(tv[3][0], tv[3][1])));
      if (!__any(mx == ~0u) || ++spins > (1u << 18)) break;
      if (SSI) __builtin_amdgcn_s_sleep(SSI);
    }
  }
  STAMP(3, 6);
  const float mean = rowsum4((__uint_as_float(tv[0][0]) + __uint_as_float(tv[1][0])) + (__uint_as_float(tv[2][0]) + __uint_as_float(tv[3][0]))) * (1.0f / D_DIM);
  float m2 = 0;
#pragma unroll
  for (int i = 0; i < 4; ++i) { const float d = __uint_as_float(tv[i][0]) * (1.0f / 64.0f) - mean; m2 += __uint_as_float(tv[i][1]) + 64.0f * d * d; }
  const float rstd = rsqrtf(rowsum4(m2) * (1.0f / D_DIM) + 1e-5f), rm = -mean * rstd;
  u32x2 pk[4];
#pragma unroll
  for (int f = 0; f < 4; ++f) {
    asm volatile("" : "+v"(st[f]));
    const f32x4 yv = (fin[f] * rstd + rm) * __builtin_bit_cast(f32x4, ct[f]) + __builtin_bit_cast(f32x4, st[f]);
    pk[f] = u32x2{pk_bf16(yv[0], yv[1]), pk_bf16(yv[2], yv[3])};
  }
#pragma unroll
  for (int j = 0; j < 2; ++j) {
    const auto s0 = __builtin_amdgcn_permlane16_swap(pk[j][0], pk[j + 2][0], false, false);
    const auto s1 = __builtin_amdgcn_permlane16_swap(pk[j][1], pk[j + 2][1], false, false);
    gst128(yb + j * 512, u32x4{s0[0], s1[0], s0[1], s1[1]});
  }
  if (X2P == 1) x2_store(x2p, ws, lane, w, cm * 16 + cn);
}

#define EPR 3
#define TPOS 62
#define GMB 0
#define GLX 12
#define GLP 0
#define EPS 3
#define EPS4 0
#define EST 1
#define GD4 4
#define GD3 3
struct Stg { rsrc_t rA, rB; unsigned vA, vB, sA, sB, dA, dB, e1, e3; };
#define G3_STAGE(SLOT, P)                                                                                  \
  do {                                                                                                     \
    if ((P) == 0) bload_lds_a<0>(m0w + (SLOT) * 32768, st.vA, st.rA, st.sA);                             \
    else if ((P) == 1) { bload_lds_a<1024>(m0w + (SLOT) * 32768, st.vA, st.rA, st.sA + st.e1); st.sA += st.dA; } \
    else if ((P) == 2) bload_lds_b<0>(m0w + (SLOT) * 32768 + 16384, st.vB, st.rB, st.sB);                    \
    else { bload_lds_b<0>(m0w + (SLOT) * 32768 + 16384 + 1024, st.vB, st.rB, st.sB + st.e3); st.sB += st.dB; } \
  } while (0)

#define DNF 24
namespace d4 {
constexpr int NF = DNF, NH = 56 - 2 * NF, N = NF + NH, NE = 12, FR = 32768;
constexpr int eg(int s) { return (s / 6) * 2 + (s & 1); }
constexpr int ef(int s) { return (s % 6) >> 1; }
constexpr int apos(int s) { return s < NE ? eg(s) * 3 + ef(s) : s + NE; }
constexpr int wpos(int s, int k) { return s < NE ? (eg(s) * 4 + k) * 7 + 4 + ef(s) : (s - NE) * 7 + k; }
struct WSrc { rsrc_t rs; unsigned v, f[3]; };
DI WSrc wsrc(const bf16* Wd, int j) {
  const Tile t = tile();
  const int lane = lane_id(), grp = t.bn * 2 + (j >> 1), ch = lane & 7, gg = ((lane >> 4) & 1) | ((j & 1) << 1);
  WSrc w;
  w.rs = mk_rsrc((const char*)Wd + grp * 128, F_DIM * D_DIM * 2 - grp * 128);
  w.v = (unsigned)(((j & 1) * 8 + (lane >> 3)) * 2048 + ((ch >> 1) ^ gg) * 32 + (ch & 1) * 16);
  w.f[0] = t.kh * (112 * FR); w.f[1] = t.kh * ((44 - NF) * 7 * FR); w.f[2] = 0;
  asm volatile("" : "+v"(w.v), "+s"(w.f[0]), "+s"(w.f[1]), "+s"(w.f[2]));
  return w;
}
constexpr int cls(int s) { return s < NE ? 0 : (s < NF ? 1 : 2); }
}

template <int FN>
DI void g3_body(char* smem, unsigned smem_base, int wm, int wn, int wid, int lane, int bm, int bn, Stg st,
                rsrc_t rsW, const bf16* Wd, char* ws) {
  const unsigned tmask = wm < 2 ? 0x1111111u : 0x1100000u;
  constexpr int NB = 2 * FN, NM = 8 * NB;
  const int fr = lane & 15, fq = lane >> 4;
  f32x4 acc[4][NB];
#pragma unroll
  for (int i = 0; i < 4; ++i)
#pragma unroll
    for (int j = 0; j < NB; ++j) { acc[i][j] = f32x4{0, 0, 0, 0}; asm volatile("" : "+v"(acc[i][j])); }
  unsigned aad[2], bad[2][FN];
  aad[0] = smem_base + wm * 4096 + lane * 16; aad[1] = aad[0] + 65536;
  {
    const int q = (lane >> 2) & 3, p = lane & 3;
    const int r = 8 * fq + q;
    const int sw = (r & 3) | (((r >> 3) & 1) << 2);
#pragma unroll
    for (int j = 0; j < FN; ++j) {
      const int gi = wn * 4 + j + (bn & 1);
      bad[0][j] = smem_base + 16384 + r * 256 + ((gi ^ sw) * 32) + 8 * p;
      bad[1][j] = bad[0][j] + 65536;
    }
  }
  u32x4 a[2][4];
  u32x2 bl[2][NB], bh[2][NB];
  const unsigned m0w = smem_base + wid * 2048;
  constexpr int GDS = FN == 4 ? GD4 : GD3;
  constexpr int L = NM - 16;
  auto rdb = [&](auto HH, auto GG, auto RR) {
    constexpr int j = GG.value * 2 + (RR.value >> 2), k = (RR.value >> 1) & 1, off = k * 32768 + (j / FN) * 8192 + (RR.value & 1) * 1024;
    if constexpr (RR.value & 1) dsr_tr<off>(bh[k][j], bad[HH.value][j % FN]);
    else dsr_tr<off>(bl[k][j], bad[HH.value][j % FN]);
  };
  auto row = [](int h, int n) constexpr { return ((n >> 4) ^ (FN == 4 ? h : 0)) & 1 ? 3 - ((n >> 2) & 3) : (n >> 2) & 3; };
  auto tail = [&](auto HH, auto NN) {
    constexpr int h = HH.value, n = NN.value;
    if constexpr (n & 2) dsr128<(~n & 1) * 32768 + row(h, n) * 1024>(a[~n & 1][row(h, n)], aad[h]);
    else rdb(HH, ic<FN - 2>{}, ic<(n >> 2) * 2 + (n & 1)>{});
  };
  unsigned vt = (lane & 31) * 128u + (lane >> 5) * 0x40000000u;
  const rsrc_t rsD = mk_rsrc((const char*)Wd + blockIdx.x * 28672, 28672);
  auto period = [&](auto HH, auto LAST, int u) {
    constexpr int h = HH.value;
    constexpr bool last = LAST.value && h == 1;
    constexpr int X = last ? GLX : 16;
    static_for<NM>([&](auto N) {
      constexpr int n = N.value, p = n >> 1, k = (n ^ p) & 1, i = row(h, n), j = (n >> 4) * 2 + (((p + 1) >> 1) & 1);
      (void)&acc; (void)&a; (void)&bl; (void)&bh; (void)&st; (void)&rsW; (void)&rsD; (void)&vt;
      if constexpr (last && FN == 3 && n == GLP) __builtin_amdgcn_s_setprio(EPR);
      if constexpr (n < X && n % 4 == 0) asm volatile("s_waitcnt lgkmcnt(%0)" ::"n"(n < 12 ? 12 : 8) : "memory");
      if constexpr (n == X) {
        asm volatile("s_waitcnt vmcnt(%0) lgkmcnt(0)" ::"n"(last ? 63 : 4) : "memory");
        __builtin_amdgcn_s_barrier();
      }
      if constexpr (n == L && !last) {
        wait_vm<4>();
        __builtin_amdgcn_s_barrier();
      }
      if constexpr (FN == 4 && h == 1 && n == TPOS && !last) {
        if (__builtin_expect((tmask >> u) & 1u, 0))
          touch8k(smem_base + QH_OFF, vt, u >= 20 ? rsD : rsW, u >= 20 ? (u - 20 + wm) * 4096u : (u + 8u) << 18);
      }
      if constexpr (LAST.value && h == 0 && n == 10) {
        const d4::WSrc w = d4::wsrc(Wd, wm);
        st.rA = st.rB = w.rs; st.vA = st.vB = w.v;
        st.sA = w.f[0] + (4 + wn * 14) * d4::FR; st.sB = st.sA + 28 * d4::FR; st.dA = st.dB = d4::FR;
        st.e3 = 7 * d4::FR; st.e1 = st.e3 - 1024;
      }
      MFMA_AV(acc[i][j], (u32x4{bl[k][j][0], bl[k][j][1], bh[k][j][0], bh[k][j][1]}), a[k][i]);
      if constexpr (n < 8) rdb(HH, ic<FN - 1>{}, ic<n & 7>{});
      if constexpr (last) {
        if constexpr (n > X && (n - X - 1) % GDS == 0 && (n - X - 1) / GDS < 4) G3_STAGE(2, (n - X - 1) / GDS);
      } else {
        if constexpr (n >= L) tail(ic<h ^ 1>{}, ic<n & 15>{});
        if constexpr (n >= 16 && n < L && (n & 15) < 8) rdb(ic<h ^ 1>{}, ic<(n >> 4) - 1>{}, ic<n & 7>{});
        constexpr int d = n >= L ? 4 * GDS + n - L - 1 : (n - 17 < 4 * GDS ? n - 17 : -1);
        if constexpr (d >= 0 && d % GDS == 0 && d / GDS < 8) G3_STAGE(2 * h + ((d / GDS >> 1) & 1), (d / GDS & 1) + (d / GDS < 4 ? 2 : 0));
      }
    });
  };
  static_for<8 * (FN - 2)>([&](auto RR) { rdb(ic<0>{}, ic<RR.value / 8>{}, ic<RR.value & 7>{}); });
#pragma unroll
  for (int j = 0; j < FN; ++j) asm volatile("" : "+v"(bad[1][j]));
  asm volatile("" : "+v"(aad[0]), "+v"(aad[1]), "+v"(vt), "+s"(st.sA), "+s"(st.sB) : "s"(rsD));
  wait_vm<4>();
  __builtin_amdgcn_s_barrier();
  STAMPDECL;
  STAMPNW(5, 1);
  static_for<16>([&](auto RR) { tail(ic<0>{}, RR); });
  for (int u = 0; u < 28; u += 4) {
    period(ic<0>{}, ic<0>{}, u);
    period(ic<1>{}, ic<0>{}, u);
  }
  const int J0 = FN == 4 ? (24 + bn) * 4 : (bn >> 2) * 12 + (bn & 3);
  const rsrc_t rsS = mk_rsrc(ws + WS_ACT + ((bm * 4 + wm) * (F_DIM / 32) + (J0 >> 1)) * 4096 + (J0 & 1) * 512, 20480);
  unsigned vs = (fq & 1) * 1024 + ((fq >> 1) * 16 + fr) * 16;
  asm volatile("" : "+v"(vs) : "s"(rsS));
  period(ic<0>{}, ic<1>{}, 28);
  period(ic<1>{}, ic<1>{}, 28);
  asm volatile("s_nop 15" ::: "memory");
#pragma unroll
  for (int j = 0; j < NB; ++j) asm volatile("" : "+v"(acc[0][j]), "+v"(acc[1][j]), "+v"(acc[2][j]), "+v"(acc[3][j]));
  SCHED0();
  STAMP(5, 2);
  STAMPT(3, 5, 256);
  STAMPFLUSH(5, 1);
  if constexpr (FN == 3 && GLP >= NM) __builtin_amdgcn_s_setprio(EPR);
  static_for<FN>([&](auto JJ) {
    constexpr int j = JJ.value;
    (void)&vs; (void)&rsS;
    u32x2 pk[4];
    {
      f32x2 e[8], gu[8];
#pragma unroll
      for (int g = 0; g < 8; ++g) e[g] = ((g & 1) ? hi2(acc[g >> 1][j]) : lo2(acc[g >> 1][j])) * -1.4426950408889634f;
      SCHED0();
#pragma unroll
      for (int g = 0; g < 8; ++g) e[g] = f32x2{__builtin_amdgcn_exp2f(e[g][0]), __builtin_amdgcn_exp2f(e[g][1])};
      SCHED0();
#pragma unroll
      for (int g = 0; g < 8; ++g) e[g] = e[g] + 1.0f;
      SCHED0();
#pragma unroll
      for (int g = 0; g < 8; ++g) e[g] = f32x2{__builtin_amdgcn_rcpf(e[g][0]), __builtin_amdgcn_rcpf(e[g][1])};
      SCHED0();
#pragma unroll
      for (int g = 0; g < 8; ++g) gu[g] = (g & 1) ? hi2(acc[g >> 1][j]) * hi2(acc[g >> 1][FN + j]) : lo2(acc[g >> 1][j]) * lo2(acc[g >> 1][FN + j]);
      SCHED0();
#pragma unroll
      for (int g = 0; g < 8; ++g) gu[g] = gu[g] * e[g];
      SCHED0();
#pragma unroll
      for (int g = 0; g < 8; ++g) pk[g >> 1][g & 1] = pk_bf16(gu[g][0], gu[g][1]);
    }
#pragma unroll
    for (int i = 0; i < 4; i += 2) {
      const auto s0 = __builtin_amdgcn_permlane16_swap(pk[i][0], pk[i + 1][0], false, false);
      const auto s1 = __builtin_amdgcn_permlane16_swap(pk[i][1], pk[i + 1][1], false, false);
      asm volatile("buffer_store_dwordx4 %0, %1, %2, %3 offen offset:%4 sc1\n\ts_nop 1" ::"v"(u32x4{s0[0], s1[0], s0[1], s1[1]}), "v"(vs), "s"(rsS),
                   "s"(FN == 4 ? (j >> 1) * 4096 : j * 8192), "i"(i * 1024 + (FN == 4 ? (j & 1) * 512 : 0)) : "memory");
    }
  });
  STAMP(5, 3);
  STAMPT(5, 5, 256);
}
DI unsigned flag_off(unsigned b, unsigned wid) {
  const unsigned xcd = b & 7, idx = b >> 3;
  return ((idx & 7) * 2 + ((wid >> 1) & 1)) * 512 + (xcd * 4 + (idx >> 3)) * 16 + (wid & 1) * 8 + (wid >> 2) * 4;
}
DI void act_flag(char* ws, int lane, int wid, unsigned tag) {
  asm volatile("buffer_store_dword %0, %1, %2, 0 offen sc1" ::"v"(tag), "v"(lane * 8192), "s"(mk_rsrc(ws + WS_FA + flag_off(blockIdx.x, wid), 7 * 8192 + 4)) : "memory");
}

#define XRD 1
#define DSI 0
#define DPS (N - 3)
#define DFS 0
#define DSL 4
#define DLA 4
#define DMX 3
#define LPR 0
namespace d4 {
struct Sched { int tA[N], tW[N], eA[N], eW[N], eB, cnt[N + 2], last[N], tL; };
constexpr Sched sched() {
  Sched q = {};
  unsigned fr = 7u << 6;
  int nA = 0, nW = 6, c = 0;
  auto take = [&]() { int e = 0; while (!((fr >> e) & 1)) ++e; fr &= ~(1u << e); return e; };
  for (int s = 0; s < 6; ++s) { q.tW[s] = -1; q.eW[s] = s; }
  for (int t = 0; t <= N; ++t) {
    q.cnt[t] = c;
    if (nA >= NE - 2 && !q.tL) { q.tL = t; c += 1; }
    if (t == 1) fr |= 1u << 9;
    if (t >= 2) fr |= (1u << q.eA[t - 2]) | (1u << q.eW[t - 2]);
    for (int m = 0; m < DMX && fr; ++m) {
      const bool a = nA <= nW;
      const int s = a ? nA : nW;
      if (s >= N || s > t + DLA) break;
      if (a) { q.tA[s] = t; q.eA[s] = take(); c += s < NF ? 4 : 2; ++nA; } else { q.tW[s] = t; q.eW[s] = take(); c += 4; ++nW; }
      q.last[s] = c;
    }
    if (t == N - 1) q.eB = take();
  }
  q.cnt[N + 1] = c;
  return q;
}
constexpr Sched Q = sched();
template <int M, int S, int W = 0>
DI void dma(unsigned m0b, unsigned voff, rsrc_t rs, unsigned so) {
  unsigned t;
  if constexpr (W)
    asm volatile("s_add_u32 m0, %1, %5\n\ts_add_u32 %0, %4, %6\n\tbuffer_load_dwordx4 %2, %3, %0 offen " DPW " lds"
                 : "=&s"(t) : "s"(m0b), "v"(voff), "s"(rs), "s"(so), "n"(M), "n"(S) : "memory", "scc");
  else
    asm volatile("s_add_u32 m0, %1, %5\n\ts_add_u32 %0, %4, %6\n\tbuffer_load_dwordx4 %2, %3, %0 offen " DPA " lds"
                 : "=&s"(t) : "s"(m0b), "v"(voff), "s"(rs), "s"(so), "n"(M), "n"(S) : "memory", "scc");
}
}
DI void d4_loader(char* smem, unsigned smem_base, int lane, int j, const bf16* Wd, char* ws, unsigned tag) {
  using namespace d4;
  wait_vm<0>();
  act_flag(ws, lane, 4 + j, tag);
  if (blockIdx.x == 0 && j == 3) asm volatile("buffer_store_dword %0, %1, %2, 0 offen sc1" ::"v"(tag), "v"(lane * 4), "s"(mk_rsrc(ws, 4)) : "memory");
  STAMPT(5, 6, 256);
  __builtin_amdgcn_s_setprio(LPR);
  const Tile t = tile();
  const rsrc_t rsA = mk_rsrc(ws + WS_ACT + t.bm * 917504, 917504);
  const WSrc w = wsrc(Wd, j);
  unsigned vA = lane * 16 + j * 1024;
  const unsigned mA = smem_base + j * 1024, mW = smem_base + j * 2048;
  unsigned sO[3] = {t.kh * (458752u + NE * 8192), t.kh * (458752u + (44 - NF) * 8192), t.kh * 458752u};
  unsigned sP[2] = {458752u - t.kh * (458752u - NE * 8192), 458752u - t.kh * (458752u - (44 - NF) * 8192)};
  asm volatile("" : "+v"(vA));
  asm volatile("" : "+s"(sO[0]), "+s"(sO[1]), "+s"(sO[2]), "+s"(sP[0]), "+s"(sP[1]));
  const rsrc_t rsF = mk_rsrc(ws + WS_FA + t.bn * 8192, 8192);
  if (j == 0) {
    if (DSL) __builtin_amdgcn_s_sleep(DSL);
    unsigned spins = 0, f;
    do {
      asm volatile("buffer_load_dword %0, %1, %2, %3 offen offset:4 sc1\n\ts_waitcnt vmcnt(0)" : "=v"(f) : "v"((lane & 31) * 8), "s"(rsF), "s"(t.bm * 512 + t.kh * 256) : "memory");
    } while (!__all(f == tag) && ++spins < (1u << 18));
    dsw32<768>(smem_base + QT_OFF, 1u);
  } else {
    unsigned n;
    do {
      asm volatile("s_sleep 1\n\tds_read_b32 %0, %1 offset:768\n\ts_waitcnt lgkmcnt(0)" : "=v"(n) : "v"(smem_base + QT_OFF) : "memory");
    } while (__builtin_amdgcn_readfirstlane(n) == 0u);
  }
  STAMPT(6, 0, 256);
  unsigned fl;
  const int cn = t.bn * 2 + ((lane >> 2) & 1);
  const unsigned vF = lane < 2 * (44 - NF) ? t.bm * 512 + t.kh * ((NF - NE) * 16) + lane * 8
                    : (lane < 56 ? flag_off(blockIdx.x ^ 1, 4 + (lane & 3)) : flag_off(((cn & 7) * 4 + (t.bm & 3)) * 8 + (t.bm >> 2) * 2 + (cn >> 3), t.kh * 2 + (lane & 1) + (lane & 2) * 2));
  static_for<N>([&](auto T) {
    constexpr int i = T.value;
    (void)&fl;
    if constexpr (i == Q.tL) { asm volatile("" : "=v"(fl)); bload1_sc1(fl, vF, rsF); }
    if constexpr (i == Q.tA[NE]) {
      asm volatile("s_waitcnt vmcnt(%1)" : "+v"(fl) : "n"(Q.cnt[i] - Q.cnt[Q.tL] - 1) : "memory");
      if (__builtin_expect(!__all(fl == tag), 0)) {
        unsigned spins = 0, f;
        do {
          asm volatile("buffer_load_dword %0, %1, %2, 0 offen sc1\n\ts_waitcnt vmcnt(0)" : "=v"(f) : "v"(vF), "s"(rsF) : "memory");
        } while (!__all(f == tag) && ++spins < (1u << 18));
      }
    }
    static_for<N>([&](auto S) {
      constexpr int s = S.value;
      if constexpr (Q.tA[s] == i) {
        static_for<2>([&](auto K) {
          constexpr int k = K.value, m = Q.eA[s] * 16384 + k * 8192;
          if constexpr (s < NF) dma<m, (apos(s) * 2 + k) * 4096>(mA, vA, rsA, sP[cls(s) & 1]);
          dma<m + 4096, (apos(s) * 2 + k) * 4096>(mA, vA, rsA, sO[cls(s)]);
        });
      }
      if constexpr (Q.tW[s] == i) {
        static_for<4>([&](auto K) {
          constexpr int k = K.value;
          dma<Q.eW[s] * 16384 + (k >> 1) * 8192 + (k & 1) * 1024, wpos(s, k) * FR, 1>(mW, w.v, w.rs, w.f[cls(s)]);
        });
      }
    });
    wait_vm<Q.cnt[i + 1] - Q.last[i]>();
    __builtin_amdgcn_s_barrier();
  });
}
DI bool missing(const u32x4 (&pv)[4]) {
  unsigned m = 0;
#pragma unroll
  for (int f = 0; f < 4; ++f) m = max(max(m, max(pv[f][0], pv[f][1])), max(pv[f][2], pv[f][3]));
  return __any(m == ~0u);
}
DI void d4_mfma(char* smem, unsigned smem_base, int lane, int w, bf16* out, char* ws, unsigned tag) {
  using namespace d4;
  const Tile t = tile();
  const int fr = lane & 15, fq = lane >> 4, wr = w & 1, wc = w >> 1;
  unsigned brd[3][4], ard[3], vL = lane * 16;
  {
    const int q = (lane >> 2) & 3, p = lane & 3, g = (q >> 1) | ((fq & 1) << 1);
#pragma unroll
    for (int i = 0; i < 3; ++i) {
#pragma unroll
      for (int f = 0; f < 4; ++f) {
        brd[i][f] = smem_base + i * 65536 + wc * 4096 + (fq & 1) * 2048 + (fq >> 1) * 1024 + q * 128 + ((f ^ g) * 32) + 8 * p;
        asm volatile("" : "+v"(brd[i][f]));
      }
      ard[i] = smem_base + i * 65536 + wr * 2048 + lane * 16;
      asm volatile("" : "+v"(ard[i]));
    }
  }
  asm volatile("" : "+v"(vL));
  f32x4 acc[4][4];
#pragma unroll
  for (int i = 0; i < 4; ++i)
#pragma unroll
    for (int f = 0; f < 4; ++f) { acc[i][f] = f32x4{0, 0, 0, 0}; asm volatile("" : "+v"(acc[i][f])); }
  const rsrc_t rsX = mk_rsrc(ws + xoff(blockIdx.x) + w * 4096, 4096), rsP = mk_rsrc(ws + xoff(blockIdx.x ^ 1) + w * 4096, 4096);
  const unsigned bo = Q.eB * 16384 + w * 4096;
  unsigned tw[4], tr = smem_base + bo + (lane >> 3) * 128 + (((lane & 7) ^ (lane >> 3)) * 16);
  const rsrc_t rsO = mk_rsrc((char*)out + ((t.bm * 2 + t.kh) * 64 + wr * 32) * (D_DIM * 2) + t.bn * 256 + wc * 128, 31 * D_DIM * 2 + 128);
  unsigned vO = (lane >> 3) * (D_DIM * 2) + (lane & 7) * 16;
#pragma unroll
  for (int f = 0; f < 4; ++f) tw[f] = smem_base + bo + fr * 128 + (((f * 2 + (fq >> 1)) ^ (fr & 7)) * 16) + (fq & 1) * 8;
  asm volatile("" : "+v"(tw[0]), "+v"(tw[1]), "+v"(tw[2]), "+v"(tw[3]), "+v"(tr), "+v"(vO) : "s"(rsO));
  __builtin_amdgcn_s_setprio(2);
  u32x4 gt[4], xq[4], ex, pv[4];
  f32x2 base[16];
  const rsrc_t rsR = mk_rsrc(ws + WS_X2 + (((t.bm * 2 + t.kh) * 4 + wr * 2) * 16 + t.bn * 2 + wc) * 2048, 32768 + 2048);
  unsigned k32 = 32768;
  asm volatile("" : "+s"(k32) : "s"(rsR));
  static_for<4>([&](auto F) { dsr128<1024 + F.value * 64>(gt[F.value], smem_base + QT_OFF + wc * 256 + fq * 16); });
  asm volatile("s_waitcnt vmcnt(8) lgkmcnt(0)" ::: "memory");
  STAMP(5, 4);
  __builtin_amdgcn_s_barrier();
  STAMPDECL;
  STAMPNW(6, 1);
  u32x4 a[2][2][4];
  u32x2 bl[2][2][4], bh[2][2][4];
  auto rd = [&](auto S, auto RR) {
    constexpr int s = S.value, per = s < NF ? 12 : 10, k = RR.value / per, r = RR.value % per;
    if constexpr (r < 8) {
      constexpr int e = Q.eW[s], off = (e & 3) * 16384 + k * 8192 + (r & 1) * 512;
      if constexpr (r & 1) dsr_tr<off>(bh[s & 1][k][r >> 1], brd[e >> 2][r >> 1]);
      else dsr_tr<off>(bl[s & 1][k][r >> 1], brd[e >> 2][r >> 1]);
    } else {
      constexpr int e = Q.eA[s], i = s < NF ? r - 8 : r - 6;
      dsr128<(e & 3) * 16384 + k * 8192 + (i >> 1) * 4096 + (i & 1) * 1024>(a[s & 1][k][i], ard[e >> 2]);
    }
  };
  static_for<24>([&](auto RR) { rd(ic<0>{}, RR); });
  static_for<N>([&](auto S) {
    constexpr int s = S.value, nm = s < NF ? 32 : 16, nr = s + 1 == N ? 0 : (s + 1 < NF ? 24 : 20), pg = s < NF ? 1 : 2;
    WAIT_LGKM0();
    u32x4 bb[2][4];
#pragma unroll
    for (int k = 0; k < 2; ++k)
#pragma unroll
      for (int f = 0; f < 4; ++f) bb[k][f] = u32x4{bl[s & 1][k][f][0], bl[s & 1][k][f][1], bh[s & 1][k][f][0], bh[s & 1][k][f][1]};
    SCHED0();
    static_for<nm>([&](auto M) {
      constexpr int n = M.value;
      (void)&base; (void)&xq; (void)&rsR; (void)&k32; (void)&rsX; (void)&vL; (void)&ex; (void)&pv; (void)&rsP;
      if constexpr (n == 2 && s + 1 < N) { __builtin_amdgcn_s_barrier(); ALIGN(DAL); }
      if constexpr (DORD == 2) {
        constexpr int p = n >> 1, k = (n ^ p) & 1, f = s < NF ? p >> 2 : p >> 1, i = s < NF ? ((f & 1) ? 3 - (p & 3) : p & 3) : 2 + ((p ^ f) & 1);
        MFMA_AV(acc[i][f], bb[k][f], a[s & 1][k][i]);
      } else {
        constexpr int mi = s < NF ? (DORD && (n & 4) ? 3 - (n & 3) : n & 3) : 2 + ((n ^ (DORD ? n >> 1 : 0)) & 1);
        if constexpr (s < NF) MFMA_AV(acc[mi][(n >> 2) & 3], bb[n >> 4][(n >> 2) & 3], a[s & 1][n >> 4][mi]);
        else MFMA_AV(acc[mi][(n >> 1) & 3], bb[n >> 3][(n >> 1) & 3], a[s & 1][n >> 3][mi]);
      }
      static_for<pg>([&](auto G) {
        constexpr int r = (n - 2) * pg + G.value;
        if constexpr (n >= 2 && r < nr && XRD) rd(ic<(s + 1 < N ? s + 1 : 0)>{}, ic<(r >= 0 && r < nr ? r : 0)>{});
      });
      if constexpr (s == DFS && n == 28) { wait_vm<0>(); act_flag(ws, lane, w, tag); }
      if constexpr (s == NE + 1 && n >= 28) { asm volatile("" : "=v"(xq[n & 3])); bload_sc1<(n & 1) * 1024>(xq[n & 3], vL, rsR, (n & 2) ? k32 : 0u); }
      if constexpr (s == NF - 1 && n == 28) wait_vm<0>();
      if constexpr ((s == NF || s == NF + 1) && n >= 4 && n < 14) {
        constexpr int i = s - NF, p = (n - 4) / 5, h = (n - 4) % 5;
        if constexpr (h < 4) {
          asm volatile("v_cvt_pk_bf16_f32 %0, %1, %2\n\tv_min_u32 %0, -2, %0" : "=v"(ex[h]) : "v"(acc[i][2 * p + (h >> 1)][2 * (h & 1)]), "v"(acc[i][2 * p + (h >> 1)][2 * (h & 1) + 1]));
        } else {
          asm volatile("buffer_store_dwordx4 %0, %1, %2, 0 offen offset:%3 sc1" ::"v"(ex), "v"(vL), "s"(rsX), "i"(i * 2048 + p * 1024) : "memory");
        }
      }
      if constexpr (s == DPS && n >= 12) { asm volatile("" : "=v"(pv[n & 3])); bload_sc1<(n & 3) * 1024>(pv[n & 3], vL, rsP, 0); }
      if constexpr (s + 1 == N) base[n] = unpk_v(xq[n >> 2][n & 3]);
    });
    SCHED0();
  });
  asm volatile("s_nop 7\n\ts_waitcnt vmcnt(0) lgkmcnt(0)" : "+v"(gt[0]), "+v"(gt[1]), "+v"(gt[2]), "+v"(gt[3]), "+v"(pv[0]), "+v"(pv[1]), "+v"(pv[2]), "+v"(pv[3])::"memory");
  if (__builtin_expect(missing(pv), 0)) {
    unsigned spins = 0;
    do {
      asm volatile("buffer_load_dwordx4 %0, %4, %5, 0 offen sc1\n\tbuffer_load_dwordx4 %1, %4, %5, 0 offen offset:1024 sc1\n\t"
                   "buffer_load_dwordx4 %2, %4, %5, 0 offen offset:2048 sc1\n\tbuffer_load_dwordx4 %3, %4, %5, 0 offen offset:3072 sc1\n\t"
                   "s_waitcnt vmcnt(0)"
                   : "=&v"(pv[0]), "=&v"(pv[1]), "=&v"(pv[2]), "=&v"(pv[3]) : "v"(vL), "s"(rsP) : "memory");
    } while (missing(pv) && ++spins < (1u << 18));
  }
  STAMP(6, 2); STAMPFLUSH(6, 1);
  u32x4 ov[4];
  static_for<8>([&](auto E) {
    constexpr int e = E.value, i = e >> 2, f = e & 3;
    asm volatile("" : "+v"(acc[2 + i][f]));
    const unsigned p0 = pv[e >> 1][(e & 1) * 2], p1 = pv[e >> 1][(e & 1) * 2 + 1];
    const f32x4 v = f32x4{base[2 * e][0], base[2 * e][1], base[2 * e + 1][0], base[2 * e + 1][1]} + __builtin_bit_cast(f32x4, gt[f]) * (acc[2 + i][f] + f32x4{bf_lo(p0), bf_hi(p0), bf_lo(p1), bf_hi(p1)});
    asm volatile("ds_write_b64 %0, %1 offset:%2" ::"v"(tw[f]), "v"(u32x2{pk_bf16(v[0], v[1]), pk_bf16(v[2], v[3])}), "i"(i * 2048) : "memory");
    if constexpr (f == 3) { dsr128<i * 2048>(ov[2 * i], tr); dsr128<i * 2048 + 1024>(ov[2 * i + 1], tr); }
    if constexpr (e == 6) {
      asm volatile("s_waitcnt lgkmcnt(3)" : "+v"(ov[0]), "+v"(ov[1])::"memory");
      gstore4_sc1(ov[0], vO, rsO, 0); gstore4_sc1(ov[1], vO, rsO, 8 * D_DIM * 2);
    }
  });
  asm volatile("s_waitcnt lgkmcnt(0)" : "+v"(ov[2]), "+v"(ov[3])::"memory");
  gstore4_sc1(ov[2], vO, rsO, 16 * D_DIM * 2); gstore4_sc1(ov[3], vO, rsO, 24 * D_DIM * 2);
  STAMP(6, 3);
  STAMP(6, 4);
}

DI void g3_phase(char* smem, unsigned smem_base, int tid, int wid, const bf16* __restrict__ Wg, const bf16* __restrict__ Wu,
                 const bf16* __restrict__ Wd, bf16* out, char* ws, int own, unsigned tag, const u32x4 (&x2p)[2]) {
  const int lane = tid & 63;
  const int wm = wid & 3, wn = wid >> 2;
  const int xcd = blockIdx.x & 7, idx = blockIdx.x >> 3;
  const int bm = idx & 7, bn = xcd * 4 + (idx >> 3);
  const int n0 = bn * 112, n0a = n0 & ~31;
  unsigned* yflags = (unsigned*)(ws + WS_FY);
  const rsrc_t rsW = mk_rsrc((const char*)(wm & 1 ? Wu : Wg) + blockIdx.x * 4096, D_DIM * F_DIM * 2 - blockIdx.x * 4096);
  Stg st;
  st.rA = mk_rsrc(ws + WS_Y + (size_t)bm * 32 * 16384, 32 * 16384);
  st.rB = mk_rsrc(wn ? Wu : Wg, D_DIM * F_DIM * 2);
  st.vA = wid * 2048 + lane * 16;
  {
    const int r = wm * 8 + (lane >> 4);
    const int sw = (r & 3) | (((r >> 3) & 1) << 2);
    const int c16 = lane & 15;
    st.vB = (unsigned)(r * (F_DIM * 2) + (n0a + (((c16 >> 1) ^ sw) * 16) + (c16 & 1) * 8) * 2);
    if (GMB && ((c16 >> 1) ^ sw) == ((bn & 1) ? 0 : 7)) st.vB = 1u << 30;
  }
  st.sA = 0; st.sB = 0; st.dA = 16384; st.dB = 229376; st.e1 = 0; st.e3 = 4 * F_DIM * 2;
  const unsigned m0w = smem_base + wid * 2048;
  asm volatile("" : "+s"(out), "+s"(Wd));
  G3_STAGE(0, 2); G3_STAGE(0, 3); G3_STAGE(1, 2); G3_STAGE(1, 3);
  wait_vm<4>();
  __builtin_amdgcn_s_barrier();
  STAMP(3, 4);
  if (wid == 0) asm volatile("buffer_store_dword %0, %1, %2, 0 offen sc1" ::"v"(tag), "v"(lane * (1024 * YFS)), "s"(mk_rsrc(yflags + own * YFS, 7 * 1024 * YFS + 4)) : "memory");
  G3_STAGE(2, 2); G3_STAGE(2, 3); G3_STAGE(3, 2); G3_STAGE(3, 3);
  if (wn == 0) {
    if (wm == 0) {
      const rsrc_t rsF = mk_rsrc(yflags + (xcd * 256 + bm * 32) * YFS, 128 * YFS);
      if (YSL) __builtin_amdgcn_s_sleep(YSL);
      unsigned spins = 0, f;
      do {
        asm volatile("buffer_load_dword %0, %1, %2, 0 offen sc1\n\ts_waitcnt vmcnt(0)" : "=v"(f) : "v"((lane & 31) * (4 * YFS)), "s"(rsF) : "memory");
      } while (!__all(f == tag) && ++spins < (1u << 18));
    }
    wait_vm<4>();
    __builtin_amdgcn_s_barrier();
    STAMP(5, 0);
    G3_STAGE(0, 0); G3_STAGE(0, 1); G3_STAGE(1, 0); G3_STAGE(1, 1);
    G3_STAGE(2, 0); G3_STAGE(2, 1); G3_STAGE(3, 0); G3_STAGE(3, 1);
    g3_body<4>(smem, smem_base, wm, wn, wid, lane, bm, bn, st, rsW, Wd, ws);
    d4_mfma(smem, smem_base, lane_id(), wm, out, ws, tag);
  } else {
    wait_vm<4>();
    __builtin_amdgcn_s_barrier();
    G3_STAGE(0, 0); G3_STAGE(0, 1); G3_STAGE(1, 0); G3_STAGE(1, 1);
    G3_STAGE(2, 0); G3_STAGE(2, 1); G3_STAGE(3, 0); G3_STAGE(3, 1);
    g3_body<3>(smem, smem_base, wm, wn, wid, lane, bm, bn, st, rsW, Wd, ws);
    d4_loader(smem, smem_base, lane_id(), wm, Wd, ws, tag);
  }
}

__global__ __launch_bounds__(512, 2) void block_kernel(const bf16* x_, const bf16* c_, const bf16* wmod_, char* ws_, const bf16* wq_,
                                                       const bf16* wo_, const bf16* wg_, const bf16* wu_, const bf16* __restrict__ wd, bf16* out) {
  __shared__ __attribute__((aligned(16))) char smem[Q_LDS];
  u64x8 ka;
  asm volatile("s_load_dwordx16 %0, %1, 0x0" : "=s"(ka) : "s"(__builtin_amdgcn_kernarg_segment_ptr()));
  STAMP(0, 0);
  const int tid = threadIdx.x;
  const int wid = __builtin_amdgcn_readfirstlane(tid >> 6);
  const unsigned smem_base = (unsigned)(uintptr_t)smem;
  const int xcd = blockIdx.x & 7, idx = blockIdx.x >> 3;
  {
    unsigned long long pc;
    asm volatile("s_getpc_b64 %0" : "=s"(pc));
    constexpr int CH = (CODESZ + 4095) / 4096 * 128;
    touch8k(smem_base + QH_OFF, tid - 448u < CH / 128 ? (tid - 448) * 128u : 1u << 30, mk_rsrc((const char*)(uintptr_t)(pc & ~255ull), CODESZ), idx * CH);
  }
  const int bm = (xcd >> 2) * 8 + (idx & 7), bn = (xcd & 3) * 4 + (idx >> 3);
  const int cm = (xcd >> 1) * 4 + (idx & 3), cn = __builtin_amdgcn_readfirstlane((xcd & 1) * 8 + (idx >> 2));
  const int lane = tid & 63;
  Pre p;
  {
    const bool gv = ((bm + bn) & 1) == 0;
    unsigned oa = ((bm >> 2) * 4 + (wid & 3)) * 786432 + (bm & 2) * 1024 + (bn ^ 1) * 128, ob = (bm * 128 + (bn >> 1) * 16 + (wid & 3) * 4) * 2048;
    unsigned va = (lane >> 3) * (8 * 12288) + (lane & 7) * 16, vb = lane * 16;
    asm volatile("" : "+s"(oa), "+s"(ob));
    asm volatile("" : "+v"(va), "+v"(vb));
    p.o1 = gv ? oa : ob; p.n1 = gv ? 63 * 12288 + 128 : 8192; p.vo = gv ? va : vb; p.st = gv ? 12288 : 1024;
    dsw32<0>(smem_base + QT_OFF, 0u);
    dsw32<4>(smem_base + QT_OFF, 0u);
    dsw32<768>(smem_base + QT_OFF, 0u);
    asm volatile("" : "+v"(p.vo));
    asm volatile("s_waitcnt lgkmcnt(0)" : "+s"(ka), "+s"(p.o1), "+s"(p.n1), "+s"(p.st));
  }
  asm volatile("s_cmp_lt_u32 %0, 4\n\ts_cbranch_scc1 1\n\ts_barrier" ::"s"(wid) : "scc", "memory");
  const Args g = {(const bf16*)ka[0], (const bf16*)ka[1], (const bf16*)ka[2], (const bf16*)ka[4], (const bf16*)ka[5], (const bf16*)ka[6], (const bf16*)ka[7], (char*)ka[3]};
  char* ws = g.ws;
  const bf16 *x = g.x, *wg = g.wg, *wu = g.wu;
  bf16* O = (bf16*)(ws + WS_O);
  bf16* Y = (bf16*)(ws + WS_Y);
  unsigned* oflags = (unsigned*)(ws + WS_FO);
  float* stat = (float*)(ws + WS_STAT);
  unsigned tag = 0;
  if (__builtin_expect(wid < 4, 1)) {
    __builtin_amdgcn_s_setprio(2);
    tag = front1(smem, smem_base, lane, wid, bm, bn, g, p);
    front_h(smem, smem_base, lane, wid, bm, bn, g, tag);
    qkv_mfma(smem, smem_base, tid, wid, bm * 128, bn * 64, O, oflags, tag);
  } else if (wid < 7) {
    tag = qkv_loader(smem, smem_base, lane, wid - 4, bm, bn, cn, g);
  } else {
    tag = qkv_spare(smem, smem_base, lane, bm, bn, cn, g, wd, out);
  }
  STAMPT(2, 6, 320);
  STAMPT(3, 0, 448);
  u32x4 x2p[2];
  o_phase(smem, smem_base, tid & 63, wid, cm, cn, O, oflags, x, Y, stat, ws, x2p, tag);
  g3_phase(smem, smem_base, tid, wid, wg, wu, wd, out, ws, cm * 16 + cn, tag, x2p);
}

extern "C" void kernel_launch(void* const* d_in, const int* in_sizes, int n_in, void* d_out, int out_size,
                              void* d_ws, size_t ws_size, hipStream_t stream) {
  const bf16* x = (const bf16*)d_in[0];
  const bf16* wqkv = (const bf16*)d_in[1];
  const bf16* wo = (const bf16*)d_in[2];
  const bf16* wg = (const bf16*)d_in[4];
  const bf16* wu = (const bf16*)d_in[5];
  const bf16* wd = (const bf16*)d_in[6];
  const bf16* c = (const bf16*)d_in[7];
  const bf16* wmod = (const bf16*)d_in[8];
  bf16* out = (bf16*)d_out;

  char* ws = (char*)d_ws;
  block_kernel<<<256, 512, 0, stream>>>(x, c, wmod, ws, wqkv, wo, wg, wu, wd, out);
}
```

```cpp
#include <hip/hip_runtime.h>
#include <hip/hip_bf16.h>
#include <stdint.h>

typedef __hip_bfloat16 bf16;
typedef __attribute__((ext_vector_type(8))) short bf16x8;
typedef __attribute__((ext_vector_type(4))) float f32x4;
typedef __attribute__((ext_vector_type(2))) float f32x2;
typedef __attribute__((ext_vector_type(4))) unsigned u32x4;
typedef __attribute__((ext_vector_type(2))) unsigned u32x2;
typedef __attribute__((ext_vector_type(2))) __bf16 bf16x2_t;
typedef __attribute__((ext_vector_type(8))) unsigned long long u64x8;
typedef __amdgpu_buffer_rsrc_t rsrc_t;

#define DI __device__ __forceinline__
#define TQ 8
#define QSD 4
#define DWO 6
#define DW0 14
#define DWS 16
#define OFS 4
#define ORP 1
#define YFS 32
#define YSL 6
#define CODESZ 78400

#define PW 0
#define STAMP(k, s) do { } while (0)
#define STAMPT(k, s, T) do { } while (0)
#define STAMPNW(k, s)
#define STAMPFLUSH(k, s)
#define STAMPDECL

constexpr int S_LEN = 2048;
constexpr int D_DIM = 1024;
constexpr int F_DIM = 3584;

template <int V> struct ic { static constexpr int value = V; };
template <int N, int I = 0, class F>
DI void static_for(F&& f) {
  if constexpr (I < N) { f(ic<I>{}); static_for<N, I + 1>(f); }
}

DI float bf_lo(unsigned u) { return __uint_as_float(u << 16); }
DI float bf_hi(unsigned u) { return __uint_as_float(u & 0xffff0000u); }
DI unsigned pk_bf16(float lo, float hi) {
  const f32x2 v = {lo, hi};
  return __builtin_bit_cast(unsigned, __builtin_convertvector(v, bf16x2_t));
}
DI rsrc_t mk_rsrc(const void* p, unsigned bytes) { return __builtin_amdgcn_make_buffer_rsrc((void*)p, (short)0, (int)bytes, 0x00020000); }

template <int N>
DI void wait_vm() { asm volatile("s_waitcnt vmcnt(%0)" ::"n"(N > 63 ? 63 : N) : "memory"); }
#define WAIT_LGKM0() do { asm volatile("s_waitcnt lgkmcnt(0)" ::: "memory"); __builtin_amdgcn_sched_barrier(0); } while (0)
#define SCHED0() __builtin_amdgcn_sched_barrier(0)

#define MFMA_A(C, B, A) asm volatile("v_mfma_f32_16x16x32_bf16 %0, %1, %2, %0" : "+a"(C) : "v"(B), "a"(A))
#define MFMA_AV(C, B, A) asm volatile("v_mfma_f32_16x16x32_bf16 %0, %1, %2, %0" : "+v"(C) : "v"(B), "v"(A))
#define PERM_LO(D, X, Y) asm volatile("v_perm_b32 %0, %1, %2, %3" : "=v"(D) : "v"(Y), "v"(X), "s"(0x05040100u))
#define PERM_HI(D, X, Y) asm volatile("v_perm_b32 %0, %1, %2, %3" : "=v"(D) : "v"(Y), "v"(X), "s"(0x07060302u))
template <int OFF>
DI void bload_v(u32x4& d, unsigned voff, rsrc_t rs, unsigned soff) {
  asm volatile("buffer_load_dwordx4 %0, %1, %2, %3 offen offset:%4" : "+v"(d) : "v"(voff), "s"(rs), "s"(soff), "i"(OFF) : "memory");
}
template <int OFF>
DI void bload2_v(u32x2& d, unsigned voff, rsrc_t rs, unsigned soff) {
  asm volatile("buffer_load_dwordx2 %0, %1, %2, %3 offen offset:%4" : "+v"(d) : "v"(voff), "s"(rs), "s"(soff), "i"(OFF) : "memory");
}
template <int OFF>
DI void bload1_v(unsigned& d, unsigned voff, rsrc_t rs, unsigned soff) {
  asm volatile("buffer_load_dword %0, %1, %2, %3 offen offset:%4" : "+v"(d) : "v"(voff), "s"(rs), "s"(soff), "i"(OFF) : "memory");
}
template <int OFF>
DI void bload_lds(unsigned m0v, unsigned voff, rsrc_t rs, unsigned soff) {
  asm volatile("s_mov_b32 m0, %0\n\ts_nop 0\n\tbuffer_load_dwordx4 %1, %2, %3 offen offset:%4 lds" ::"s"(m0v), "v"(voff), "s"(rs), "s"(soff), "i"(OFF) : "memory");
}
DI void touch8k(unsigned m0v, unsigned voff, rsrc_t rs, unsigned soff) {
  asm volatile("s_mov_b32 m0, %0\n\ts_nop 0\n\tbuffer_load_dword %1, %2, %3 offen lds" ::"s"(m0v), "v"(voff), "s"(rs), "s"(soff) : "memory");
}
template <int OFF>
DI void bload_lds_sc1(unsigned m0v, unsigned voff, rsrc_t rs, unsigned soff) {
  asm volatile("s_mov_b32 m0, %0\n\ts_nop 0\n\tbuffer_load_dwordx4 %1, %2, %3 offen offset:%4 sc1 lds" ::"s"(m0v), "v"(voff), "s"(rs), "s"(soff), "i"(OFF) : "memory");
}
template <int OFF>
DI void bload_lds_b(unsigned m0v, unsigned voff, rsrc_t rs, unsigned soff) {
  asm volatile("s_mov_b32 m0, %0\n\ts_nop 0\n\tbuffer_load_dwordx4 %1, %2, %3 offen offset:%4 lds" ::"s"(m0v), "v"(voff), "s"(rs), "s"(soff), "i"(OFF) : "memory");
}
template <int OFF>
DI void bload_lds_a(unsigned m0v, unsigned voff, rsrc_t rs, unsigned soff) {
  asm volatile("s_mov_b32 m0, %0\n\ts_nop 0\n\tbuffer_load_dwordx4 %1, %2, %3 offen offset:%4 sc1 lds" ::"s"(m0v), "v"(voff), "s"(rs), "s"(soff), "i"(OFF) : "memory");
}
template <int OFF>
DI void dsr128(u32x4& d, unsigned addr) { asm volatile("ds_read_b128 %0, %1 offset:%2" : "=v"(d) : "v"(addr), "i"(OFF) : "memory"); }
template <int OFF>
DI void dsr_tr(u32x2& d, unsigned addr) { asm volatile("ds_read_b64_tr_b16 %0, %1 offset:%2" : "=v"(d) : "v"(addr), "i"(OFF) : "memory"); }
template <int OFF>
DI void dsw128(unsigned addr, u32x4 d) { asm volatile("ds_write_b128 %0, %1 offset:%2" ::"v"(addr), "v"(d), "i"(OFF) : "memory"); }
template <int OFF>
DI void dsw32(unsigned addr, unsigned d) { asm volatile("ds_write_b32 %0, %1 offset:%2" ::"v"(addr), "v"(d), "i"(OFF) : "memory"); }

DI void gst128(void* p, u32x4 v) {
  asm volatile("global_store_dwordx4 %0, %1, off sc1\n\ts_nop 1" ::"v"(p), "v"(v) : "memory");
}
DI f32x2 lo2(f32x4 v) { return __builtin_shufflevector(v, v, 0, 1); }
DI f32x2 hi2(f32x4 v) { return __builtin_shufflevector(v, v, 2, 3); }
DI float rowsum4(float v) {
  const unsigned u = __float_as_uint(v);
  const auto a = __builtin_amdgcn_permlane16_swap(u, u, false, false);
  const unsigned t = __float_as_uint(__uint_as_float(a[0]) + __uint_as_float(a[1]));
  const auto b = __builtin_amdgcn_permlane32_swap(t, t, false, false);
  return __uint_as_float(b[0]) + __uint_as_float(b[1]);
}
DI int slow_col(const void* p) { return (int)((3u - ((unsigned)(uintptr_t)p >> 7)) & 7u); }
DI float silu_f(float v) { return v / (1.0f + __expf(-v)); }

constexpr unsigned WS_G1M = 0x20000;
constexpr unsigned WS_G1S = 0x40000;
constexpr unsigned WS_G2 = 0x60000;
constexpr unsigned WS_STAT = 0x80000;
constexpr unsigned WS_FO = 0x141000;
constexpr unsigned WS_FC = 0x146000;
constexpr unsigned WS_FY = 0x148000;
constexpr unsigned WS_FH = 0x188000;
constexpr unsigned WS_FA = 0x1C8000;
constexpr unsigned WS_WC = 2u << 20;
constexpr unsigned WS_H = 4u << 20, WS_O = 8u << 20, WS_Y = 12u << 20, WS_ACT = 16u << 20, WS_X = 32u << 20, WS_X2 = 36u << 20;

template <int CTRL>
DI float dpp_get(float b) { return __uint_as_float(__builtin_amdgcn_update_dpp(0u, __float_as_uint(b), CTRL, 0xf, 0xf, true)); }
DI float swap32_add(float a, float b) {
  const auto sw = __builtin_amdgcn_permlane32_swap(__float_as_uint(a), __float_as_uint(b), false, false);
  return __uint_as_float(sw[0]) + __uint_as_float(sw[1]);
}
DI float swap16_add(float a, float b) {
  const auto sw = __builtin_amdgcn_permlane16_swap(__float_as_uint(a), __float_as_uint(b), false, false);
  return __uint_as_float(sw[0]) + __uint_as_float(sw[1]);
}
DI float scatter8(const f32x2 (&a)[4], int lane) {
  const float t0 = swap32_add(a[0][0], a[2][0]), t1 = swap32_add(a[0][1], a[2][1]), t2 = swap32_add(a[1][0], a[3][0]), t3 = swap32_add(a[1][1], a[3][1]);
  const float u0 = swap16_add(t0, t2), u1 = swap16_add(t1, t3);
  const bool b3 = lane & 8;
  return (b3 ? u1 : u0) + dpp_get<0x128>(b3 ? u0 : u1);
}
DI float sum4(const f32x2 (&a)[4]) {
  float t = swap16_add(swap32_add(a[0][0] + a[0][1], a[2][0] + a[2][1]), swap32_add(a[1][0] + a[1][1], a[3][0] + a[3][1]));
  t += dpp_get<0x128>(t); t += dpp_get<0x124>(t); t += dpp_get<0x122>(t); t += dpp_get<0x121>(t);
  return t;
}
DI int lane_id() {
  int l;
  asm volatile("v_mbcnt_lo_u32_b32 %0, -1, 0\n\tv_mbcnt_hi_u32_b32 %0, -1, %0" : "=v"(l));
  return l;
}
DI f32x2 unpk_v(unsigned u) {
  f32x2 r;
  asm volatile("v_lshlrev_b32 %0, 16, %1" : "=v"(r[0]) : "v"(u));
  asm volatile("v_and_b32 %0, 0xffff0000, %1" : "=v"(r[1]) : "v"(u));
  return r;
}

constexpr int Q_SLOT = 21504, Q_NSL = 7;
constexpr int WO_H0 = 4 * Q_SLOT;
constexpr int QX_OFF = Q_SLOT * Q_NSL + 1024;
constexpr int QH_OFF = QX_OFF + 8192;
constexpr int QT_OFF = QH_OFF + 1024;
constexpr int Q_LDS = 163840;
#define POLLW 0
#define OD 3
#define SSL 14
#define SSI 4
#define OMB 6
template <int OFF>
DI void bload_sc1(u32x4& d, unsigned voff, rsrc_t rs, unsigned soff) {
  asm volatile("buffer_load_dwordx4 %0, %1, %2, %3 offen offset:%4 sc1" : "+v"(d) : "v"(voff), "s"(rs), "s"(soff), "i"(OFF) : "memory");
}
DI void bload1_sc1(unsigned& d, unsigned voff, rsrc_t rs) {
  asm volatile("buffer_load_dword %0, %1, %2, 0 offen sc1" : "+v"(d) : "v"(voff), "s"(rs) : "memory");
}
DI unsigned wo_voff(int lane, int odd, int sh) {
  const int q = (lane >> 3) & 3, h = lane >> 5, ch = lane & 7, g = (q >> 1) | (odd << 1);
  return (unsigned)(((h * 16 + q) << sh) + ((((ch >> 1) ^ g) * 16) + (ch & 1) * 8) * 2);
}
DI rsrc_t wo_rsrc(const bf16* wo, const char* wc, int cn) {
  const bool c3 = (cn & 7) == slow_col(wo);
  return mk_rsrc(c3 ? wc + 786432 + (cn >> 3) * 131072 : (const char*)wo + cn * 128, c3 ? 131072 : D_DIM * D_DIM * 2 - cn * 128);
}

#define QY 26
DI unsigned xoff(unsigned b) { return WS_X + b * 16384; }
struct Args { const bf16 *x, *c, *wmod, *wq, *wo, *wg, *wu; char* ws; };
#define CK 3
#define TW 8
#define TO 4
#define TT 2
#define PSL 8
#define PSI 4
DI void gstore2_sc1(u32x2 d, unsigned voff, rsrc_t rs) {
  asm volatile("buffer_store_dwordx2 %0, %1, %2, 0 offen sc1" ::"v"(d), "v"(voff), "s"(rs) : "memory");
}
DI void gstore4_sc1(u32x4 d, unsigned voff, rsrc_t rs, unsigned soff) {
  asm volatile("buffer_store_dwordx4 %0, %1, %2, %3 offen sc1\n\ts_nop 1" ::"v"(d), "v"(voff), "s"(rs), "s"(soff) : "memory");
}
DI void bload_us(unsigned& d, unsigned voff, rsrc_t rs, unsigned soff) {
  asm volatile("buffer_load_ushort %0, %1, %2, %3 offen" : "+v"(d) : "v"(voff), "s"(rs), "s"(soff) : "memory");
}
template <int W0, int W1>
DI void gemv8(float* red, int lane, const u32x4 (&d)[8], unsigned cv) {
  wait_vm<W0>();
  const unsigned sv = __float_as_uint(silu_f(unpk_v(cv)[0]));
  f32x2 a[4] = {{0, 0}, {0, 0}, {0, 0}, {0, 0}};
  static_for<8>([&](auto J) {
    constexpr int j = J.value;
    if constexpr (j == 4 && W1 != W0) wait_vm<W1>();
    const float s = __uint_as_float(__builtin_amdgcn_update_dpp(__builtin_amdgcn_update_dpp(0u, sv, 0x150 + j, 0xf, 0x3, false), sv, 0x158 + j, 0xf, 0xc, false));
#pragma unroll
    for (int e = 0; e < 4; ++e) a[e] += s * unpk_v(d[j][e]);
  });
  red[(lane & 7) * 8 + (lane >> 3)] = scatter8(a, lane);
}
struct Pre { unsigned o1, n1, vo, st; };
DI unsigned front1(char* smem, unsigned smem_base, int lane, int w4, int bm, int bn, const Args& g, const Pre& p) {
  const bool gv = ((bm + bn) & 1) == 0;
  u32x4 d[8];
  unsigned tag, cv;
  asm volatile("" : "=v"(d[0]), "=v"(d[1]), "=v"(d[2]), "=v"(d[3]), "=v"(d[4]), "=v"(d[5]), "=v"(d[6]), "=v"(d[7]), "=v"(tag), "=v"(cv));
  {
    const rsrc_t rs = mk_rsrc((const char*)(gv ? g.wmod : g.x) + p.o1, p.n1), rsC = mk_rsrc(g.c, 2048);
#pragma unroll
    for (int i = 0; i < 4; ++i) bload_v<0>(d[i], p.vo, rs, i * p.st);
    bload_us(cv, lane * 2, rsC, (bm >> 2) * 512 + w4 * 128);
    bload1_v<0>(tag, 0u, mk_rsrc(g.ws, 4), 0);
#pragma unroll
    for (int i = 4; i < 8; ++i) bload_v<0>(d[i], p.vo, rs, i * p.st);
  }
  STAMPT(0, 6, 192);
  __builtin_amdgcn_s_barrier();
  float* red = reinterpret_cast<float*>(smem + QX_OFF);
  if (gv) {
    gemv8<4, 0>(red + w4 * 64, lane, d, cv);
  } else {
    f32x2 v[4][8], t[4];
    asm volatile("s_waitcnt vmcnt(4)" ::"v"(cv) : "memory");
#pragma unroll
    for (int r = 0; r < 4; ++r) {
      if (r == 2) wait_vm<0>();
#pragma unroll
      for (int e = 0; e < 8; ++e) v[r][e] = unpk_v(d[r * 2 + (e >> 2)][e & 3]);
      t[r] = ((v[r][0] + v[r][1]) + (v[r][2] + v[r][3])) + ((v[r][4] + v[r][5]) + (v[r][6] + v[r][7]));
    }
    const float mean = sum4(t) * (1.0f / D_DIM);
#pragma unroll
    for (int r = 0; r < 4; ++r) {
      const float m = __uint_as_float(__builtin_amdgcn_readlane(__float_as_uint(mean), r * 16));
      f32x2 q0 = {0, 0}, q1 = {0, 0};
#pragma unroll
      for (int e = 0; e < 8; e += 2) { const f32x2 e0 = v[r][e] - m, e1 = v[r][e + 1] - m; q0 += e0 * e0; q1 += e1 * e1; }
      t[r] = q0 + q1;
    }
    const float rstd = rsqrtf(sum4(t) * (1.0f / D_DIM) + 1e-5f);
    if ((lane & 15) == 0) *reinterpret_cast<f32x2*>(red + w4 * 8 + (lane >> 3)) = f32x2{rstd, -mean * rstd};
  }
  asm volatile("ds_add_u32 %1, %2" : "+v"(tag) : "v"(smem_base + QT_OFF), "v"(1u) : "memory");
  STAMPT(0, 1, 192);
  tag = __builtin_amdgcn_readfirstlane(tag) + 0x9E3779B9u;
  const unsigned o = gv ? WS_G1M + (w4 >> 1) * 65536 + ((bm & 2) * 2 + (bm >> 2)) * 8192 + (bn ^ 1) * 512 + (w4 & 1) * 256 : WS_G1S + w4 * 32768 + (bm * 128 + (bn >> 1) * 16) * 16;
  const rsrc_t rsG = mk_rsrc(g.ws + o, 256);
  const f32x2* rp = reinterpret_cast<const f32x2*>(red) + (lane & 15) + (gv ? (w4 & 1) * 16 : 0);
  unsigned n;
  do {
    asm volatile("ds_read_b32 %0, %1\n\ts_waitcnt lgkmcnt(0)" : "=v"(n) : "v"(smem_base + QT_OFF) : "memory");
  } while (__builtin_amdgcn_readfirstlane(n) != 256u);
  f32x2 m = rp[0];
  const f32x2 m2 = (m + rp[32]) + (rp[64] + rp[96]);
  m = gv ? m2 : m;
  gstore4_sc1(u32x4{__float_as_uint(m[0]), tag, __float_as_uint(m[1]), tag}, lane * 16, rsG, 0);
  STAMPT(0, 2, 192);
  return tag;
}
DI void front_ocld(int lane, int w4, int bm, int bn, const Args& g, u32x4 (&oc)[8], unsigned& c2) {
  const rsrc_t rs = mk_rsrc((const char*)g.wmod + ((bm & 3) * 4 + w4) * 786432 + 4096 + (bm >> 2) * 2048 + (bn ^ 2) * 128, 63 * 12288 + 128);
  asm volatile("" : "=v"(c2));
  bload_us(c2, lane * 2, mk_rsrc(g.c, 2048), (bm & 3) * 512 + w4 * 128);
#pragma unroll
  for (int i = 0; i < 8; ++i) {
    wait_vm<TO - 1>();
    asm volatile("" : "=v"(oc[i]));
    bload_v<0>(oc[i], (lane >> 3) * (8 * 12288) + (lane & 7) * 16, rs, i * 12288);
  }
}
template <int NW>
DI void front_oc(char* smem, int lane, int w4, const u32x4 (&oc)[8], unsigned c2) {
  gemv8<NW, NW>(reinterpret_cast<float*>(smem + QX_OFF + 1024) + w4 * 64, lane, oc, c2);
  asm volatile("s_waitcnt lgkmcnt(0)" ::: "memory");
}

DI void front_h(char* smem, unsigned smem_base, int lane, int wid, int bm, int bn, const Args& g, unsigned tag) {
  const int fr = lane & 15, fq = lane >> 4;
  const int kA = ((bn ^ 3) * 2) ^ (wid & 1), kB = ((bn ^ 3) * 2) ^ (2 + wid), f0 = (wid >> 1) * 4;
  u32x4 xa[5], xb[9];
  const unsigned hc = bm == 0 ? 1u : 0u;
  const unsigned la = smem_base + (wid & 1) * Q_SLOT + f0 * 1024 + lane * 16, lb = smem_base + (2 + wid) * Q_SLOT + lane * 16;
  {
    unsigned n;
    do {
      asm volatile("ds_read_b32 %0, %1 offset:4\n\ts_waitcnt lgkmcnt(0)" : "=v"(n) : "v"(smem_base + QT_OFF) : "memory");
    } while (__builtin_amdgcn_readfirstlane(n) != 192u);
  }
  const unsigned lx = fr * 64 + (fq ^ ((fr >> 1) & 2)) * 16 - lane * 16;
  static_for<5>([&](auto I) { dsr128<I.value * 1024>(xa[I.value], la + lx); });
  static_for<9>([&](auto I) { dsr128<I.value * 1024>(xb[I.value], lb + lx); });
  f32x2 fa[5][4], fb[9][4];
  asm volatile("s_waitcnt lgkmcnt(9)" ::: "memory");
#pragma unroll
  for (int i = 0; i < 5; ++i)
#pragma unroll
    for (int e = 0; e < 4; ++e) fa[i][e] = unpk_v(xa[i][e]);
  asm volatile("s_waitcnt lgkmcnt(0)" ::: "memory");
#pragma unroll
  for (int i = 0; i < 9; ++i)
#pragma unroll
    for (int e = 0; e < 4; ++e) fb[i][e] = unpk_v(xb[i][e]);
  STAMP(0, 4);
  u32x4 pm[4], ps[3];
  {
    const rsrc_t rsM = mk_rsrc(g.ws + WS_G1M + (bm >> 3) * 65536, 65536), rsS = mk_rsrc(g.ws + WS_G1S + (bn >> 2) * 32768, 32768);
    const unsigned vm = ((fr >> 2) & 1) * 32768 + (fr >> 3) * 8192 + fq * 64 + (fr & 3) * 16;
    const unsigned vs0 = (bm * 128 - 16 + lane + (lane < 16 ? hc * 16 : 0)) * 16, vs1 = (bm * 128 + 48 + lane) * 16, vs2 = (bm * 128 + 112 + fr) * 16;
    if (PSL) __builtin_amdgcn_s_sleep(PSL);
    unsigned spins = 0;
    for (;;) {
      asm volatile("buffer_load_dwordx4 %4, %8, %12, 0 offen sc1\n\tbuffer_load_dwordx4 %5, %9, %12, 0 offen sc1\n\tbuffer_load_dwordx4 %6, %10, %12, 0 offen sc1\n\t"
                   "buffer_load_dwordx4 %0, %7, %11, %13 offen sc1\n\tbuffer_load_dwordx4 %1, %7, %11, %14 offen sc1\n\t"
                   "buffer_load_dwordx4 %2, %7, %11, %15 offen sc1\n\tbuffer_load_dwordx4 %3, %7, %11, %16 offen sc1\n\t"
                   "s_waitcnt vmcnt(0)"
                   : "=&v"(pm[0]), "=&v"(pm[1]), "=&v"(pm[2]), "=&v"(pm[3]), "=&v"(ps[0]), "=&v"(ps[1]), "=&v"(ps[2])
                   : "v"(vm), "v"(vs0), "v"(vs1), "v"(vs2), "s"(rsM), "s"(rsS), "s"(kA * 256), "s"(kA * 256 + 16384), "s"(kB * 256), "s"(kB * 256 + 16384) : "memory");
      unsigned bad = 0;
#pragma unroll
      for (int i = 0; i < 4; ++i) bad |= (pm[i][1] ^ tag) | (pm[i][3] ^ tag);
#pragma unroll
      for (int i = 0; i < 3; ++i) bad |= (ps[i][1] ^ tag) | (ps[i][3] ^ tag);
      asm volatile("" : "+v"(bad));
      if (__all(bad == 0u) || ++spins > (1u << 18)) break;
      if (PSI) __builtin_amdgcn_s_sleep(PSI);
    }
  }
  STAMP(0, 3);
  f32x2 rs[9], ra[5];
  {
    u32x2* sp = reinterpret_cast<u32x2*>(smem + QX_OFF + 2048 + wid * 1536);
#pragma unroll
    for (int i = 0; i < 3; ++i) sp[i * 64 + lane] = u32x2{ps[i][0], ps[i][2]};
    __builtin_amdgcn_wave_barrier();
#pragma unroll
    for (int i = 0; i < 5; ++i) ra[i] = __builtin_bit_cast(f32x2, sp[(f0 + i) * 16 + fr]);
#pragma unroll
    for (int i = 0; i < 9; ++i) rs[i] = __builtin_bit_cast(f32x2, sp[i * 16 + fr]);
  }
  auto cols = [&](const u32x4& p0, const u32x4& p1, f32x2 (&sa)[4], f32x2 (&ca)[4]) {
    float m0 = __uint_as_float(p0[0]) + __uint_as_float(p1[0]), m1 = __uint_as_float(p0[2]) + __uint_as_float(p1[2]);
    const float one = (fr & 4) ? 1.0f : 0.0f;
    m0 = (m0 + dpp_get<0x128>(m0)) + one; m1 = (m1 + dpp_get<0x128>(m1)) + one;
    static_for<4>([&](auto E) {
      constexpr int e = E.value;
      sa[e] = f32x2{dpp_get<0x150 + e>(m0), dpp_get<0x150 + e>(m1)};
      ca[e] = f32x2{dpp_get<0x154 + e>(m0), dpp_get<0x154 + e>(m1)};
    });
  };
  auto frag = [&](const f32x2 (&xf)[4], const f32x2 r, const f32x2 (&sa)[4], const f32x2 (&ca)[4]) {
    u32x4 ho;
#pragma unroll
    for (int e = 0; e < 4; ++e) { const f32x2 h = (xf[e] * r[0] + r[1]) * ca[e] + sa[e]; ho[e] = pk_bf16(h[0], h[1]); }
    return ho;
  };
  f32x2 sa[4], ca[4];
  cols(pm[0], pm[1], sa, ca);
  const rsrc_t rsH = mk_rsrc(g.ws + WS_H + ((bm * 8 + f0) * 32 + kA) * 1024, 3 * 32768 + 1024);
  static_for<5>([&](auto I) {
    constexpr int i = (I.value + 1) % 5;
    const u32x4 ho = frag(fa[i], ra[i], sa, ca);
    if constexpr (i > 0) gstore4_sc1(ho, lane * 16, rsH, (i - 1) * 32768);
    dsw128<i * 1024>(i == 0 && f0 ? smem_base + QH_OFF + lane * 16 : la, ho);
  });
  cols(pm[2], pm[3], sa, ca);
  static_for<9>([&](auto I) { dsw128<I.value * 1024>(lb, frag(fb[I.value], rs[I.value], sa, ca)); });
  STAMP(0, 5);
  wait_vm<0>();
  asm volatile("buffer_store_dword %0, %1, %2, 0 offen sc1" ::"v"(tag), "v"(lane * 32768), "s"(mk_rsrc(g.ws + WS_FH + (bm * 16 + bn) * 128 + wid * 4, 7 * 32768 + 4)) : "memory");
  STAMP(1, 1);
}

DI unsigned qkv_loader(char* smem, unsigned smem_base, int lane, int sw, int bm, int bn, int cn, const Args& g) {
  const int m0 = bm * 128, n0 = bn * 64;
  const unsigned u0 = ((bn ^ 3) * 2);
  const char* wc = g.ws + WS_WC;
  unsigned m0a = smem_base + sw * 3072, m0w = smem_base + 9216 + sw * 4096;
  auto wrow = [&](int i, unsigned sec) {
    const int r = i * 8 + (lane >> 3), ch = lane & 7;
    const int gg = ((r >> 1) & 1) | (((r >> 3) & 1) << 1);
    return (unsigned)((r * 3 + sw) * sec + ((((ch >> 1) ^ gg) * 16) + (ch & 1) * 8) * 2);
  };
  int fb0 = (m0 >> 4) - 1 + 3 * sw; fb0 = fb0 < 0 ? 0 : fb0;
  {
    const rsrc_t rsX = mk_rsrc(g.x, S_LEN * D_DIM * 2);
    const unsigned vx = (lane >> 2) * 2048 + ((lane & 3) ^ ((lane >> 3) & 2)) * 16;
#pragma unroll
    for (int i = 0; i < 18; ++i) {
      const int u = (i / 6) * 2 + (i & 1), r = (i >> 1) % 3;
      if (i >= TW) wait_vm<TW - 1>();
      bload_lds<0>(m0a + u * Q_SLOT + r * 1024, vx, rsX, (r ? (m0 >> 4) - 1 + 3 * sw + r : fb0) * 32768 + (u ^ u0) * 64);
    }
    const rsrc_t rsW0 = mk_rsrc((const char*)g.wq + n0 * 2, D_DIM * 3 * D_DIM * 2 - n0 * 2);
    const unsigned voffW0[2] = {wrow(0, 2048), wrow(1, 2048)};
#pragma unroll
    for (int u = 0; u < Q_NSL - 1; ++u)
#pragma unroll
      for (int i = 0; i < 4; ++i) {
        wait_vm<TW - 1>();
        if (u * 4 + i == TW - 1) asm volatile("ds_add_u32 %0, %1 offset:4" ::"v"(smem_base + QT_OFF), "v"(1u) : "memory");
        bload_lds<0>(m0w + u * Q_SLOT + i * 1024, voffW0[i & 1], rsW0, ((u ^ u0) * 2u + (i >> 1)) * 98304u);
      }
  }
  u32x4 oc[8];
  unsigned c2;
  front_ocld(lane, sw, bm, bn, g, oc, c2);
  const rsrc_t rsT = mk_rsrc((const char*)g.wq + (blockIdx.x * 3 + sw) * 8192, 8192);
    wait_vm<TT>();
  touch8k(smem_base + QH_OFF, lane * 128u, rsT, 0);
  const rsrc_t rsH = mk_rsrc(g.ws + WS_H, S_LEN * D_DIM * 2);
  const bool c3 = (bn & 7) == slow_col(g.wq);
  const rsrc_t rsW = mk_rsrc(c3 ? wc + (n0 >> 9) * 393216 : (const char*)g.wq + n0 * 2, c3 ? 393216 : D_DIM * 3 * D_DIM * 2 - n0 * 2);
  const unsigned sec = c3 ? 128u : 2048u, p16 = sec * 48u;
  unsigned voffH0 = (unsigned)(fb0 * 32768 + lane * 16);
  unsigned voffH1 = (unsigned)(((m0 >> 4) + 3 * sw) * 32768 + lane * 16);
  unsigned voffW[2] = {wrow(0, sec), wrow(1, sec)};
  asm volatile("" : "+v"(voffH0), "+v"(voffH1), "+v"(voffW[0]), "+v"(voffW[1]));
  asm volatile("" : "+s"(m0a), "+s"(m0w));
#define Q_LOADH(SOFF, U)                                                                                         \
  do {                                                                                                           \
    const unsigned so_ = ((unsigned)(U) ^ u0) * 1024u;                                                           \
    bload_lds_sc1<0>(m0a + (SOFF), voffH0, rsH, so_);                                                            \
    bload_lds_sc1<0>(m0a + (SOFF) + 1024, voffH1, rsH, so_);                                                     \
    bload_lds_sc1<0>(m0a + (SOFF) + 2048, voffH1, rsH, so_ + 32768u);                                            \
  } while (0)
#define Q_LOADW(SOFF, U)                                                                                         \
  do {                                                                                                           \
    _Pragma("unroll") for (int i = 0; i < 4; ++i)                                                                \
      bload_lds_sc1<0>(m0w + (SOFF) + i * 1024, voffW[i & 1], rsW, (((unsigned)(U) ^ u0) * 2u + (i >> 1)) * p16); \
  } while (0)
  front_oc<1>(smem, lane, sw, oc, c2);
  __builtin_amdgcn_s_barrier();
  Q_LOADW(6 * Q_SLOT, 6);
  __builtin_amdgcn_s_barrier();
  Q_LOADW(0, 7); Q_LOADW(Q_SLOT, 8);
  __builtin_amdgcn_s_barrier();
  Q_LOADH(6 * Q_SLOT, 6); Q_LOADH(0, 7); Q_LOADH(Q_SLOT, 8);
  Q_LOADH(2 * Q_SLOT, 9); Q_LOADW(2 * Q_SLOT, 9); Q_LOADH(3 * Q_SLOT, 10); Q_LOADW(3 * Q_SLOT, 10);
  wait_vm<17>();
  __builtin_amdgcn_s_barrier();
  unsigned sb = 4 * Q_SLOT;
  for (int u = 11; u < 31; u += 2) {
#pragma unroll
    for (int t = 0; t < 2; ++t) {
      Q_LOADH(sb, u + t);
      Q_LOADW(sb, u + t);
      sb = (sb == (Q_NSL - 1) * Q_SLOT) ? 0u : sb + Q_SLOT;
    }
    wait_vm<21>();
    __builtin_amdgcn_s_barrier();
  }
  Q_LOADH(3 * Q_SLOT, 31); Q_LOADW(3 * Q_SLOT, 31);
  const rsrc_t rsO = wo_rsrc(g.wo, wc, cn);
  const int sh = ((cn & 7) == slow_col(g.wo)) ? 7 : 11;
  const unsigned voffO[2] = {wo_voff(lane, sw & 1, sh), wo_voff(lane, (sw & 1) ^ 1, sh)};
  static_for<6>([&](auto U) {
    constexpr int u = U.value;
#pragma unroll
    for (int j = 0; j < 7; ++j) {
      const int pc = (u < 3 ? u * 21 : 64 + (u - 3) * 21) + 3 * j;
      const unsigned p = pc + sw;
      bload_lds_sc1<0>(smem_base + (u < 3 ? WO_H0 : -65536) + p * 1024u, voffO[pc & 1], rsO, ((p >> 2) * 32u + (p & 3) * 4u) << sh);
    }
    if constexpr (u == 0 || u == 2) wait_vm<21>();
    if constexpr (u == 0 || u == 2 || u >= 4) __builtin_amdgcn_s_barrier();
  });
  unsigned tag;
#pragma unroll
  for (int i = 0; i < 4; ++i) gstore4_sc1(u32x4{~0u, ~0u, ~0u, ~0u}, lane * 16, mk_rsrc(g.ws + WS_X + blockIdx.x * 16384 + sw * 4096, 4096), i * 1024);
  asm volatile("ds_read_b32 %0, %1 offset:512\n\ts_waitcnt vmcnt(%2) lgkmcnt(0)" : "=v"(tag) : "v"(smem_base + QH_OFF), "n"(4) : "memory");
  return __builtin_amdgcn_readfirstlane(tag);
}
namespace d4 {
struct Tile { int kh, bm, bn; };
DI Tile tile() {
  int b = blockIdx.x;
  asm volatile("" : "+s"(b));
  const int xcd = b & 7, idx = b >> 3;
  return Tile{xcd & 1, ((xcd >> 1) & 1) * 8 + (idx & 7), (xcd >> 2) + (idx >> 3) * 2};
}
}
namespace w7 {
constexpr int UC = 20, UD = 24;
constexpr int ops(int u) {
  int c = (u == 26) + (u == 32) + (u == DWO) + (u == UC);
  for (int j = 0; j < 2; ++j) c += (DW0 + (j * DWS) / 2 == u) + (DW0 + QSD + (j * DWS) / 2 == u);
  return c;
}
constexpr int after(int v, int u) { int c = 0; for (int t = v + 1; t < u; ++t) c += ops(t); return c > 63 ? 63 : c; }
}
DI unsigned qkv_spare(char* smem, unsigned smem_base, int lane, int bm, int bn, int cn, const Args& g) {
  using namespace w7;
  u32x4 cp[4], oc[8];
  unsigned tag, c2;
  const rsrc_t rsP = mk_rsrc((const char*)g.wo + (size_t)blockIdx.x * 8192, 8192);
  {
    const rsrc_t rsQ = mk_rsrc((const char*)g.wq + (size_t)blockIdx.x * 24576, 24576);
    const unsigned vc = ((lane >> 3) & 3) * 2048 + (lane >> 5) * 1024 + (lane & 7) * 16;
    asm volatile("" : "=v"(cp[0]), "=v"(cp[1]), "=v"(cp[2]), "=v"(cp[3]), "=v"(tag));
#pragma unroll
    for (int i = 0; i < 3; ++i) bload_v<0>(cp[i], vc, rsQ, i * 8192 + slow_col(g.wq) * 128);
    bload_v<0>(cp[3], vc, rsP, slow_col(g.wo) * 128);
    bload1_v<0>(tag, 0u, mk_rsrc(g.ws, 4), 0);
    {
      const unsigned o = lane < 16 ? WS_FY + blockIdx.x * 2048 + lane * 128 : (lane < 18 ? WS_FA + blockIdx.x * 256 + (lane - 16) * 128 : WS_FO + blockIdx.x * 64);
      asm volatile("s_mov_b32 m0, %0\n\ts_nop 0\n\tbuffer_load_dword %1, %2, 0 offen sc1 lds" ::"s"(smem_base + QH_OFF), "v"(lane > 18 ? 1u << 30 : o), "s"(mk_rsrc(g.ws, 2u << 20)) : "memory");
    }
    asm volatile("s_waitcnt vmcnt(%5)" : "+v"(cp[0]), "+v"(cp[1]), "+v"(cp[2]), "+v"(cp[3]), "+v"(tag) : "n"(1) : "memory");
    tag = __builtin_amdgcn_readfirstlane(tag) + 0x9E3779B9u;
    const rsrc_t rsC = mk_rsrc(g.ws + WS_WC, 1 << 20);
#pragma unroll
    for (int i = 0; i < 3; ++i) gstore4_sc1(cp[i], (lane >> 5) * 393216 + (lane & 31) * 16, rsC, (blockIdx.x * 3 + i) * 512);
    gstore4_sc1(cp[3], (lane >> 5) * 131072 + (lane & 31) * 16, rsC, 786432 + blockIdx.x * 512);
  }
  wait_vm<0>();
  asm volatile("buffer_store_dword %0, %1, %2, 0 offen sc1" ::"v"(tag), "v"(lane * 4), "s"(mk_rsrc(g.ws + WS_FC + blockIdx.x * 16, 4)) : "memory");
  STAMPT(1, 3, 448);
  front_ocld(lane, 3, bm, bn, g, oc, c2);
  front_oc<0>(smem, lane, 3, oc, c2);
  STAMPT(1, 4, 448);
  if (((bn & 7) == slow_col(g.wq)) || ((cn & 7) == slow_col(g.wo))) {
    const rsrc_t rsF = mk_rsrc(g.ws + WS_FC, 4096);
    unsigned spins = 0, f0, f1, f2, f3;
    do {
      if (spins) __builtin_amdgcn_s_sleep(8);
      asm volatile("buffer_load_dword %0, %4, %5, 0 offen sc1\n\tbuffer_load_dword %1, %4, %5, 0 offen offset:1024 sc1\n\t"
                   "buffer_load_dword %2, %4, %5, 0 offen offset:2048 sc1\n\tbuffer_load_dword %3, %4, %5, 0 offen offset:3072 sc1\n\ts_waitcnt vmcnt(0)"
                   : "=&v"(f0), "=&v"(f1), "=&v"(f2), "=&v"(f3) : "v"(lane * 16), "s"(rsF) : "memory");
    } while (!__all(((f0 ^ tag) | (f1 ^ tag) | (f2 ^ tag) | (f3 ^ tag)) == 0u) && ++spins < (1u << 16));
  }
  STAMPT(1, 0, 448);
  const rsrc_t rsX = mk_rsrc(g.ws + WS_G2 + (bn ^ 2) * 512, 131072);
  const rsrc_t rsO = wo_rsrc(g.wo, g.ws + WS_WC, cn);
  const int sh = ((cn & 7) == slow_col(g.wo)) ? 7 : 11;
  const unsigned voffO = wo_voff(lane, 1, sh);
  __builtin_amdgcn_s_barrier();
  const rsrc_t rsG = mk_rsrc((const char*)g.wg + (size_t)blockIdx.x * (TQ * 1024), TQ * 1024);
  const rsrc_t rsU = mk_rsrc((const char*)g.wu + (size_t)blockIdx.x * (TQ * 1024), TQ * 1024);
  static_for<33>([&](auto U) {
    constexpr int u = U.value;
    (void)&rsG; (void)&rsU; (void)&rsP; (void)&rsX; (void)&oc;
    if constexpr (u == 0) {
      dsw32<512>(smem_base + QH_OFF, tag);
      const float* rp = reinterpret_cast<const float*>(smem + QX_OFF + 1024) + lane;
      gstore2_sc1(u32x2{__float_as_uint((rp[0] + rp[64]) + (rp[128] + rp[192])), tag}, bm * 8192 + lane * 8, rsX);
    }
    if constexpr (u == CK) {
      const rsrc_t rsF = mk_rsrc(g.ws + WS_FH + (blockIdx.x & 7) * 32768 + (bm == 0 ? 0 : (bm - 1) * 2048), 4096);
      unsigned spins = 0, f0, f1;
      do {
        asm volatile("buffer_load_dword %0, %2, %3, 0 offen sc1\n\tbuffer_load_dword %1, %2, %3, %4 offen sc1\n\ts_waitcnt vmcnt(0)"
                     : "=&v"(f0), "=&v"(f1) : "v"((lane >> 2) * 128 + (lane & 3) * 4), "s"(rsF), "s"(bm ? 2048 : 0) : "memory");
      } while (!__all(f0 == tag && f1 == tag) && ++spins < (1u << 18));
      STAMPT(1, 2, 448);
    }
    if constexpr (u == UD) wait_vm<after(UC, u)>();
    if constexpr (u == 26) bload_lds_sc1<0>(smem_base + WO_H0 + 63 * 1024, voffO, rsO, (15 * 32u + 12u) << sh);
    if constexpr (u == 32) bload_lds_sc1<0>(smem_base + 63 * 1024, voffO, rsO, (31 * 32u + 12u) << sh);
    const unsigned par = ((blockIdx.x >> 3) & 1) << 30;
    if constexpr (u == DWO) touch8k(smem_base + QH_OFF, lane * 128u, rsP, 0);
#pragma unroll
    for (int j = 0; j < 2; ++j) {
      if (DW0 + QSD + (j * DWS) / 2 == u) touch8k(smem_base + QH_OFF, lane * 128u | (par ^ (1u << 30)), j ? rsU : rsG, 0);
      if (DW0 + (j * DWS) / 2 == u) touch8k(smem_base + QH_OFF, lane * 128u | par, j ? rsU : rsG, 0);
    }
    if constexpr (u == UC) gstore4_sc1(u32x4{~0u, ~0u, ~0u, ~0u}, lane * 16, mk_rsrc(g.ws + WS_STAT + (bm * 16 + bn) * 1024, 1024), 0);
    if constexpr ((u & 1) || u == 32) __builtin_amdgcn_s_barrier();
  });
  {
    const rsrc_t rsS = mk_rsrc(g.ws + WS_G2, 131072);
    const unsigned vc = cn * 512 + (lane >> 5) * 32768 + (lane & 31) * 16, vd = 98304 + d4::tile().bn * 1024 + lane * 16;
    u32x4 pm[12];
    unsigned spins = 0;
    for (;;) {
      asm volatile("buffer_load_dwordx4 %0, %12, %14, 0 offen sc1\n\tbuffer_load_dwordx4 %1, %12, %14, %15 offen sc1\n\t"
                   "buffer_load_dwordx4 %2, %12, %14, %16 offen sc1\n\tbuffer_load_dwordx4 %3, %12, %14, %17 offen sc1\n\t"
                   "buffer_load_dwordx4 %4, %12, %14, %18 offen sc1\n\tbuffer_load_dwordx4 %5, %12, %14, %19 offen sc1\n\t"
                   "buffer_load_dwordx4 %6, %12, %14, %20 offen sc1\n\tbuffer_load_dwordx4 %7, %12, %14, %21 offen sc1\n\t"
                   "buffer_load_dwordx4 %8, %13, %14, 0 offen sc1\n\tbuffer_load_dwordx4 %9, %13, %14, %15 offen sc1\n\t"
                   "buffer_load_dwordx4 %10, %13, %14, %16 offen sc1\n\tbuffer_load_dwordx4 %11, %13, %14, %17 offen sc1\n\ts_waitcnt vmcnt(0)"
                   : "=&v"(pm[0]), "=&v"(pm[1]), "=&v"(pm[2]), "=&v"(pm[3]), "=&v"(pm[4]), "=&v"(pm[5]), "=&v"(pm[6]), "=&v"(pm[7]),
                     "=&v"(pm[8]), "=&v"(pm[9]), "=&v"(pm[10]), "=&v"(pm[11])
                   : "v"(vc), "v"(vd), "s"(rsS), "s"(8192), "s"(16384), "s"(24576), "s"(65536), "s"(73728), "s"(81920), "s"(90112) : "memory");
      unsigned bad = 0;
#pragma unroll
      for (int i = 0; i < 12; ++i) bad |= (pm[i][1] ^ tag) | (pm[i][3] ^ tag);
      if (__all(bad == 0u) || ++spins > (1u << 16)) break;
    }
#pragma unroll
    for (int i = 0; i < 4; ++i) gstore4_sc1(u32x4{~0u, ~0u, ~0u, ~0u}, lane * 16, mk_rsrc(g.ws + WS_X + blockIdx.x * 16384 + 12288, 4096), i * 1024);
    f32x2 m[3];
#pragma unroll
    for (int v = 0; v < 3; ++v)
#pragma unroll
      for (int e = 0; e < 2; ++e)
        m[v][e] = (__uint_as_float(pm[v * 4][2 * e]) + __uint_as_float(pm[v * 4 + 1][2 * e])) + (__uint_as_float(pm[v * 4 + 2][2 * e]) + __uint_as_float(pm[v * 4 + 3][2 * e]));
    f32x2* tp = reinterpret_cast<f32x2*>(smem + QT_OFF);
    tp[(lane >> 5) * 64 + (lane & 31)] = m[0];
    if (lane < 32) tp[32 + lane] = m[1] + 1.0f;
    tp[128 + lane] = m[2];
  }
  asm volatile("s_waitcnt lgkmcnt(0)" ::: "memory");
  return tag;
}

DI void qkv_mfma(char* smem, unsigned smem_base, int tid, int wid, int m0, int n0, bf16* O, unsigned* oflags, unsigned tag) {
  const int lane = tid & 63;
  const int wm = wid & 1, wn = wid >> 1;
  const int fr = lane & 15, fq = lane >> 4;
  unsigned rad[3][4];
  {
    const int q = (lane >> 2) & 3, p = lane & 3;
    const int r = 8 * fq + q;
    const int g = ((r >> 1) & 1) | (((r >> 3) & 1) << 1);
#pragma unroll
    for (int i = 0; i < 3; ++i) {
      rad[i][3] = smem_base + i * (3 * Q_SLOT) + lane * 16; rad[i][2] = rad[i][3] + 1024 + wm * 4096;
#pragma unroll
      for (int jj = 0; jj < 2; ++jj) rad[i][jj] = smem_base + i * (3 * Q_SLOT) + 9216 + r * 128 + (((wn * 2 + (jj ^ wm)) ^ g) * 32) + 8 * p;
      asm volatile("" : "+v"(rad[i][0]), "+v"(rad[i][1]), "+v"(rad[i][2]), "+v"(rad[i][3]));
    }
  }
  f32x4 acc[4][6], acch[2];
#pragma unroll
  for (int i = 0; i < 4; ++i)
#pragma unroll
    for (int j = 0; j < 6; ++j) { acc[i][j] = f32x4{0, 0, 0, 0}; asm volatile("" : "+v"(acc[i][j])); }
#pragma unroll
  for (int j = 0; j < 2; ++j) { acch[j] = f32x4{0, 0, 0, 0}; asm volatile("" : "+v"(acch[j])); }
  asm volatile("s_waitcnt lgkmcnt(0)" ::: "memory");
  __builtin_amdgcn_s_barrier();
  STAMPDECL;
  STAMPNW(2, 1);
  u32x4 a[2][5];
  u32x2 bl[2][6], bh[2][6];
  auto rdb = [&](auto PP, auto GG, auto RR) {
    constexpr int jj = RR.value >> 2, j = GG.value * 2 + jj, k = (RR.value >> 1) & 1, sl = (PP.value * 2 + k) % Q_NSL;
    constexpr int off = (sl % 3) * Q_SLOT + GG.value * 4096 + (RR.value & 1) * 512;
    if constexpr (RR.value & 1) dsr_tr<off>(bh[k][j], rad[sl / 3][jj]);
    else dsr_tr<off>(bl[k][j], rad[sl / 3][jj]);
  };
  auto gap = [&](auto PP, auto NN) {
    constexpr int n = NN.value, k = ~n & 1, sl = (PP.value * 2 + k) % Q_NSL;
    if constexpr (n < QY + 8) rdb(PP, ic<0>{}, ic<(n - QY) & 7>{});
    else if constexpr (n == 34 || n == 35) dsr128<(sl % 3) * Q_SLOT>(a[k][4], rad[sl / 3][3]);
    else if constexpr (n >= 36 && (n & 2)) dsr128<(sl % 3) * Q_SLOT + ((n - 36) >> 2) * 1024>(a[k][((n - 36) >> 2) & 3], rad[sl / 3][2]);
    else if constexpr (n >= 36) rdb(PP, ic<1>{}, ic<((n - 36) >> 2) * 2 + (n & 1)>{});
  };
  static_for<52 - QY>([&](auto N) { gap(ic<0>{}, ic<N.value + QY>{}); });
  static_for<16>([&](auto PP) {
    static_for<52>([&](auto N) {
      constexpr int n = N.value, m = n < 36 ? n : n - 4, k = (n ^ (n >> 1)) & 1;
      constexpr int i = (m & 16) ? 3 - ((m >> 2) & 3) : (m >> 2) & 3, j = (m >> 4) * 2 + (((m + 2) >> 2) & 1);
      (void)&acc; (void)&acch; (void)&a; (void)&bl; (void)&bh;
      if constexpr (n < 16 && n % 4 == 0) asm volatile("s_waitcnt lgkmcnt(%0)" ::"n"(n < 12 ? 12 : 8) : "memory");
      if constexpr (n == QY) {
        asm volatile("s_waitcnt lgkmcnt(0)" ::: "memory");
        __builtin_amdgcn_s_barrier();
      }
      constexpr int jb = n < 32 || n >= 36 ? j : (n < 34 ? 2 : 4);
      const u32x4 b = {bl[k][jb][0], bl[k][jb][1], bh[k][jb][0], bh[k][jb][1]};
      if constexpr (n < 32) MFMA_AV(acc[i][j], b, a[k][i]);
      else if constexpr (n < 34) MFMA_AV(acch[0], b, a[k][4]);
      else if constexpr (n < 36) MFMA_AV(acch[1], a[k][4], b);
      else MFMA_AV(acc[i][j], a[k][i], b);
      if constexpr (n < 8) rdb(PP, ic<2>{}, N);
      if constexpr (n >= QY && PP.value < 15) gap(ic<PP.value + 1>{}, N);
    });
  });
  asm volatile("s_waitcnt vmcnt(0) lgkmcnt(0)\n\ts_nop 15" ::: "memory");
  SCHED0();
  __builtin_amdgcn_s_setprio(0);
  STAMP(2, 2); STAMPFLUSH(2, 1);
#pragma unroll
  for (int i = 0; i < 4; ++i)
#pragma unroll
    for (int j = 0; j < 6; ++j) asm volatile("" : "+v"(acc[i][j]));
  asm volatile("" : "+v"(acch[0]), "+v"(acch[1]));
  auto pk2 = [](const f32x4 v) { return u32x2{pk_bf16(v[0], v[1]), pk_bf16(v[2], v[3])}; };
  auto cat = [](const u32x2 a, const u32x2 b) { return __builtin_bit_cast(bf16x8, u32x4{a[0], a[1], b[0], b[1]}); };
  u32x2 kp[5][2], vp[5][2];
  {
    char* xb = smem + QX_OFF + wn * 4096 + lane * 8;
#pragma unroll
    for (int f = 0; f < 2; ++f) { kp[4][f] = pk2(acc[3][2 + f]); vp[4][f] = pk2(acc[3][4 + f]); }
    *reinterpret_cast<u32x2*>(xb + wm * 512) = pk2(acch[0]);
    *reinterpret_cast<u32x2*>(xb + 1024 + wm * 512) = pk2(acch[1]);
    if (wm == 0) {
#pragma unroll
      for (int f = 0; f < 2; ++f) {
        *reinterpret_cast<u32x2*>(xb + 2048 + f * 512) = kp[4][f];
        *reinterpret_cast<u32x2*>(xb + 2048 + 1024 + f * 512) = vp[4][f];
      }
    }
    asm volatile("s_waitcnt lgkmcnt(0)" ::: "memory");
    __builtin_amdgcn_s_barrier();
    asm volatile("" ::: "memory");
#pragma unroll
    for (int f = 0; f < 2; ++f) {
      kp[0][f] = *reinterpret_cast<const u32x2*>(xb + wm * 2048 + (f ^ wm) * 512);
      vp[0][f] = *reinterpret_cast<const u32x2*>(xb + wm * 2048 + 1024 + (f ^ wm) * 512);
    }
  }
  STAMP(2, 3);
#pragma unroll
  for (int i = 0; i < 3; ++i)
#pragma unroll
    for (int f = 0; f < 2; ++f) { kp[1 + i][f] = pk2(acc[i][2 + f]); vp[1 + i][f] = pk2(acc[i][4 + f]); }
  const bool par = fr & 1;
  const bool cura = fq * 4 + (fr & 1) <= fr, curb = fq * 4 + 2 + (fr & 1) <= fr;
  const unsigned mh = par ? 0xffff0000u : 0x0000ffffu;
  const unsigned mca = cura ? mh : 0u, mpa = cura ? 0u : mh, mcb = curb ? mh : 0u, mpb = curb ? 0u : mh;
  const float cs = 0.17677669529663687f * 1.4426950408889634f;
  char* op = (char*)O + ((size_t)((m0 >> 6) + wm) * (D_DIM / 32) + (n0 >> 5) + wn) * 4096 + lane * 16;
  const f32x4 z4 = {0, 0, 0, 0};
#pragma unroll
  for (int ii = 0; ii < 4; ++ii) {
    const int i = 3 - ii;
    const bf16x8 qf = cat(pk2(acc[i][0]), pk2(acc[i][1]));
    const f32x4 sc = __builtin_amdgcn_mfma_f32_16x16x32_bf16(cat(kp[i + 1][0], kp[i + 1][1]), qf, z4, 0, 0, 0);
    f32x4 sp = __builtin_amdgcn_mfma_f32_16x16x32_bf16(cat(kp[i][0], kp[i][1]), qf, z4, 0, 0, 0);
    if (i == 0 && m0 == 0 && wm == 0) sp = f32x4{-5.656854249e9f, -5.656854249e9f, -5.656854249e9f, -5.656854249e9f};
    const float la = cura ? (par ? sc[1] : sc[0]) : (par ? sp[1] : sp[0]);
    const float lb = curb ? (par ? sc[3] : sc[2]) : (par ? sp[3] : sp[2]);
    float mx = fmaxf(la, lb);
    {
      const auto a = __builtin_amdgcn_permlane16_swap(__float_as_uint(mx), __float_as_uint(mx), false, false);
      mx = fmaxf(__uint_as_float(a[0]), __uint_as_float(a[1]));
      const auto b = __builtin_amdgcn_permlane32_swap(__float_as_uint(mx), __float_as_uint(mx), false, false);
      mx = fmaxf(__uint_as_float(b[0]), __uint_as_float(b[1]));
    }
    const float ea = __builtin_amdgcn_exp2f((la - mx) * cs), eb = __builtin_amdgcn_exp2f((lb - mx) * cs);
    const float inv = __builtin_amdgcn_rcpf(rowsum4(ea + eb));
    const unsigned wa = pk_bf16(ea, ea), wb = pk_bf16(eb, eb);
    const bf16x8 pf = __builtin_bit_cast(bf16x8, u32x4{wa & mpa, wb & mpb, wa & mca, wb & mcb});
    const f32x4 o0 = __builtin_amdgcn_mfma_f32_16x16x32_bf16(cat(vp[i][0], vp[i + 1][0]), pf, z4, 0, 0, 0) * inv;
    const f32x4 o1 = __builtin_amdgcn_mfma_f32_16x16x32_bf16(cat(vp[i][1], vp[i + 1][1]), pf, z4, 0, 0, 0) * inv;
    const u32x2 u0 = pk2(o0), u1 = pk2(o1);
    gst128(op + i * 1024, wm ? u32x4{u1[0], u1[1], u0[0], u0[1]} : u32x4{u0[0], u0[1], u1[0], u1[1]});
  }
  STAMP(2, 4);
  wait_vm<0>();
  asm volatile("buffer_store_dword %0, %1, %2, 0 offen sc1" ::"v"(tag), "v"(lane * (4096 * OFS)), "s"(mk_rsrc(oflags + ((m0 >> 1) + wm * 32 + (n0 >> 5) + wn) * OFS, (ORP - 1) * 4096 * OFS + 4)) : "memory");
  STAMP(2, 5);
}

DI void x2_store(const u32x4 (&x2p)[2], const char* ws, int lane, int w, int own) {
  const rsrc_t rsR = mk_rsrc(ws + WS_X2 + (((own >> 4) * 8 + (w & 3) * 2 + (w >> 2)) * 16 + (own & 15)) * 2048, 2048);
  gstore4_sc1(x2p[0], lane * 16, rsR, 0); gstore4_sc1(x2p[1], lane * 16, rsR, 1024);
}
DI void o_phase(char* smem, unsigned smem_base, int lane, int w, int cm, int cn, const bf16* O, const unsigned* oflags,
                const bf16* __restrict__ x, bf16* Y, float* stat, const char* ws, unsigned tag) {
  const int wm = w & 3, kh = w >> 2;
  const int fr = lane & 15, fq = lane >> 4;
  const int R = cm * 8 + wm * 2 + kh;
  u32x2 xv[4];
  {
    const rsrc_t rsX = mk_rsrc(x, S_LEN * D_DIM * 2);
    const unsigned vx = (unsigned)(((R * 16 + fr) * D_DIM + cn * 64 + fq * 4) * 2);
#pragma unroll
    for (int f = 0; f < 4; ++f) asm volatile("" : "=v"(xv[f]));
    bload2_v<0>(xv[0], vx, rsX, 0); bload2_v<32>(xv[1], vx, rsX, 0); bload2_v<64>(xv[2], vx, rsX, 0); bload2_v<96>(xv[3], vx, rsX, 0);
  }
  const unsigned hb = smem_base + (kh ? 0 : WO_H0);
  unsigned brd[4];
  {
    const int q = (lane >> 2) & 3, p = lane & 3;
    const int r = 8 * fq + q;
    const int g = ((r >> 1) & 1) | (((r >> 3) & 1) << 1);
#pragma unroll
    for (int f = 0; f < 4; ++f) brd[f] = hb + r * 128 + ((f ^ g) * 32) + 8 * p;
  }
  const rsrc_t rsA = mk_rsrc((const char*)O + (size_t)(cm * 2 + (wm >> 1)) * 131072 + kh * 65536, 65536);
  unsigned va0 = lane * 16 + ((wm & 1) * 2 + kh) * 1024, va1 = va0 ^ 1024;
  unsigned tsrc = smem_base + QT_OFF + fq * 16, xw = hb + lane * 16 + wm * 4096, xr = smem_base + (kh ? WO_H0 : 0) + lane * 16 + wm * 4096;
  char* yb = (char*)Y + ((size_t)(R >> 4) * 32 + cn * 2 + (fq & 1)) * 16384 + (R & 15) * 1024 + ((fq >> 1) * 16 + fr) * 16;
  asm volatile("" : "+v"(tsrc), "+v"(xw), "+v"(xr), "+v"(yb), "+v"(va0), "+v"(va1), "+v"(brd[0]), "+v"(brd[1]), "+v"(brd[2]), "+v"(brd[3]));
  f32x4 acc[2][4];
#pragma unroll
  for (int i = 0; i < 2; ++i)
#pragma unroll
    for (int f = 0; f < 4; ++f) { acc[i][f] = f32x4{0, 0, 0, 0}; asm volatile("" : "+v"(acc[i][f])); }
  if (w == POLLW) {
    const rsrc_t rsF = mk_rsrc(oflags + ((blockIdx.x & (ORP - 1)) * 1024 + cm * 64) * OFS, 256 * OFS);
    unsigned spins = 0, f;
    do {
      asm volatile("buffer_load_dword %0, %1, %2, 0 offen sc1\n\ts_waitcnt vmcnt(0)" : "=v"(f) : "v"(lane * (4 * OFS)), "s"(rsF) : "memory");
    } while (!__all(f == tag) && ++spins < (1u << 18));
  }
  __builtin_amdgcn_s_barrier();
  STAMPDECL;
  STAMPNW(3, 1);
  u32x4 a[16][2], gt[4], ct[4], st[4];
  u32x2 bl[2][4], bh[2][4];
  auto rdb = [&](auto S) {
    constexpr int s = S.value;
#pragma unroll
    for (int f = 0; f < 4; ++f) { dsr_tr<s * 4096>(bl[s & 1][f], brd[f]); dsr_tr<s * 4096 + 512>(bh[s & 1][f], brd[f]); }
  };
  auto lda = [&](auto S) {
    constexpr int s = S.value;
    asm volatile("" : "=v"(a[s][0]), "=v"(a[s][1]));
    bload_sc1<0>(a[s][0], va0, rsA, s * 4096);
    bload_sc1<0>(a[s][1], va1, rsA, s * 4096);
  };
  static_for<OD>([&](auto S) {
    lda(S);
    if constexpr (S.value == 0) rdb(ic<0>{});
    if constexpr (S.value == 1) rdb(ic<1>{});
  });
  static_for<16>([&](auto S) {
    constexpr int s = S.value;
    asm volatile("s_waitcnt vmcnt(%0) lgkmcnt(8)" ::"n"(2 * ((s + OD - 1 < 15 ? s + OD - 1 : 15) - s)) : "memory");
    SCHED0();
    u32x4 bb[4];
#pragma unroll
    for (int f = 0; f < 4; ++f) bb[f] = u32x4{bl[s & 1][f][0], bl[s & 1][f][1], bh[s & 1][f][0], bh[s & 1][f][1]};
    SCHED0();
    if constexpr (s + OD < 16) lda(ic<(s + OD < 16 ? s + OD : 0)>{});
    if constexpr (s == OMB) __builtin_amdgcn_s_barrier();
#pragma unroll
    for (int n = 0; n < 8; ++n) MFMA_AV(acc[1 - (n >> 2)][n & 3], bb[n & 3], a[s][1 - (n >> 2)]);
    if constexpr (s < 14) rdb(ic<(s < 14 ? s + 2 : 0)>{});
    if constexpr (s == 14) {
      static_for<4>([&](auto F) { dsr128<F.value * 64>(gt[F.value], tsrc); dsr128<256 + F.value * 64>(ct[F.value], tsrc); });
    }
    SCHED0();
  });
  STAMP(3, 2); STAMPFLUSH(3, 1);
  asm volatile("s_nop 7" ::: "memory");
  static_for<4>([&](auto F) { dsw128<F.value * 1024>(xw, __builtin_bit_cast(u32x4, acc[1][F.value])); });
  asm volatile("s_waitcnt lgkmcnt(0)" ::: "memory");
  __builtin_amdgcn_s_barrier();
  u32x4 pr[4];
  static_for<4>([&](auto F) { dsr128<F.value * 1024>(pr[F.value], xr); });
  static_for<4>([&](auto F) { dsr128<512 + F.value * 64>(st[F.value], tsrc); });
#pragma unroll
  for (int f = 0; f < 4; ++f) asm volatile("" : "+v"(acc[0][f]), "+v"(xv[f]), "+v"(gt[f]), "+v"(ct[f]));
  asm volatile("s_waitcnt lgkmcnt(4)" : "+v"(pr[0]), "+v"(pr[1]), "+v"(pr[2]), "+v"(pr[3])::"memory");
  f32x4 fin[4];
#pragma unroll
  for (int f = 0; f < 4; ++f) {
    const f32x4 xf = {bf_lo(xv[f][0]), bf_hi(xv[f][0]), bf_lo(xv[f][1]), bf_hi(xv[f][1])};
    fin[f] = xf + __builtin_bit_cast(f32x4, gt[f]) * (acc[0][f] + __builtin_bit_cast(f32x4, pr[f]));
  }
  const rsrc_t rsS = mk_rsrc((char*)stat + R * 2048, 2048);
  {
    const f32x4 s4 = (fin[0] + fin[1]) + (fin[2] + fin[3]);
    const float sm = rowsum4((s4[0] + s4[1]) + (s4[2] + s4[3]));
    const float mb = sm * (1.0f / 64.0f);
    f32x4 q4 = {0, 0, 0, 0};
#pragma unroll
    for (int f = 0; f < 4; ++f) { const f32x4 d = fin[f] - mb; q4 += d * d; }
    const float m2 = rowsum4((q4[0] + q4[1]) + (q4[2] + q4[3]));
    gstore2_sc1(u32x2{min(__float_as_uint(sm), ~1u), min(__float_as_uint(m2), ~1u)}, lane < 16 ? cn * 128 + lane * 8 : 1u << 30, rsS);
  }
  STAMP(3, 3);
  u32x4 x2p[2];
#pragma unroll
  for (int j = 0; j < 2; ++j) {
    x2p[j] = u32x4{pk_bf16(fin[2 * j][0], fin[2 * j][1]), pk_bf16(fin[2 * j][2], fin[2 * j][3]), pk_bf16(fin[2 * j + 1][0], fin[2 * j + 1][1]), pk_bf16(fin[2 * j + 1][2], fin[2 * j + 1][3])};
    asm volatile("" : "+v"(x2p[j]));
  }
  x2_store(x2p, ws, lane, w, cm * 16 + cn);
  u32x2 tv[4];
  {
    if (SSL) __builtin_amdgcn_s_sleep(SSL);
    unsigned spins = 0;
    for (;;) {
      asm volatile("buffer_load_dwordx2 %0, %4, %5, 0 offen sc1\n\tbuffer_load_dwordx2 %1, %4, %5, 0 offen offset:512 sc1\n\t"
                   "buffer_load_dwordx2 %2, %4, %5, 0 offen offset:1024 sc1\n\tbuffer_load_dwordx2 %3, %4, %5, 0 offen offset:1536 sc1\n\ts_waitcnt vmcnt(0) lgkmcnt(0)"
                   : "=&v"(tv[0]), "=&v"(tv[1]), "=&v"(tv[2]), "=&v"(tv[3]) : "v"(lane * 8), "s"(rsS) : "memory");
      const unsigned mx = max(max(max(tv[0][0], tv[0][1]), max(tv[1][0], tv[1][1])), max(max(tv[2][0], tv[2][1]), max(tv[3][0], tv[3][1])));
      if (!__any(mx == ~0u) || ++spins > (1u << 18)) break;
      if (SSI) __builtin_amdgcn_s_sleep(SSI);
    }
  }
  STAMP(3, 6);
  const float mean = rowsum4((__uint_as_float(tv[0][0]) + __uint_as_float(tv[1][0])) + (__uint_as_float(tv[2][0]) + __uint_as_float(tv[3][0]))) * (1.0f / D_DIM);
  float m2 = 0;
#pragma unroll
  for (int i = 0; i < 4; ++i) { const float d = __uint_as_float(tv[i][0]) * (1.0f / 64.0f) - mean; m2 += __uint_as_float(tv[i][1]) + 64.0f * d * d; }
  const float rstd = rsqrtf(rowsum4(m2) * (1.0f / D_DIM) + 1e-5f), rm = -mean * rstd;
  u32x2 pk[4];
#pragma unroll
  for (int f = 0; f < 4; ++f) {
    asm volatile("" : "+v"(st[f]));
    const f32x4 yv = (fin[f] * rstd + rm) * __builtin_bit_cast(f32x4, ct[f]) + __builtin_bit_cast(f32x4, st[f]);
    pk[f] = u32x2{pk_bf16(yv[0], yv[1]), pk_bf16(yv[2], yv[3])};
  }
#pragma unroll
  for (int j = 0; j < 2; ++j) {
    const auto s0 = __builtin_amdgcn_permlane16_swap(pk[j][0], pk[j + 2][0], false, false);
    const auto s1 = __builtin_amdgcn_permlane16_swap(pk[j][1], pk[j + 2][1], false, false);
    gst128(yb + j * 512, u32x4{s0[0], s1[0], s0[1], s1[1]});
  }
}

#define EPR 3
#define TPOS 62
#define GLX 12
#define GLP 0
#define GD4 4
#define GD3 3
struct Stg { rsrc_t rA, rB; unsigned vA, vB, sA, sB, dA, dB, e1, e3; };
#define G3_STAGE(SLOT, P)                                                                                  \
  do {                                                                                                     \
    if ((P) == 0) bload_lds_a<0>(m0w + (SLOT) * 32768, st.vA, st.rA, st.sA);                             \
    else if ((P) == 1) { bload_lds_a<1024>(m0w + (SLOT) * 32768, st.vA, st.rA, st.sA + st.e1); st.sA += st.dA; } \
    else if ((P) == 2) bload_lds_b<0>(m0w + (SLOT) * 32768 + 16384, st.vB, st.rB, st.sB);                    \
    else { bload_lds_b<0>(m0w + (SLOT) * 32768 + 16384 + 1024, st.vB, st.rB, st.sB + st.e3); st.sB += st.dB; } \
  } while (0)

#define DNF 24
namespace d4 {
constexpr int NF = DNF, NH = 56 - 2 * NF, N = NF + NH, NE = 12, FR = 32768;
constexpr int eg(int s) { return (s / 6) * 2 + (s & 1); }
constexpr int ef(int s) { return (s % 6) >> 1; }
constexpr int apos(int s) { return s < NE ? eg(s) * 3 + ef(s) : s + NE; }
constexpr int wpos(int s, int k) { return s < NE ? (eg(s) * 4 + k) * 7 + 4 + ef(s) : (s - NE) * 7 + k; }
struct WSrc { rsrc_t rs; unsigned v, f[3]; };
DI WSrc wsrc(const bf16* Wd, int j) {
  const Tile t = tile();
  const int lane = lane_id(), grp = t.bn * 2 + (j >> 1), ch = lane & 7, gg = ((lane >> 4) & 1) | ((j & 1) << 1);
  WSrc w;
  w.rs = mk_rsrc((const char*)Wd + grp * 128, F_DIM * D_DIM * 2 - grp * 128);
  w.v = (unsigned)(((j & 1) * 8 + (lane >> 3)) * 2048 + ((ch >> 1) ^ gg) * 32 + (ch & 1) * 16);
  w.f[0] = t.kh * (112 * FR); w.f[1] = t.kh * ((44 - NF) * 7 * FR); w.f[2] = 0;
  asm volatile("" : "+v"(w.v), "+s"(w.f[0]), "+s"(w.f[1]), "+s"(w.f[2]));
  return w;
}
constexpr int cls(int s) { return s < NE ? 0 : (s < NF ? 1 : 2); }
}

template <int FN>
DI void g3_body(char* smem, unsigned smem_base, int wm, int wn, int wid, int lane, int bm, int bn, Stg st,
                rsrc_t rsW, const bf16* Wd, char* ws) {
  const unsigned tmask = (wm < 2 ? 0x1111111u : 0x1100000u) << ((blockIdx.x >> 3) & 1);
  constexpr int NB = 2 * FN, NM = 8 * NB;
  const int fr = lane & 15, fq = lane >> 4;
  f32x4 acc[4][NB];
#pragma unroll
  for (int i = 0; i < 4; ++i)
#pragma unroll
    for (int j = 0; j < NB; ++j) { acc[i][j] = f32x4{0, 0, 0, 0}; asm volatile("" : "+v"(acc[i][j])); }
  unsigned aad[2], bad[2][FN];
  aad[0] = smem_base + wm * 4096 + lane * 16; aad[1] = aad[0] + 65536;
  {
    const int q = (lane >> 2) & 3, p = lane & 3;
    const int r = 8 * fq + q;
    const int sw = (r & 3) | (((r >> 3) & 1) << 2);
#pragma unroll
    for (int j = 0; j < FN; ++j) {
      const int gi = wn * 4 + j + (bn & 1);
      bad[0][j] = smem_base + 16384 + r * 256 + ((gi ^ sw) * 32) + 8 * p;
      bad[1][j] = bad[0][j] + 65536;
    }
  }
  u32x4 a[2][4];
  u32x2 bl[2][NB], bh[2][NB];
  const unsigned m0w = smem_base + wid * 2048;
  constexpr int GDS = FN == 4 ? GD4 : GD3;
  constexpr int L = NM - 16;
  auto rdb = [&](auto HH, auto GG, auto RR) {
    constexpr int j = GG.value * 2 + (RR.value >> 2), k = (RR.value >> 1) & 1, off = k * 32768 + (j / FN) * 8192 + (RR.value & 1) * 1024;
    if constexpr (RR.value & 1) dsr_tr<off>(bh[k][j], bad[HH.value][j % FN]);
    else dsr_tr<off>(bl[k][j], bad[HH.value][j % FN]);
  };
  auto row = [](int h, int n) constexpr { return ((n >> 4) ^ (FN == 4 ? h : 0)) & 1 ? 3 - ((n >> 2) & 3) : (n >> 2) & 3; };
  auto tail = [&](auto HH, auto NN) {
    constexpr int h = HH.value, n = NN.value;
    if constexpr (n & 2) dsr128<(~n & 1) * 32768 + row(h, n) * 1024>(a[~n & 1][row(h, n)], aad[h]);
    else rdb(HH, ic<FN - 2>{}, ic<(n >> 2) * 2 + (n & 1)>{});
  };
  unsigned vt = (lane & 31) * 128u + (lane >> 5) * 0x40000000u;
  const rsrc_t rsD = mk_rsrc((const char*)Wd + blockIdx.x * 28672, 28672);
  auto period = [&](auto HH, auto LAST, int u) {
    constexpr int h = HH.value;
    constexpr bool last = LAST.value && h == 1;
    constexpr int X = last ? GLX : 16;
    static_for<NM>([&](auto N) {
      constexpr int n = N.value, p = n >> 1, k = (n ^ p) & 1, i = row(h, n), j = (n >> 4) * 2 + (((p + 1) >> 1) & 1);
      (void)&acc; (void)&a; (void)&bl; (void)&bh; (void)&st; (void)&rsW; (void)&rsD; (void)&vt;
      if constexpr (last && FN == 3 && n == GLP) __builtin_amdgcn_s_setprio(EPR);
      if constexpr (n < X && n % 4 == 0) asm volatile("s_waitcnt lgkmcnt(%0)" ::"n"(n < 12 ? 12 : 8) : "memory");
      if constexpr (n == X) {
        asm volatile("s_waitcnt vmcnt(%0) lgkmcnt(0)" ::"n"(last ? 63 : 4) : "memory");
        __builtin_amdgcn_s_barrier();
      }
      if constexpr (n == L && !last) {
        wait_vm<4>();
        __builtin_amdgcn_s_barrier();
      }
      if constexpr (FN == 4 && n == TPOS && !LAST.value) {
        if (__builtin_expect((tmask >> (u + h)) & 1u, 0))
          touch8k(smem_base + QH_OFF, vt, u >= 20 ? rsD : rsW, u >= 20 ? (u - 20 + wm) * 4096u : (u + 8u) << 18);
      }
      if constexpr (LAST.value && h == 0 && n == 10) {
        const d4::WSrc w = d4::wsrc(Wd, wm);
        st.rA = st.rB = w.rs; st.vA = st.vB = w.v;
        st.sA = w.f[0] + (4 + wn * 14) * d4::FR; st.sB = st.sA + 28 * d4::FR; st.dA = st.dB = d4::FR;
        st.e3 = 7 * d4::FR; st.e1 = st.e3 - 1024;
      }
      MFMA_AV(acc[i][j], (u32x4{bl[k][j][0], bl[k][j][1], bh[k][j][0], bh[k][j][1]}), a[k][i]);
      if constexpr (n < 8) rdb(HH, ic<FN - 1>{}, ic<n & 7>{});
      if constexpr (last) {
        if constexpr (n > X && (n - X - 1) % GDS == 0 && (n - X - 1) / GDS < 4) G3_STAGE(2, (n - X - 1) / GDS);
      } else {
        if constexpr (n >= L) tail(ic<h ^ 1>{}, ic<n & 15>{});
        if constexpr (n >= 16 && n < L && (n & 15) < 8) rdb(ic<h ^ 1>{}, ic<(n >> 4) - 1>{}, ic<n & 7>{});
        constexpr int d = n >= L ? 4 * GDS + n - L - 1 : (n - 17 < 4 * GDS ? n - 17 : -1);
        if constexpr (d >= 0 && d % GDS == 0 && d / GDS < 8) G3_STAGE(2 * h + ((d / GDS >> 1) & 1), (d / GDS & 1) + (d / GDS < 4 ? 2 : 0));
      }
    });
  };
  static_for<8 * (FN - 2)>([&](auto RR) { rdb(ic<0>{}, ic<RR.value / 8>{}, ic<RR.value & 7>{}); });
#pragma unroll
  for (int j = 0; j < FN; ++j) asm volatile("" : "+v"(bad[1][j]));
  asm volatile("" : "+v"(aad[0]), "+v"(aad[1]), "+v"(vt), "+s"(st.sA), "+s"(st.sB) : "s"(rsD));
  wait_vm<4>();
  __builtin_amdgcn_s_barrier();
  STAMPDECL;
  STAMPNW(5, 1);
  static_for<16>([&](auto RR) { tail(ic<0>{}, RR); });
  for (int u = 0; u < 28; u += 4) {
    period(ic<0>{}, ic<0>{}, u);
    period(ic<1>{}, ic<0>{}, u);
  }
  const int J0 = FN == 4 ? (24 + bn) * 4 : (bn >> 2) * 12 + (bn & 3);
  const rsrc_t rsS = mk_rsrc(ws + WS_ACT + ((bm * 4 + wm) * (F_DIM / 32) + (J0 >> 1)) * 4096 + (J0 & 1) * 512, 20480);
  unsigned vs = (fq & 1) * 1024 + ((fq >> 1) * 16 + fr) * 16;
  asm volatile("" : "+v"(vs) : "s"(rsS));
  period(ic<0>{}, ic<1>{}, 28);
  period(ic<1>{}, ic<1>{}, 28);
  asm volatile("s_nop 15" ::: "memory");
#pragma unroll
  for (int j = 0; j < NB; ++j) asm volatile("" : "+v"(acc[0][j]), "+v"(acc[1][j]), "+v"(acc[2][j]), "+v"(acc[3][j]));
  SCHED0();
  STAMP(5, 2);
  STAMPT(3, 5, 256);
  STAMPFLUSH(5, 1);
  if constexpr (FN == 3 && GLP >= NM) __builtin_amdgcn_s_setprio(EPR);
  static_for<FN>([&](auto JJ) {
    constexpr int j = JJ.value;
    (void)&vs; (void)&rsS;
    u32x2 pk[4];
    {
      f32x2 e[8], gu[8];
#pragma unroll
      for (int g = 0; g < 8; ++g) e[g] = ((g & 1) ? hi2(acc[g >> 1][j]) : lo2(acc[g >> 1][j])) * -1.4426950408889634f;
      SCHED0();
#pragma unroll
      for (int g = 0; g < 8; ++g) e[g] = f32x2{__builtin_amdgcn_exp2f(e[g][0]), __builtin_amdgcn_exp2f(e[g][1])};
      SCHED0();
#pragma unroll
      for (int g = 0; g < 8; ++g) e[g] = e[g] + 1.0f;
      SCHED0();
#pragma unroll
      for (int g = 0; g < 8; ++g) e[g] = f32x2{__builtin_amdgcn_rcpf(e[g][0]), __builtin_amdgcn_rcpf(e[g][1])};
      SCHED0();
#pragma unroll
      for (int g = 0; g < 8; ++g) gu[g] = (g & 1) ? hi2(acc[g >> 1][j]) * hi2(acc[g >> 1][FN + j]) : lo2(acc[g >> 1][j]) * lo2(acc[g >> 1][FN + j]);
      SCHED0();
#pragma unroll
      for (int g = 0; g < 8; ++g) gu[g] = gu[g] * e[g];
      SCHED0();
#pragma unroll
      for (int g = 0; g < 8; ++g) pk[g >> 1][g & 1] = pk_bf16(gu[g][0], gu[g][1]);
    }
#pragma unroll
    for (int i = 0; i < 4; i += 2) {
      const auto s0 = __builtin_amdgcn_permlane16_swap(pk[i][0], pk[i + 1][0], false, false);
      const auto s1 = __builtin_amdgcn_permlane16_swap(pk[i][1], pk[i + 1][1], false, false);
      asm volatile("buffer_store_dwordx4 %0, %1, %2, %3 offen offset:%4 sc1\n\ts_nop 1" ::"v"(u32x4{s0[0], s1[0], s0[1], s1[1]}), "v"(vs), "s"(rsS),
                   "s"(FN == 4 ? (j >> 1) * 4096 : j * 8192), "i"(i * 1024 + (FN == 4 ? (j & 1) * 512 : 0)) : "memory");
    }
  });
  STAMP(5, 3);
  STAMPT(5, 5, 256);
}
DI unsigned flag_off(unsigned b, unsigned wid) {
  const unsigned xcd = b & 7, idx = b >> 3;
  return ((idx & 7) * 2 + ((wid >> 1) & 1)) * 512 + (xcd * 4 + (idx >> 3)) * 16 + (wid & 1) * 8 + (wid >> 2) * 4;
}
DI void act_flag(char* ws, int lane, int wid, unsigned tag) {
  asm volatile("buffer_store_dword %0, %1, %2, 0 offen sc1" ::"v"(tag), "v"(lane * 8192), "s"(mk_rsrc(ws + WS_FA + flag_off(blockIdx.x, wid), 7 * 8192 + 4)) : "memory");
}

#define DPS (N - 3)
#define DFS 0
#define DSL 4
#define DLA 4
#define DMX 3
#define LPR 0
namespace d4 {
struct Sched { int tA[N], tW[N], eA[N], eW[N], eB, cnt[N + 2], last[N], tL; };
constexpr Sched sched() {
  Sched q = {};
  unsigned fr = 7u << 6;
  int nA = 0, nW = 6, c = 0;
  auto take = [&]() { int e = 0; while (!((fr >> e) & 1)) ++e; fr &= ~(1u << e); return e; };
  for (int s = 0; s < 6; ++s) { q.tW[s] = -1; q.eW[s] = s; }
  for (int t = 0; t <= N; ++t) {
    q.cnt[t] = c;
    if (nA >= NE - 2 && !q.tL) { q.tL = t; c += 1; }
    if (t == 1) fr |= 1u << 9;
    if (t >= 2) fr |= (1u << q.eA[t - 2]) | (1u << q.eW[t - 2]);
    for (int m = 0; m < DMX && fr; ++m) {
      const bool a = nA <= nW;
      const int s = a ? nA : nW;
      if (s >= N || s > t + DLA) break;
      if (a) { q.tA[s] = t; q.eA[s] = take(); c += s < NF ? 4 : 2; ++nA; } else { q.tW[s] = t; q.eW[s] = take(); c += 4; ++nW; }
      q.last[s] = c;
    }
    if (t == N - 1) q.eB = take();
  }
  q.cnt[N + 1] = c;
  return q;
}
constexpr Sched Q = sched();
template <int M, int S, int W = 0>
DI void dma(unsigned m0b, unsigned voff, rsrc_t rs, unsigned so) {
  unsigned t;
  if constexpr (W)
    asm volatile("s_add_u32 m0, %1, %5\n\ts_add_u32 %0, %4, %6\n\tbuffer_load_dwordx4 %2, %3, %0 offen lds"
                 : "=&s"(t) : "s"(m0b), "v"(voff), "s"(rs), "s"(so), "n"(M), "n"(S) : "memory", "scc");
  else
    asm volatile("s_add_u32 m0, %1, %5\n\ts_add_u32 %0, %4, %6\n\tbuffer_load_dwordx4 %2, %3, %0 offen sc1 lds"
                 : "=&s"(t) : "s"(m0b), "v"(voff), "s"(rs), "s"(so), "n"(M), "n"(S) : "memory", "scc");
}
}
DI void d4_loader(char* smem, unsigned smem_base, int lane, int j, const bf16* Wd, char* ws, unsigned tag) {
  using namespace d4;
  wait_vm<0>();
  act_flag(ws, lane, 4 + j, tag);
  if (blockIdx.x == 0 && j == 3) asm volatile("buffer_store_dword %0, %1, %2, 0 offen sc1" ::"v"(tag), "v"(lane * 4), "s"(mk_rsrc(ws, 4)) : "memory");
  STAMPT(5, 6, 256);
  __builtin_amdgcn_s_setprio(LPR);
  const Tile t = tile();
  const rsrc_t rsA = mk_rsrc(ws + WS_ACT + t.bm * 917504, 917504);
  const WSrc w = wsrc(Wd, j);
  unsigned vA = lane * 16 + j * 1024;
  const unsigned mA = smem_base + j * 1024, mW = smem_base + j * 2048;
  unsigned sO[3] = {t.kh * (458752u + NE * 8192), t.kh * (458752u + (44 - NF) * 8192), t.kh * 458752u};
  unsigned sP[2] = {458752u - t.kh * (458752u - NE * 8192), 458752u - t.kh * (458752u - (44 - NF) * 8192)};
  asm volatile("" : "+v"(vA));
  asm volatile("" : "+s"(sO[0]), "+s"(sO[1]), "+s"(sO[2]), "+s"(sP[0]), "+s"(sP[1]));
  const rsrc_t rsF = mk_rsrc(ws + WS_FA + t.bn * 8192, 8192);
  if (j == 0) {
    if (DSL) __builtin_amdgcn_s_sleep(DSL);
    unsigned spins = 0, f;
    do {
      asm volatile("buffer_load_dword %0, %1, %2, %3 offen offset:4 sc1\n\ts_waitcnt vmcnt(0)" : "=v"(f) : "v"((lane & 31) * 8), "s"(rsF), "s"(t.bm * 512 + t.kh * 256) : "memory");
    } while (!__all(f == tag) && ++spins < (1u << 18));
    dsw32<768>(smem_base + QT_OFF, 1u);
  } else {
    unsigned n;
    do {
      asm volatile("s_sleep 1\n\tds_read_b32 %0, %1 offset:768\n\ts_waitcnt lgkmcnt(0)" : "=v"(n) : "v"(smem_base + QT_OFF) : "memory");
    } while (__builtin_amdgcn_readfirstlane(n) == 0u);
  }
  STAMPT(6, 0, 256);
  unsigned fl;
  const int cn = t.bn * 2 + ((lane >> 2) & 1);
  const unsigned vF = lane < 2 * (44 - NF) ? t.bm * 512 + t.kh * ((NF - NE) * 16) + lane * 8
                    : (lane < 56 ? flag_off(blockIdx.x ^ 1, 4 + (lane & 3)) : flag_off(((cn & 7) * 4 + (t.bm & 3)) * 8 + (t.bm >> 2) * 2 + (cn >> 3), t.kh * 2 + (lane & 1) + (lane & 2) * 2));
  static_for<N>([&](auto T) {
    constexpr int i = T.value;
    (void)&fl;
    if constexpr (i == Q.tL) { asm volatile("" : "=v"(fl)); bload1_sc1(fl, vF, rsF); }
    if constexpr (i == Q.tA[NE]) {
      asm volatile("s_waitcnt vmcnt(%1)" : "+v"(fl) : "n"(Q.cnt[i] - Q.cnt[Q.tL] - 1) : "memory");
      if (__builtin_expect(!__all(fl == tag), 0)) {
        unsigned spins = 0, f;
        do {
          asm volatile("buffer_load_dword %0, %1, %2, 0 offen sc1\n\ts_waitcnt vmcnt(0)" : "=v"(f) : "v"(vF), "s"(rsF) : "memory");
        } while (!__all(f == tag) && ++spins < (1u << 18));
      }
    }
    static_for<N>([&](auto S) {
      constexpr int s = S.value;
      if constexpr (Q.tA[s] == i) {
        static_for<2>([&](auto K) {
          constexpr int k = K.value, m = Q.eA[s] * 16384 + k * 8192;
          if constexpr (s < NF) dma<m, (apos(s) * 2 + k) * 4096>(mA, vA, rsA, sP[cls(s) & 1]);
          dma<m + 4096, (apos(s) * 2 + k) * 4096>(mA, vA, rsA, sO[cls(s)]);
        });
      }
      if constexpr (Q.tW[s] == i) {
        static_for<4>([&](auto K) {
          constexpr int k = K.value;
          dma<Q.eW[s] * 16384 + (k >> 1) * 8192 + (k & 1) * 1024, wpos(s, k) * FR, 1>(mW, w.v, w.rs, w.f[cls(s)]);
        });
      }
    });
    wait_vm<Q.cnt[i + 1] - Q.last[i]>();
    __builtin_amdgcn_s_barrier();
  });
}
DI bool missing(const u32x4 (&pv)[4]) {
  unsigned m = 0;
#pragma unroll
  for (int f = 0; f < 4; ++f) m = max(max(m, max(pv[f][0], pv[f][1])), max(pv[f][2], pv[f][3]));
  return __any(m == ~0u);
}
DI void d4_mfma(char* smem, unsigned smem_base, int lane, int w, bf16* out, char* ws, unsigned tag) {
  using namespace d4;
  const Tile t = tile();
  const int fr = lane & 15, fq = lane >> 4, wr = w & 1, wc = w >> 1;
  unsigned brd[3][4], ard[3], vL = lane * 16;
  {
    const int q = (lane >> 2) & 3, p = lane & 3, g = (q >> 1) | ((fq & 1) << 1);
#pragma unroll
    for (int i = 0; i < 3; ++i) {
#pragma unroll
      for (int f = 0; f < 4; ++f) {
        brd[i][f] = smem_base + i * 65536 + wc * 4096 + (fq & 1) * 2048 + (fq >> 1) * 1024 + q * 128 + ((f ^ g) * 32) + 8 * p;
        asm volatile("" : "+v"(brd[i][f]));
      }
      ard[i] = smem_base + i * 65536 + wr * 2048 + lane * 16;
      asm volatile("" : "+v"(ard[i]));
    }
  }
  asm volatile("" : "+v"(vL));
  f32x4 acc[4][4];
#pragma unroll
  for (int i = 0; i < 4; ++i)
#pragma unroll
    for (int f = 0; f < 4; ++f) { acc[i][f] = f32x4{0, 0, 0, 0}; asm volatile("" : "+v"(acc[i][f])); }
  const rsrc_t rsX = mk_rsrc(ws + xoff(blockIdx.x) + w * 4096, 4096), rsP = mk_rsrc(ws + xoff(blockIdx.x ^ 1) + w * 4096, 4096);
  const unsigned bo = Q.eB * 16384 + w * 4096;
  unsigned tw[4], tr = smem_base + bo + (lane >> 3) * 128 + (((lane & 7) ^ (lane >> 3)) * 16);
  const rsrc_t rsO = mk_rsrc((char*)out + ((t.bm * 2 + t.kh) * 64 + wr * 32) * (D_DIM * 2) + t.bn * 256 + wc * 128, 31 * D_DIM * 2 + 128);
  unsigned vO = (lane >> 3) * (D_DIM * 2) + (lane & 7) * 16;
#pragma unroll
  for (int f = 0; f < 4; ++f) tw[f] = smem_base + bo + fr * 128 + (((f * 2 + (fq >> 1)) ^ (fr & 7)) * 16) + (fq & 1) * 8;
  asm volatile("" : "+v"(tw[0]), "+v"(tw[1]), "+v"(tw[2]), "+v"(tw[3]), "+v"(tr), "+v"(vO) : "s"(rsO));
  __builtin_amdgcn_s_setprio(2);
  u32x4 gt[4], xq[4], ex, pv[4];
  f32x2 base[16];
  const rsrc_t rsR = mk_rsrc(ws + WS_X2 + (((t.bm * 2 + t.kh) * 4 + wr * 2) * 16 + t.bn * 2 + wc) * 2048, 32768 + 2048);
  unsigned k32 = 32768;
  asm volatile("" : "+s"(k32) : "s"(rsR));
  static_for<4>([&](auto F) { dsr128<1024 + F.value * 64>(gt[F.value], smem_base + QT_OFF + wc * 256 + fq * 16); });
  asm volatile("s_waitcnt vmcnt(8) lgkmcnt(0)" ::: "memory");
  STAMP(5, 4);
  __builtin_amdgcn_s_barrier();
  STAMPDECL;
  STAMPNW(6, 1);
  u32x4 a[2][2][4];
  u32x2 bl[2][2][4], bh[2][2][4];
  auto rd = [&](auto S, auto RR) {
    constexpr int s = S.value, per = s < NF ? 12 : 10, k = RR.value / per, r = RR.value % per;
    if constexpr (r < 8) {
      constexpr int e = Q.eW[s], off = (e & 3) * 16384 + k * 8192 + (r & 1) * 512;
      if constexpr (r & 1) dsr_tr<off>(bh[s & 1][k][r >> 1], brd[e >> 2][r >> 1]);
      else dsr_tr<off>(bl[s & 1][k][r >> 1], brd[e >> 2][r >> 1]);
    } else {
      constexpr int e = Q.eA[s], i = s < NF ? r - 8 : r - 6;
      dsr128<(e & 3) * 16384 + k * 8192 + (i >> 1) * 4096 + (i & 1) * 1024>(a[s & 1][k][i], ard[e >> 2]);
    }
  };
  static_for<24>([&](auto RR) { rd(ic<0>{}, RR); });
  static_for<N>([&](auto S) {
    constexpr int s = S.value, nm = s < NF ? 32 : 16, nr = s + 1 == N ? 0 : (s + 1 < NF ? 24 : 20), pg = s < NF ? 1 : 2;
    WAIT_LGKM0();
    u32x4 bb[2][4];
#pragma unroll
    for (int k = 0; k < 2; ++k)
#pragma unroll
      for (int f = 0; f < 4; ++f) bb[k][f] = u32x4{bl[s & 1][k][f][0], bl[s & 1][k][f][1], bh[s & 1][k][f][0], bh[s & 1][k][f][1]};
    SCHED0();
    static_for<nm>([&](auto M) {
      constexpr int n = M.value;
      (void)&base; (void)&xq; (void)&rsR; (void)&k32; (void)&rsX; (void)&vL; (void)&ex; (void)&pv; (void)&rsP;
      if constexpr (n == 2 && s + 1 < N) { __builtin_amdgcn_s_barrier(); asm volatile(".p2align 3"); }
      {
        constexpr int p = n >> 1, k = (n ^ p) & 1, f = s < NF ? p >> 2 : p >> 1, i = s < NF ? ((f & 1) ? 3 - (p & 3) : p & 3) : 2 + ((p ^ f) & 1);
        MFMA_AV(acc[i][f], bb[k][f], a[s & 1][k][i]);
      }
      static_for<pg>([&](auto G) {
        constexpr int r = (n - 2) * pg + G.value;
        if constexpr (n >= 2 && r < nr) rd(ic<(s + 1 < N ? s + 1 : 0)>{}, ic<(r >= 0 && r < nr ? r : 0)>{});
      });
      if constexpr (s == DFS && n == 28) { wait_vm<0>(); act_flag(ws, lane, w, tag); }
      if constexpr (s == NE + 1 && n >= 28) { asm volatile("" : "=v"(xq[n & 3])); bload_sc1<(n & 1) * 1024>(xq[n & 3], vL, rsR, (n & 2) ? k32 : 0u); }
      if constexpr (s == NF - 1 && n == 28) wait_vm<0>();
      if constexpr ((s == NF || s == NF + 1) && n >= 4 && n < 14) {
        constexpr int i = s - NF, p = (n - 4) / 5, h = (n - 4) % 5;
        if constexpr (h < 4) {
          asm volatile("v_cvt_pk_bf16_f32 %0, %1, %2\n\tv_min_u32 %0, -2, %0" : "=v"(ex[h]) : "v"(acc[i][2 * p + (h >> 1)][2 * (h & 1)]), "v"(acc[i][2 * p + (h >> 1)][2 * (h & 1) + 1]));
        } else {
          asm volatile("buffer_store_dwordx4 %0, %1, %2, 0 offen offset:%3 sc1" ::"v"(ex), "v"(vL), "s"(rsX), "i"(i * 2048 + p * 1024) : "memory");
        }
      }
      if constexpr (s == DPS && n >= 12) { asm volatile("" : "=v"(pv[n & 3])); bload_sc1<(n & 3) * 1024>(pv[n & 3], vL, rsP, 0); }
      if constexpr (s + 1 == N) base[n] = unpk_v(xq[n >> 2][n & 3]);
    });
    SCHED0();
  });
  asm volatile("s_nop 7\n\ts_waitcnt vmcnt(0) lgkmcnt(0)" : "+v"(gt[0]), "+v"(gt[1]), "+v"(gt[2]), "+v"(gt[3]), "+v"(pv[0]), "+v"(pv[1]), "+v"(pv[2]), "+v"(pv[3])::"memory");
  if (__builtin_expect(missing(pv), 0)) {
    unsigned spins = 0;
    do {
      asm volatile("buffer_load_dwordx4 %0, %4, %5, 0 offen sc1\n\tbuffer_load_dwordx4 %1, %4, %5, 0 offen offset:1024 sc1\n\t"
                   "buffer_load_dwordx4 %2, %4, %5, 0 offen offset:2048 sc1\n\tbuffer_load_dwordx4 %3, %4, %5, 0 offen offset:3072 sc1\n\t"
                   "s_waitcnt vmcnt(0)"
                   : "=&v"(pv[0]), "=&v"(pv[1]), "=&v"(pv[2]), "=&v"(pv[3]) : "v"(vL), "s"(rsP) : "memory");
    } while (missing(pv) && ++spins < (1u << 18));
  }
  STAMP(6, 2); STAMPFLUSH(6, 1);
  u32x4 ov[4];
  static_for<8>([&](auto E) {
    constexpr int e = E.value, i = e >> 2, f = e & 3;
    asm volatile("" : "+v"(acc[2 + i][f]));
    const unsigned p0 = pv[e >> 1][(e & 1) * 2], p1 = pv[e >> 1][(e & 1) * 2 + 1];
    const f32x4 v = f32x4{base[2 * e][0], base[2 * e][1], base[2 * e + 1][0], base[2 * e + 1][1]} + __builtin_bit_cast(f32x4, gt[f]) * (acc[2 + i][f] + f32x4{bf_lo(p0), bf_hi(p0), bf_lo(p1), bf_hi(p1)});
    asm volatile("ds_write_b64 %0, %1 offset:%2" ::"v"(tw[f]), "v"(u32x2{pk_bf16(v[0], v[1]), pk_bf16(v[2], v[3])}), "i"(i * 2048) : "memory");
    if constexpr (f == 3) { dsr128<i * 2048>(ov[2 * i], tr); dsr128<i * 2048 + 1024>(ov[2 * i + 1], tr); }
    if constexpr (e == 6) {
      asm volatile("s_waitcnt lgkmcnt(3)" : "+v"(ov[0]), "+v"(ov[1])::"memory");
      gstore4_sc1(ov[0], vO, rsO, 0); gstore4_sc1(ov[1], vO, rsO, 8 * D_DIM * 2);
    }
  });
  asm volatile("s_waitcnt lgkmcnt(0)" : "+v"(ov[2]), "+v"(ov[3])::"memory");
  gstore4_sc1(ov[2], vO, rsO, 16 * D_DIM * 2); gstore4_sc1(ov[3], vO, rsO, 24 * D_DIM * 2);
  STAMP(6, 3);
  STAMP(6, 4);
}

DI void g3_phase(char* smem, unsigned smem_base, int tid, int wid, const bf16* __restrict__ Wg, const bf16* __restrict__ Wu,
                 const bf16* __restrict__ Wd, bf16* out, char* ws, int own, unsigned tag) {
  const int lane = tid & 63;
  const int wm = wid & 3, wn = wid >> 2;
  const int xcd = blockIdx.x & 7, idx = blockIdx.x >> 3;
  const int bm = idx & 7, bn = xcd * 4 + (idx >> 3);
  const int n0 = bn * 112, n0a = n0 & ~31;
  unsigned* yflags = (unsigned*)(ws + WS_FY);
  const rsrc_t rsW = mk_rsrc((const char*)(wm & 1 ? Wu : Wg) + blockIdx.x * 4096, D_DIM * F_DIM * 2 - blockIdx.x * 4096);
  Stg st;
  st.rA = mk_rsrc(ws + WS_Y + (size_t)bm * 32 * 16384, 32 * 16384);
  st.rB = mk_rsrc(wn ? Wu : Wg, D_DIM * F_DIM * 2);
  st.vA = wid * 2048 + lane * 16;
  {
    const int r = wm * 8 + (lane >> 4);
    const int sw = (r & 3) | (((r >> 3) & 1) << 2);
    const int c16 = lane & 15;
    st.vB = (unsigned)(r * (F_DIM * 2) + (n0a + (((c16 >> 1) ^ sw) * 16) + (c16 & 1) * 8) * 2);
  }
  st.sA = 0; st.sB = 0; st.dA = 16384; st.dB = 229376; st.e1 = 0; st.e3 = 4 * F_DIM * 2;
  const unsigned m0w = smem_base + wid * 2048;
  asm volatile("" : "+s"(out), "+s"(Wd));
  G3_STAGE(0, 2); G3_STAGE(0, 3); G3_STAGE(1, 2); G3_STAGE(1, 3);
  wait_vm<4>();
  __builtin_amdgcn_s_barrier();
  STAMP(3, 4);
  if (wid == 0) asm volatile("buffer_store_dword %0, %1, %2, 0 offen sc1" ::"v"(tag), "v"(lane * (1024 * YFS)), "s"(mk_rsrc(yflags + own * YFS, 7 * 1024 * YFS + 4)) : "memory");
  G3_STAGE(2, 2); G3_STAGE(2, 3); G3_STAGE(3, 2); G3_STAGE(3, 3);
  if (wn == 0) {
    if (wm == 0) {
      const rsrc_t rsF = mk_rsrc(yflags + (xcd * 256 + bm * 32) * YFS, 128 * YFS);
      if (YSL) __builtin_amdgcn_s_sleep(YSL);
      unsigned spins = 0, f;
      do {
        asm volatile("buffer_load_dword %0, %1, %2, 0 offen sc1\n\ts_waitcnt vmcnt(0)" : "=v"(f) : "v"((lane & 31) * (4 * YFS)), "s"(rsF) : "memory");
      } while (!__all(f == tag) && ++spins < (1u << 18));
    }
    wait_vm<4>();
    __builtin_amdgcn_s_barrier();
    STAMP(5, 0);
    G3_STAGE(0, 0); G3_STAGE(0, 1); G3_STAGE(1, 0); G3_STAGE(1, 1);
    G3_STAGE(2, 0); G3_STAGE(2, 1); G3_STAGE(3, 0); G3_STAGE(3, 1);
    g3_body<4>(smem, smem_base, wm, wn, wid, lane, bm, bn, st, rsW, Wd, ws);
    d4_mfma(smem, smem_base, lane_id(), wm, out, ws, tag);
  } else {
    wait_vm<4>();
    __builtin_amdgcn_s_barrier();
    G3_STAGE(0, 0); G3_STAGE(0, 1); G3_STAGE(1, 0); G3_STAGE(1, 1);
    G3_STAGE(2, 0); G3_STAGE(2, 1); G3_STAGE(3, 0); G3_STAGE(3, 1);
    g3_body<3>(smem, smem_base, wm, wn, wid, lane, bm, bn, st, rsW, Wd, ws);
    d4_loader(smem, smem_base, lane_id(), wm, Wd, ws, tag);
  }
}

__global__ __launch_bounds__(512, 2) void block_kernel(const bf16* x_, const bf16* c_, const bf16* wmod_, char* ws_, const bf16* wq_,
                                                       const bf16* wo_, const bf16* wg_, const bf16* wu_, const bf16* __restrict__ wd, bf16* out) {
  __shared__ __attribute__((aligned(16))) char smem[Q_LDS];
  u64x8 ka;
  asm volatile("s_load_dwordx16 %0, %1, 0x0" : "=s"(ka) : "s"(__builtin_amdgcn_kernarg_segment_ptr()));
  STAMP(0, 0);
  const int tid = threadIdx.x;
  const int wid = __builtin_amdgcn_readfirstlane(tid >> 6);
  const unsigned smem_base = (unsigned)(uintptr_t)smem;
  const int xcd = blockIdx.x & 7, idx = blockIdx.x >> 3;
  {
    unsigned long long pc;
    asm volatile("s_getpc_b64 %0" : "=s"(pc));
    constexpr int CH = (CODESZ + 4095) / 4096 * 128;
    touch8k(smem_base + QH_OFF, tid - 448u < CH / 128 ? (tid - 448) * 128u : 1u << 30, mk_rsrc((const char*)(uintptr_t)(pc & ~255ull), CODESZ), idx * CH);
  }
  const int bm = (xcd >> 2) * 8 + (idx & 7), bn = (xcd & 3) * 4 + (idx >> 3);
  const int cm = (xcd >> 1) * 4 + (idx & 3), cn = __builtin_amdgcn_readfirstlane((xcd & 1) * 8 + (idx >> 2));
  const int lane = tid & 63;
  Pre p;
  {
    const bool gv = ((bm + bn) & 1) == 0;
    unsigned oa = ((bm >> 2) * 4 + (wid & 3)) * 786432 + (bm & 2) * 1024 + (bn ^ 1) * 128, ob = (bm * 128 + (bn >> 1) * 16 + (wid & 3) * 4) * 2048;
    unsigned va = (lane >> 3) * (8 * 12288) + (lane & 7) * 16, vb = lane * 16;
    asm volatile("" : "+s"(oa), "+s"(ob));
    asm volatile("" : "+v"(va), "+v"(vb));
    p.o1 = gv ? oa : ob; p.n1 = gv ? 63 * 12288 + 128 : 8192; p.vo = gv ? va : vb; p.st = gv ? 12288 : 1024;
    dsw32<0>(smem_base + QT_OFF, 0u);
    dsw32<4>(smem_base + QT_OFF, 0u);
    dsw32<768>(smem_base + QT_OFF, 0u);
    asm volatile("" : "+v"(p.vo));
    asm volatile("s_waitcnt lgkmcnt(0)" : "+s"(ka), "+s"(p.o1), "+s"(p.n1), "+s"(p.st));
  }
  asm volatile("s_cmp_lt_u32 %0, 4\n\ts_cbranch_scc1 1\n\ts_barrier" ::"s"(wid) : "scc", "memory");
  const Args g = {(const bf16*)ka[0], (const bf16*)ka[1], (const bf16*)ka[2], (const bf16*)ka[4], (const bf16*)ka[5], (const bf16*)ka[6], (const bf16*)ka[7], (char*)ka[3]};
  char* ws = g.ws;
  const bf16 *x = g.x, *wg = g.wg, *wu = g.wu;
  bf16* O = (bf16*)(ws + WS_O);
  bf16* Y = (bf16*)(ws + WS_Y);
  unsigned* oflags = (unsigned*)(ws + WS_FO);
  float* stat = (float*)(ws + WS_STAT);
  unsigned tag = 0;
  if (__builtin_expect(wid < 4, 1)) {
    __builtin_amdgcn_s_setprio(2);
    tag = front1(smem, smem_base, lane, wid, bm, bn, g, p);
    front_h(smem, smem_base, lane, wid, bm, bn, g, tag);
    qkv_mfma(smem, smem_base, tid, wid, bm * 128, bn * 64, O, oflags, tag);
  } else if (wid < 7) {
    tag = qkv_loader(smem, smem_base, lane, wid - 4, bm, bn, cn, g);
  } else {
    tag = qkv_spare(smem, smem_base, lane, bm, bn, cn, g);
  }
  STAMPT(2, 6, 320);
  STAMPT(3, 0, 448);
  o_phase(smem, smem_base, tid & 63, wid, cm, cn, O, oflags, x, Y, stat, ws, tag);
  g3_phase(smem, smem_base, tid, wid, wg, wu, wd, out, ws, cm * 16 + cn, tag);
}

extern "C" void kernel_launch(void* const* d_in, const int* in_sizes, int n_in, void* d_out, int out_size,
                              void* d_ws, size_t ws_size, hipStream_t stream) {
  const bf16* x = (const bf16*)d_in[0];
  const bf16* wqkv = (const bf16*)d_in[1];
  const bf16* wo = (const bf16*)d_in[2];
  const bf16* wg = (const bf16*)d_in[4];
  const bf16* wu = (const bf16*)d_in[5];
  const bf16* wd = (const bf16*)d_in[6];
  const bf16* c = (const bf16*)d_in[7];
  const bf16* wmod = (const bf16*)d_in[8];
  bf16* out = (bf16*)d_out;

  char* ws = (char*)d_ws;
  block_kernel<<<256, 512, 0, stream>>>(x, c, wmod, ws, wqkv, wo, wg, wu, wd, out);
}
```

```cpp
#include <hip/hip_runtime.h>
#include <hip/hip_bf16.h>
#include <stdint.h>

typedef __hip_bfloat16 bf16;
typedef __attribute__((ext_vector_type(8))) short bf16x8;
typedef __attribute__((ext_vector_type(4))) float f32x4;
typedef __attribute__((ext_vector_type(2))) float f32x2;
typedef __attribute__((ext_vector_type(4))) unsigned u32x4;
typedef __attribute__((ext_vector_type(2))) unsigned u32x2;
typedef __attribute__((ext_vector_type(2))) __bf16 bf16x2_t;
typedef __attribute__((ext_vector_type(8))) unsigned long long u64x8;
typedef __amdgpu_buffer_rsrc_t rsrc_t;

#define DI __device__ __forceinline__
#define TQ 8
#define QSD 4
#define DWO 6
#define DW0 14
#define DWS 16
#define OFS 4
#define ORP 1
#define YFS 32
#define YSL 6
#define CODESZ 78900

#define PW 0
#define STAMP(k, s) do { } while (0)
#define STAMPT(k, s, T) do { } while (0)
#define STAMPNW(k, s)
#define STAMPFLUSH(k, s)
#define STAMPDECL

constexpr int S_LEN = 2048;
constexpr int D_DIM = 1024;
constexpr int F_DIM = 3584;

template <int V> struct ic { static constexpr int value = V; };
template <int N, int I = 0, class F>
DI void static_for(F&& f) {
  if constexpr (I < N) { f(ic<I>{}); static_for<N, I + 1>(f); }
}

DI float bf_lo(unsigned u) { return __uint_as_float(u << 16); }
DI float bf_hi(unsigned u) { return __uint_as_float(u & 0xffff0000u); }
DI unsigned pk_bf16(float lo, float hi) {
  const f32x2 v = {lo, hi};
  return __builtin_bit_cast(unsigned, __builtin_convertvector(v, bf16x2_t));
}
DI rsrc_t mk_rsrc(const void* p, unsigned bytes) { return __builtin_amdgcn_make_buffer_rsrc((void*)p, (short)0, (int)bytes, 0x00020000); }

template <int N>
DI void wait_vm() { asm volatile("s_waitcnt vmcnt(%0)" ::"n"(N > 63 ? 63 : N) : "memory"); }
#define WAIT_LGKM0() do { asm volatile("s_waitcnt lgkmcnt(0)" ::: "memory"); __builtin_amdgcn_sched_barrier(0); } while (0)
#define SCHED0() __builtin_amdgcn_sched_barrier(0)

#define MFMA_A(C, B, A) asm volatile("v_mfma_f32_16x16x32_bf16 %0, %1, %2, %0" : "+a"(C) : "v"(B), "a"(A))
#define MFMA_AV(C, B, A) asm volatile("v_mfma_f32_16x16x32_bf16 %0, %1, %2, %0" : "+v"(C) : "v"(B), "v"(A))
#define PERM_LO(D, X, Y) asm volatile("v_perm_b32 %0, %1, %2, %3" : "=v"(D) : "v"(Y), "v"(X), "s"(0x05040100u))
#define PERM_HI(D, X, Y) asm volatile("v_perm_b32 %0, %1, %2, %3" : "=v"(D) : "v"(Y), "v"(X), "s"(0x07060302u))
template <int OFF>
DI void bload_v(u32x4& d, unsigned voff, rsrc_t rs, unsigned soff) {
  asm volatile("buffer_load_dwordx4 %0, %1, %2, %3 offen offset:%4" : "+v"(d) : "v"(voff), "s"(rs), "s"(soff), "i"(OFF) : "memory");
}
template <int OFF>
DI void bload2_v(u32x2& d, unsigned voff, rsrc_t rs, unsigned soff) {
  asm volatile("buffer_load_dwordx2 %0, %1, %2, %3 offen offset:%4" : "+v"(d) : "v"(voff), "s"(rs), "s"(soff), "i"(OFF) : "memory");
}
template <int OFF>
DI void bload1_v(unsigned& d, unsigned voff, rsrc_t rs, unsigned soff) {
  asm volatile("buffer_load_dword %0, %1, %2, %3 offen offset:%4" : "+v"(d) : "v"(voff), "s"(rs), "s"(soff), "i"(OFF) : "memory");
}
template <int OFF>
DI void bload_lds(unsigned m0v, unsigned voff, rsrc_t rs, unsigned soff) {
  asm volatile("s_mov_b32 m0, %0\n\ts_nop 0\n\tbuffer_load_dwordx4 %1, %2, %3 offen offset:%4 lds" ::"s"(m0v), "v"(voff), "s"(rs), "s"(soff), "i"(OFF) : "memory");
}
DI void touch8k(unsigned m0v, unsigned voff, rsrc_t rs, unsigned soff) {
  asm volatile("s_mov_b32 m0, %0\n\ts_nop 0\n\tbuffer_load_dword %1, %2, %3 offen lds" ::"s"(m0v), "v"(voff), "s"(rs), "s"(soff) : "memory");
}
template <int OFF>
DI void bload_lds_sc1(unsigned m0v, unsigned voff, rsrc_t rs, unsigned soff) {
  asm volatile("s_mov_b32 m0, %0\n\ts_nop 0\n\tbuffer_load_dwordx4 %1, %2, %3 offen offset:%4 sc1 lds" ::"s"(m0v), "v"(voff), "s"(rs), "s"(soff), "i"(OFF) : "memory");
}
template <int OFF>
DI void bload_lds_b(unsigned m0v, unsigned voff, rsrc_t rs, unsigned soff) {
  asm volatile("s_mov_b32 m0, %0\n\ts_nop 0\n\tbuffer_load_dwordx4 %1, %2, %3 offen offset:%4 lds" ::"s"(m0v), "v"(voff), "s"(rs), "s"(soff), "i"(OFF) : "memory");
}
template <int OFF>
DI void bload_lds_a(unsigned m0v, unsigned voff, rsrc_t rs, unsigned soff) {
  asm volatile("s_mov_b32 m0, %0\n\ts_nop 0\n\tbuffer_load_dwordx4 %1, %2, %3 offen offset:%4 sc1 lds" ::"s"(m0v), "v"(voff), "s"(rs), "s"(soff), "i"(OFF) : "memory");
}
template <int OFF>
DI void dsr128(u32x4& d, unsigned addr) { asm volatile("ds_read_b128 %0, %1 offset:%2" : "=v"(d) : "v"(addr), "i"(OFF) : "memory"); }
template <int OFF>
DI void dsr_tr(u32x2& d, unsigned addr) { asm volatile("ds_read_b64_tr_b16 %0, %1 offset:%2" : "=v"(d) : "v"(addr), "i"(OFF) : "memory"); }
template <int OFF>
DI void dsw128(unsigned addr, u32x4 d) { asm volatile("ds_write_b128 %0, %1 offset:%2" ::"v"(addr), "v"(d), "i"(OFF) : "memory"); }
template <int OFF>
DI void dsw32(unsigned addr, unsigned d) { asm volatile("ds_write_b32 %0, %1 offset:%2" ::"v"(addr), "v"(d), "i"(OFF) : "memory"); }

DI void gst128(void* p, u32x4 v) {
  asm volatile("global_store_dwordx4 %0, %1, off sc1\n\ts_nop 1" ::"v"(p), "v"(v) : "memory");
}
DI f32x2 lo2(f32x4 v) { return __builtin_shufflevector(v, v, 0, 1); }
DI f32x2 hi2(f32x4 v) { return __builtin_shufflevector(v, v, 2, 3); }
DI float rowsum4(float v) {
  const unsigned u = __float_as_uint(v);
  const auto a = __builtin_amdgcn_permlane16_swap(u, u, false, false);
  const unsigned t = __float_as_uint(__uint_as_float(a[0]) + __uint_as_float(a[1]));
  const auto b = __builtin_amdgcn_permlane32_swap(t, t, false, false);
  return __uint_as_float(b[0]) + __uint_as_float(b[1]);
}
DI int slow_col(const void* p) { return (int)((3u - ((unsigned)(uintptr_t)p >> 7)) & 7u); }
DI float rbf(float v) { return bf_lo(pk_bf16(v, v)); }
DI f32x2 rbf2(f32x2 v) { const unsigned u = pk_bf16(v[0], v[1]); return f32x2{bf_lo(u), bf_hi(u)}; }
DI float silu_f(float v) { return v / (1.0f + __expf(-v)); }

constexpr unsigned WS_G1M = 0x20000;
constexpr unsigned WS_G1S = 0x40000;
constexpr unsigned WS_G2 = 0x60000;
constexpr unsigned WS_STAT = 0x80000;
constexpr unsigned WS_FO = 0x141000;
constexpr unsigned WS_FC = 0x146000;
constexpr unsigned WS_FY = 0x148000;
constexpr unsigned WS_FH = 0x188000;
constexpr unsigned WS_FA = 0x1C8000;
constexpr unsigned WS_WC = 2u << 20;
constexpr unsigned WS_H = 4u << 20, WS_O = 8u << 20, WS_Y = 12u << 20, WS_ACT = 16u << 20, WS_X = 32u << 20, WS_X2 = 36u << 20;

template <int CTRL>
DI float dpp_get(float b) { return __uint_as_float(__builtin_amdgcn_update_dpp(0u, __float_as_uint(b), CTRL, 0xf, 0xf, true)); }
DI float swap32_add(float a, float b) {
  const auto sw = __builtin_amdgcn_permlane32_swap(__float_as_uint(a), __float_as_uint(b), false, false);
  return __uint_as_float(sw[0]) + __uint_as_float(sw[1]);
}
DI float swap16_add(float a, float b) {
  const auto sw = __builtin_amdgcn_permlane16_swap(__float_as_uint(a), __float_as_uint(b), false, false);
  return __uint_as_float(sw[0]) + __uint_as_float(sw[1]);
}
DI float scatter8(const f32x2 (&a)[4], int lane) {
  const float t0 = swap32_add(a[0][0], a[2][0]), t1 = swap32_add(a[0][1], a[2][1]), t2 = swap32_add(a[1][0], a[3][0]), t3 = swap32_add(a[1][1], a[3][1]);
  const float u0 = swap16_add(t0, t2), u1 = swap16_add(t1, t3);
  const bool b3 = lane & 8;
  return (b3 ? u1 : u0) + dpp_get<0x128>(b3 ? u0 : u1);
}
DI float sum4(const f32x2 (&a)[4]) {
  float t = swap16_add(swap32_add(a[0][0] + a[0][1], a[2][0] + a[2][1]), swap32_add(a[1][0] + a[1][1], a[3][0] + a[3][1]));
  t += dpp_get<0x128>(t); t += dpp_get<0x124>(t); t += dpp_get<0x122>(t); t += dpp_get<0x121>(t);
  return t;
}
DI int lane_id() {
  int l;
  asm volatile("v_mbcnt_lo_u32_b32 %0, -1, 0\n\tv_mbcnt_hi_u32_b32 %0, -1, %0" : "=v"(l));
  return l;
}
DI f32x2 unpk_v(unsigned u) {
  f32x2 r;
  asm volatile("v_lshlrev_b32 %0, 16, %1" : "=v"(r[0]) : "v"(u));
  asm volatile("v_and_b32 %0, 0xffff0000, %1" : "=v"(r[1]) : "v"(u));
  return r;
}

constexpr int Q_SLOT = 21504, Q_NSL = 7;
constexpr int WO_H0 = 4 * Q_SLOT;
constexpr int QX_OFF = Q_SLOT * Q_NSL + 1024;
constexpr int QH_OFF = QX_OFF + 8192;
constexpr int QT_OFF = QH_OFF + 1024;
constexpr int Q_LDS = 163840;
#define POLLW 0
#define OD 3
#define SSL 14
#define SSI 4
#define OMB 6
template <int OFF>
DI void bload_sc1(u32x4& d, unsigned voff, rsrc_t rs, unsigned soff) {
  asm volatile("buffer_load_dwordx4 %0, %1, %2, %3 offen offset:%4 sc1" : "+v"(d) : "v"(voff), "s"(rs), "s"(soff), "i"(OFF) : "memory");
}
DI void bload1_sc1(unsigned& d, unsigned voff, rsrc_t rs) {
  asm volatile("buffer_load_dword %0, %1, %2, 0 offen sc1" : "+v"(d) : "v"(voff), "s"(rs) : "memory");
}
DI unsigned wo_voff(int lane, int odd, int sh) {
  const int q = (lane >> 3) & 3, h = lane >> 5, ch = lane & 7, g = (q >> 1) | (odd << 1);
  return (unsigned)(((h * 16 + q) << sh) + ((((ch >> 1) ^ g) * 16) + (ch & 1) * 8) * 2);
}
DI rsrc_t wo_rsrc(const bf16* wo, const char* wc, int cn) {
  const bool c3 = (cn & 7) == slow_col(wo);
  return mk_rsrc(c3 ? wc + 786432 + (cn >> 3) * 131072 : (const char*)wo + cn * 128, c3 ? 131072 : D_DIM * D_DIM * 2 - cn * 128);
}

#define QY 26
DI unsigned xoff(unsigned b) { return WS_X + b * 16384; }
struct Args { const bf16 *x, *c, *wmod, *wq, *wo, *wg, *wu; char* ws; };
#define CK 3
#define TW 8
#define TO 4
#define TT 2
#define PSL 8
#define PSI 4
DI void gstore2_sc1(u32x2 d, unsigned voff, rsrc_t rs) {
  asm volatile("buffer_store_dwordx2 %0, %1, %2, 0 offen sc1" ::"v"(d), "v"(voff), "s"(rs) : "memory");
}
DI void gstore4_sc1(u32x4 d, unsigned voff, rsrc_t rs, unsigned soff) {
  asm volatile("buffer_store_dwordx4 %0, %1, %2, %3 offen sc1\n\ts_nop 1" ::"v"(d), "v"(voff), "s"(rs), "s"(soff) : "memory");
}
DI void bload_us(unsigned& d, unsigned voff, rsrc_t rs, unsigned soff) {
  asm volatile("buffer_load_ushort %0, %1, %2, %3 offen" : "+v"(d) : "v"(voff), "s"(rs), "s"(soff) : "memory");
}
template <int W0, int W1>
DI void gemv8(float* red, int lane, const u32x4 (&d)[8], unsigned cv) {
  wait_vm<W0>();
  const unsigned sv = __float_as_uint(rbf(silu_f(unpk_v(cv)[0])));
  f32x2 a[4] = {{0, 0}, {0, 0}, {0, 0}, {0, 0}};
  static_for<8>([&](auto J) {
    constexpr int j = J.value;
    if constexpr (j == 4 && W1 != W0) wait_vm<W1>();
    const float s = __uint_as_float(__builtin_amdgcn_update_dpp(__builtin_amdgcn_update_dpp(0u, sv, 0x150 + j, 0xf, 0x3, false), sv, 0x158 + j, 0xf, 0xc, false));
#pragma unroll
    for (int e = 0; e < 4; ++e) a[e] += s * unpk_v(d[j][e]);
  });
  red[(lane & 7) * 8 + (lane >> 3)] = scatter8(a, lane);
}
struct Pre { unsigned o1, n1, vo, st; };
DI unsigned front1(char* smem, unsigned smem_base, int lane, int w4, int bm, int bn, const Args& g, const Pre& p) {
  const bool gv = ((bm + bn) & 1) == 0;
  u32x4 d[8];
  unsigned tag, cv;
  asm volatile("" : "=v"(d[0]), "=v"(d[1]), "=v"(d[2]), "=v"(d[3]), "=v"(d[4]), "=v"(d[5]), "=v"(d[6]), "=v"(d[7]), "=v"(tag), "=v"(cv));
  {
    const rsrc_t rs = mk_rsrc((const char*)(gv ? g.wmod : g.x) + p.o1, p.n1), rsC = mk_rsrc(g.c, 2048);
#pragma unroll
    for (int i = 0; i < 4; ++i) bload_v<0>(d[i], p.vo, rs, i * p.st);
    bload_us(cv, lane * 2, rsC, (bm >> 2) * 512 + w4 * 128);
    bload1_v<0>(tag, 0u, mk_rsrc(g.ws, 4), 0);
#pragma unroll
    for (int i = 4; i < 8; ++i) bload_v<0>(d[i], p.vo, rs, i * p.st);
  }
  STAMPT(0, 6, 192);
  __builtin_amdgcn_s_barrier();
  float* red = reinterpret_cast<float*>(smem + QX_OFF);
  if (gv) {
    gemv8<4, 0>(red + w4 * 64, lane, d, cv);
  } else {
    f32x2 v[4][8], t[4];
    asm volatile("s_waitcnt vmcnt(4)" ::"v"(cv) : "memory");
#pragma unroll
    for (int r = 0; r < 4; ++r) {
      if (r == 2) wait_vm<0>();
#pragma unroll
      for (int e = 0; e < 8; ++e) v[r][e] = unpk_v(d[r * 2 + (e >> 2)][e & 3]);
      t[r] = ((v[r][0] + v[r][1]) + (v[r][2] + v[r][3])) + ((v[r][4] + v[r][5]) + (v[r][6] + v[r][7]));
    }
    const float mean = sum4(t) * (1.0f / D_DIM);
#pragma unroll
    for (int r = 0; r < 4; ++r) {
      const float m = __uint_as_float(__builtin_amdgcn_readlane(__float_as_uint(mean), r * 16));
      f32x2 q0 = {0, 0}, q1 = {0, 0};
#pragma unroll
      for (int e = 0; e < 8; e += 2) { const f32x2 e0 = v[r][e] - m, e1 = v[r][e + 1] - m; q0 += e0 * e0; q1 += e1 * e1; }
      t[r] = q0 + q1;
    }
    const float rstd = 1.0f / rbf(sqrtf(rbf(rbf(sum4(t) * (1.0f / D_DIM)) + 1e-5f)));
    if ((lane & 15) == 0) *reinterpret_cast<f32x2*>(red + w4 * 8 + (lane >> 3)) = f32x2{rstd, -rbf(mean) * rstd};
  }
  asm volatile("ds_add_u32 %1, %2" : "+v"(tag) : "v"(smem_base + QT_OFF), "v"(1u) : "memory");
  STAMPT(0, 1, 192);
  tag = __builtin_amdgcn_readfirstlane(tag) + 0x9E3779B9u;
  const unsigned o = gv ? WS_G1M + (w4 >> 1) * 65536 + ((bm & 2) * 2 + (bm >> 2)) * 8192 + (bn ^ 1) * 512 + (w4 & 1) * 256 : WS_G1S + w4 * 32768 + (bm * 128 + (bn >> 1) * 16) * 16;
  const rsrc_t rsG = mk_rsrc(g.ws + o, 256);
  const f32x2* rp = reinterpret_cast<const f32x2*>(red) + (lane & 15) + (gv ? (w4 & 1) * 16 : 0);
  unsigned n;
  do {
    asm volatile("ds_read_b32 %0, %1\n\ts_waitcnt lgkmcnt(0)" : "=v"(n) : "v"(smem_base + QT_OFF) : "memory");
  } while (__builtin_amdgcn_readfirstlane(n) != 256u);
  f32x2 m = rp[0];
  const f32x2 m2 = (m + rp[32]) + (rp[64] + rp[96]);
  m = gv ? m2 : m;
  gstore4_sc1(u32x4{__float_as_uint(m[0]), tag, __float_as_uint(m[1]), tag}, lane * 16, rsG, 0);
  STAMPT(0, 2, 192);
  return tag;
}
DI void front_ocld(int lane, int w4, int bm, int bn, const Args& g, u32x4 (&oc)[8], unsigned& c2) {
  const rsrc_t rs = mk_rsrc((const char*)g.wmod + ((bm & 3) * 4 + w4) * 786432 + 4096 + (bm >> 2) * 2048 + (bn ^ 2) * 128, 63 * 12288 + 128);
  asm volatile("" : "=v"(c2));
  bload_us(c2, lane * 2, mk_rsrc(g.c, 2048), (bm & 3) * 512 + w4 * 128);
#pragma unroll
  for (int i = 0; i < 8; ++i) {
    wait_vm<TO - 1>();
    asm volatile("" : "=v"(oc[i]));
    bload_v<0>(oc[i], (lane >> 3) * (8 * 12288) + (lane & 7) * 16, rs, i * 12288);
  }
}
template <int NW>
DI void front_oc(char* smem, int lane, int w4, const u32x4 (&oc)[8], unsigned c2) {
  gemv8<NW, NW>(reinterpret_cast<float*>(smem + QX_OFF + 1024) + w4 * 64, lane, oc, c2);
  asm volatile("s_waitcnt lgkmcnt(0)" ::: "memory");
}

DI void h_flag(char* ws, int lane, int wid, int t, unsigned tag) {
  asm volatile("s_waitcnt vmcnt(0)\n\tbuffer_store_dword %0, %1, %2, 0 offen sc1" ::"v"(tag), "v"(lane * 32768), "s"(mk_rsrc(ws + WS_FH + t * 128 + wid * 4, 7 * 32768 + 4)) : "memory");
  STAMP(1, 1);
}
DI void front_h(char* smem, unsigned smem_base, int lane, int wid, int bm, int bn, const Args& g, unsigned tag) {
  const int fr = lane & 15, fq = lane >> 4;
  const int kA = ((bn ^ 3) * 2) ^ (wid & 1), kB = ((bn ^ 3) * 2) ^ (2 + wid), f0 = (wid >> 1) * 4;
  u32x4 xa[5], xb[9];
  const unsigned hc = bm == 0 ? 1u : 0u;
  const unsigned la = smem_base + (wid & 1) * Q_SLOT + f0 * 1024 + lane * 16, lb = smem_base + (2 + wid) * Q_SLOT + lane * 16;
  {
    unsigned n;
    do {
      asm volatile("ds_read_b32 %0, %1 offset:4\n\ts_waitcnt lgkmcnt(0)" : "=v"(n) : "v"(smem_base + QT_OFF) : "memory");
    } while (__builtin_amdgcn_readfirstlane(n) != 192u);
  }
  const unsigned lx = fr * 64 + (fq ^ ((fr >> 1) & 2)) * 16 - lane * 16;
  static_for<5>([&](auto I) { dsr128<I.value * 1024>(xa[I.value], la + lx); });
  static_for<9>([&](auto I) { dsr128<I.value * 1024>(xb[I.value], lb + lx); });
  f32x2 fa[5][4], fb[9][4];
  asm volatile("s_waitcnt lgkmcnt(9)" ::: "memory");
#pragma unroll
  for (int i = 0; i < 5; ++i)
#pragma unroll
    for (int e = 0; e < 4; ++e) fa[i][e] = unpk_v(xa[i][e]);
  asm volatile("s_waitcnt lgkmcnt(0)" ::: "memory");
#pragma unroll
  for (int i = 0; i < 9; ++i)
#pragma unroll
    for (int e = 0; e < 4; ++e) fb[i][e] = unpk_v(xb[i][e]);
  STAMP(0, 4);
  u32x4 pm[4], ps[3];
  {
    const rsrc_t rsM = mk_rsrc(g.ws + WS_G1M + (bm >> 3) * 65536, 65536), rsS = mk_rsrc(g.ws + WS_G1S + (bn >> 2) * 32768, 32768);
    const unsigned vm = ((fr >> 2) & 1) * 32768 + (fr >> 3) * 8192 + fq * 64 + (fr & 3) * 16;
    const unsigned vs0 = (bm * 128 - 16 + lane + (lane < 16 ? hc * 16 : 0)) * 16, vs1 = (bm * 128 + 48 + lane) * 16, vs2 = (bm * 128 + 112 + fr) * 16;
    if (PSL) __builtin_amdgcn_s_sleep(PSL);
    unsigned spins = 0;
    for (;;) {
      asm volatile("buffer_load_dwordx4 %4, %8, %12, 0 offen sc1\n\tbuffer_load_dwordx4 %5, %9, %12, 0 offen sc1\n\tbuffer_load_dwordx4 %6, %10, %12, 0 offen sc1\n\t"
                   "buffer_load_dwordx4 %0, %7, %11, %13 offen sc1\n\tbuffer_load_dwordx4 %1, %7, %11, %14 offen sc1\n\t"
                   "buffer_load_dwordx4 %2, %7, %11, %15 offen sc1\n\tbuffer_load_dwordx4 %3, %7, %11, %16 offen sc1\n\t"
                   "s_waitcnt vmcnt(0)"
                   : "=&v"(pm[0]), "=&v"(pm[1]), "=&v"(pm[2]), "=&v"(pm[3]), "=&v"(ps[0]), "=&v"(ps[1]), "=&v"(ps[2])
                   : "v"(vm), "v"(vs0), "v"(vs1), "v"(vs2), "s"(rsM), "s"(rsS), "s"(kA * 256), "s"(kA * 256 + 16384), "s"(kB * 256), "s"(kB * 256 + 16384) : "memory");
      unsigned bad = 0;
#pragma unroll
      for (int i = 0; i < 4; ++i) bad |= (pm[i][1] ^ tag) | (pm[i][3] ^ tag);
#pragma unroll
      for (int i = 0; i < 3; ++i) bad |= (ps[i][1] ^ tag) | (ps[i][3] ^ tag);
      asm volatile("" : "+v"(bad));
      if (__all(bad == 0u) || ++spins > (1u << 18)) break;
      if (PSI) __builtin_amdgcn_s_sleep(PSI);
    }
  }
  STAMP(0, 3);
  f32x2 rs[9], ra[5];
  {
    u32x2* sp = reinterpret_cast<u32x2*>(smem + QX_OFF + 2048 + wid * 1536);
#pragma unroll
    for (int i = 0; i < 3; ++i) sp[i * 64 + lane] = u32x2{ps[i][0], ps[i][2]};
    __builtin_amdgcn_wave_barrier();
#pragma unroll
    for (int i = 0; i < 5; ++i) ra[i] = __builtin_bit_cast(f32x2, sp[(f0 + i) * 16 + fr]);
#pragma unroll
    for (int i = 0; i < 9; ++i) rs[i] = __builtin_bit_cast(f32x2, sp[i * 16 + fr]);
  }
  auto cols = [&](const u32x4& p0, const u32x4& p1, f32x2 (&sa)[4], f32x2 (&ca)[4]) {
    float m0 = __uint_as_float(p0[0]) + __uint_as_float(p1[0]), m1 = __uint_as_float(p0[2]) + __uint_as_float(p1[2]);
    const float one = (fr & 4) ? 1.0f : 0.0f;
    const f32x2 mr = rbf2(rbf2(f32x2{m0 + dpp_get<0x128>(m0), m1 + dpp_get<0x128>(m1)}) + one);
    m0 = mr[0]; m1 = mr[1];
    static_for<4>([&](auto E) {
      constexpr int e = E.value;
      sa[e] = f32x2{dpp_get<0x150 + e>(m0), dpp_get<0x150 + e>(m1)};
      ca[e] = f32x2{dpp_get<0x154 + e>(m0), dpp_get<0x154 + e>(m1)};
    });
  };
  auto frag = [&](const f32x2 (&xf)[4], const f32x2 r, const f32x2 (&sa)[4], const f32x2 (&ca)[4]) {
    u32x4 ho;
#pragma unroll
    for (int e = 0; e < 4; ++e) { const f32x2 h = (xf[e] * r[0] + r[1]) * ca[e] + sa[e]; ho[e] = pk_bf16(h[0], h[1]); }
    return ho;
  };
  f32x2 sa[4], ca[4];
  cols(pm[0], pm[1], sa, ca);
  const rsrc_t rsH = mk_rsrc(g.ws + WS_H + ((bm * 8 + f0) * 32 + kA) * 1024, 3 * 32768 + 1024);
  static_for<5>([&](auto I) {
    constexpr int i = (I.value + 1) % 5;
    const u32x4 ho = frag(fa[i], ra[i], sa, ca);
    if constexpr (i > 0) gstore4_sc1(ho, lane * 16, rsH, (i - 1) * 32768);
    dsw128<i * 1024>(i == 0 && f0 ? smem_base + QH_OFF + lane * 16 : la, ho);
  });
  cols(pm[2], pm[3], sa, ca);
  static_for<9>([&](auto I) { dsw128<I.value * 1024>(lb, frag(fb[I.value], rs[I.value], sa, ca)); });
  STAMP(0, 5);
  h_flag(g.ws, lane, wid, bm * 16 + bn, tag);
}

DI unsigned qkv_loader(char* smem, unsigned smem_base, int lane, int sw, int bm, int bn, int cn, const Args& g) {
  const int m0 = bm * 128, n0 = bn * 64;
  const unsigned u0 = ((bn ^ 3) * 2);
  const char* wc = g.ws + WS_WC;
  unsigned m0a = smem_base + sw * 3072, m0w = smem_base + 9216 + sw * 4096;
  auto wrow = [&](int i, unsigned sec) {
    const int r = i * 8 + (lane >> 3), ch = lane & 7;
    const int gg = ((r >> 1) & 1) | (((r >> 3) & 1) << 1);
    return (unsigned)((r * 3 + sw) * sec + ((((ch >> 1) ^ gg) * 16) + (ch & 1) * 8) * 2);
  };
  int fb0 = (m0 >> 4) - 1 + 3 * sw; fb0 = fb0 < 0 ? 0 : fb0;
  {
    const rsrc_t rsX = mk_rsrc(g.x, S_LEN * D_DIM * 2);
    const unsigned vx = (lane >> 2) * 2048 + ((lane & 3) ^ ((lane >> 3) & 2)) * 16;
#pragma unroll
    for (int i = 0; i < 18; ++i) {
      const int u = (i / 6) * 2 + (i & 1), r = (i >> 1) % 3;
      if (i >= TW) wait_vm<TW - 1>();
      bload_lds<0>(m0a + u * Q_SLOT + r * 1024, vx, rsX, (r ? (m0 >> 4) - 1 + 3 * sw + r : fb0) * 32768 + (u ^ u0) * 64);
    }
    const rsrc_t rsW0 = mk_rsrc((const char*)g.wq + n0 * 2, D_DIM * 3 * D_DIM * 2 - n0 * 2);
    const unsigned voffW0[2] = {wrow(0, 2048), wrow(1, 2048)};
#pragma unroll
    for (int u = 0; u < Q_NSL - 1; ++u)
#pragma unroll
      for (int i = 0; i < 4; ++i) {
        wait_vm<TW - 1>();
        if (u * 4 + i == TW - 1) asm volatile("ds_add_u32 %0, %1 offset:4" ::"v"(smem_base + QT_OFF), "v"(1u) : "memory");
        bload_lds<0>(m0w + u * Q_SLOT + i * 1024, voffW0[i & 1], rsW0, ((u ^ u0) * 2u + (i >> 1)) * 98304u);
      }
  }
  u32x4 oc[8];
  unsigned c2;
  front_ocld(lane, sw, bm, bn, g, oc, c2);
  const rsrc_t rsT = mk_rsrc((const char*)g.wq + (blockIdx.x * 3 + sw) * 8192, 8192);
    wait_vm<TT>();
  touch8k(smem_base + QH_OFF, lane * 128u, rsT, 0);
  const rsrc_t rsH = mk_rsrc(g.ws + WS_H, S_LEN * D_DIM * 2);
  const bool c3 = (bn & 7) == slow_col(g.wq);
  const rsrc_t rsW = mk_rsrc(c3 ? wc + (n0 >> 9) * 393216 : (const char*)g.wq + n0 * 2, c3 ? 393216 : D_DIM * 3 * D_DIM * 2 - n0 * 2);
  const unsigned sec = c3 ? 128u : 2048u, p16 = sec * 48u;
  unsigned voffH0 = (unsigned)(fb0 * 32768 + lane * 16);
  unsigned voffH1 = (unsigned)(((m0 >> 4) + 3 * sw) * 32768 + lane * 16);
  unsigned voffW[2] = {wrow(0, sec), wrow(1, sec)};
  asm volatile("" : "+v"(voffH0), "+v"(voffH1), "+v"(voffW[0]), "+v"(voffW[1]));
  asm volatile("" : "+s"(m0a), "+s"(m0w));
#define Q_LOADH(SOFF, U)                                                                                         \
  do {                                                                                                           \
    const unsigned so_ = ((unsigned)(U) ^ u0) * 1024u;                                                           \
    bload_lds_sc1<0>(m0a + (SOFF), voffH0, rsH, so_);                                                            \
    bload_lds_sc1<0>(m0a + (SOFF) + 1024, voffH1, rsH, so_);                                                     \
    bload_lds_sc1<0>(m0a + (SOFF) + 2048, voffH1, rsH, so_ + 32768u);                                            \
  } while (0)
#define Q_LOADW(SOFF, U)                                                                                         \
  do {                                                                                                           \
    _Pragma("unroll") for (int i = 0; i < 4; ++i)                                                                \
      bload_lds_sc1<0>(m0w + (SOFF) + i * 1024, voffW[i & 1], rsW, (((unsigned)(U) ^ u0) * 2u + (i >> 1)) * p16); \
  } while (0)
  front_oc<1>(smem, lane, sw, oc, c2);
  __builtin_amdgcn_s_barrier();
  Q_LOADW(6 * Q_SLOT, 6);
  __builtin_amdgcn_s_barrier();
  Q_LOADW(0, 7); Q_LOADW(Q_SLOT, 8);
  __builtin_amdgcn_s_barrier();
  Q_LOADH(6 * Q_SLOT, 6); Q_LOADH(0, 7); Q_LOADH(Q_SLOT, 8);
  Q_LOADH(2 * Q_SLOT, 9); Q_LOADW(2 * Q_SLOT, 9); Q_LOADH(3 * Q_SLOT, 10); Q_LOADW(3 * Q_SLOT, 10);
  wait_vm<17>();
  __builtin_amdgcn_s_barrier();
  unsigned sb = 4 * Q_SLOT;
  for (int u = 11; u < 31; u += 2) {
#pragma unroll
    for (int t = 0; t < 2; ++t) {
      Q_LOADH(sb, u + t);
      Q_LOADW(sb, u + t);
      sb = (sb == (Q_NSL - 1) * Q_SLOT) ? 0u : sb + Q_SLOT;
    }
    wait_vm<21>();
    __builtin_amdgcn_s_barrier();
  }
  Q_LOADH(3 * Q_SLOT, 31); Q_LOADW(3 * Q_SLOT, 31);
  const rsrc_t rsO = wo_rsrc(g.wo, wc, cn);
  const int sh = ((cn & 7) == slow_col(g.wo)) ? 7 : 11;
  const unsigned voffO[2] = {wo_voff(lane, sw & 1, sh), wo_voff(lane, (sw & 1) ^ 1, sh)};
  static_for<6>([&](auto U) {
    constexpr int u = U.value;
#pragma unroll
    for (int j = 0; j < 7; ++j) {
      const int pc = (u < 3 ? u * 21 : 64 + (u - 3) * 21) + 3 * j;
      const unsigned p = pc + sw;
      bload_lds_sc1<0>(smem_base + (u < 3 ? WO_H0 : -65536) + p * 1024u, voffO[pc & 1], rsO, ((p >> 2) * 32u + (p & 3) * 4u) << sh);
    }
    if constexpr (u == 0 || u == 2) wait_vm<21>();
    if constexpr (u == 0 || u == 2 || u >= 4) __builtin_amdgcn_s_barrier();
  });
  unsigned tag;
#pragma unroll
  for (int i = 0; i < 4; ++i) gstore4_sc1(u32x4{~0u, ~0u, ~0u, ~0u}, lane * 16, mk_rsrc(g.ws + WS_X + blockIdx.x * 16384 + sw * 4096, 4096), i * 1024);
  asm volatile("ds_read_b32 %0, %1 offset:512\n\ts_waitcnt vmcnt(%2) lgkmcnt(0)" : "=v"(tag) : "v"(smem_base + QH_OFF), "n"(4) : "memory");
  return __builtin_amdgcn_readfirstlane(tag);
}
namespace d4 {
struct Tile { int kh, bm, bn; };
DI Tile tile() {
  int b = blockIdx.x;
  asm volatile("" : "+s"(b));
  const int xcd = b & 7, idx = b >> 3;
  return Tile{xcd & 1, ((xcd >> 1) & 1) * 8 + (idx & 7), (xcd >> 2) + (idx >> 3) * 2};
}
}
namespace w7 {
constexpr int UC = 20, UD = 24;
constexpr int ops(int u) {
  int c = (u == 26) + (u == 32) + (u == DWO) + (u == UC);
  for (int j = 0; j < 2; ++j) c += (DW0 + (j * DWS) / 2 == u) + (DW0 + QSD + (j * DWS) / 2 == u);
  return c;
}
constexpr int after(int v, int u) { int c = 0; for (int t = v + 1; t < u; ++t) c += ops(t); return c > 63 ? 63 : c; }
}
DI unsigned qkv_spare(char* smem, unsigned smem_base, int lane, int bm, int bn, int cn, const Args& g) {
  using namespace w7;
  u32x4 cp[4], oc[8];
  unsigned tag, c2;
  const rsrc_t rsP = mk_rsrc((const char*)g.wo + (size_t)blockIdx.x * 8192, 8192);
  {
    const rsrc_t rsQ = mk_rsrc((const char*)g.wq + (size_t)blockIdx.x * 24576, 24576);
    const unsigned vc = ((lane >> 3) & 3) * 2048 + (lane >> 5) * 1024 + (lane & 7) * 16;
    asm volatile("" : "=v"(cp[0]), "=v"(cp[1]), "=v"(cp[2]), "=v"(cp[3]), "=v"(tag));
#pragma unroll
    for (int i = 0; i < 3; ++i) bload_v<0>(cp[i], vc, rsQ, i * 8192 + slow_col(g.wq) * 128);
    bload_v<0>(cp[3], vc, rsP, slow_col(g.wo) * 128);
    bload1_v<0>(tag, 0u, mk_rsrc(g.ws, 4), 0);
    {
      const unsigned o = lane < 16 ? WS_FY + blockIdx.x * 2048 + lane * 128 : (lane < 18 ? WS_FA + blockIdx.x * 256 + (lane - 16) * 128 : WS_FO + blockIdx.x * 64);
      asm volatile("s_mov_b32 m0, %0\n\ts_nop 0\n\tbuffer_load_dword %1, %2, 0 offen sc1 lds" ::"s"(smem_base + QH_OFF), "v"(lane > 18 ? 1u << 30 : o), "s"(mk_rsrc(g.ws, 2u << 20)) : "memory");
    }
    asm volatile("s_waitcnt vmcnt(%5)" : "+v"(cp[0]), "+v"(cp[1]), "+v"(cp[2]), "+v"(cp[3]), "+v"(tag) : "n"(1) : "memory");
    tag = __builtin_amdgcn_readfirstlane(tag) + 0x9E3779B9u;
    const rsrc_t rsC = mk_rsrc(g.ws + WS_WC, 1 << 20);
#pragma unroll
    for (int i = 0; i < 3; ++i) gstore4_sc1(cp[i], (lane >> 5) * 393216 + (lane & 31) * 16, rsC, (blockIdx.x * 3 + i) * 512);
    gstore4_sc1(cp[3], (lane >> 5) * 131072 + (lane & 31) * 16, rsC, 786432 + blockIdx.x * 512);
  }
  wait_vm<0>();
  asm volatile("buffer_store_dword %0, %1, %2, 0 offen sc1" ::"v"(tag), "v"(lane * 4), "s"(mk_rsrc(g.ws + WS_FC + blockIdx.x * 16, 4)) : "memory");
  STAMPT(1, 3, 448);
  front_ocld(lane, 3, bm, bn, g, oc, c2);
  front_oc<0>(smem, lane, 3, oc, c2);
  STAMPT(1, 4, 448);
  if (((bn & 7) == slow_col(g.wq)) || ((cn & 7) == slow_col(g.wo))) {
    const rsrc_t rsF = mk_rsrc(g.ws + WS_FC, 4096);
    unsigned spins = 0, f0, f1, f2, f3;
    do {
      if (spins) __builtin_amdgcn_s_sleep(8);
      asm volatile("buffer_load_dword %0, %4, %5, 0 offen sc1\n\tbuffer_load_dword %1, %4, %5, 0 offen offset:1024 sc1\n\t"
                   "buffer_load_dword %2, %4, %5, 0 offen offset:2048 sc1\n\tbuffer_load_dword %3, %4, %5, 0 offen offset:3072 sc1\n\ts_waitcnt vmcnt(0)"
                   : "=&v"(f0), "=&v"(f1), "=&v"(f2), "=&v"(f3) : "v"(lane * 16), "s"(rsF) : "memory");
    } while (!__all(((f0 ^ tag) | (f1 ^ tag) | (f2 ^ tag) | (f3 ^ tag)) == 0u) && ++spins < (1u << 16));
  }
  STAMPT(1, 0, 448);
  const rsrc_t rsX = mk_rsrc(g.ws + WS_G2 + (bn ^ 2) * 512, 131072);
  const rsrc_t rsO = wo_rsrc(g.wo, g.ws + WS_WC, cn);
  const int sh = ((cn & 7) == slow_col(g.wo)) ? 7 : 11;
  const unsigned voffO = wo_voff(lane, 1, sh);
  __builtin_amdgcn_s_barrier();
  const rsrc_t rsG = mk_rsrc((const char*)g.wg + (size_t)blockIdx.x * (TQ * 1024), TQ * 1024);
  const rsrc_t rsU = mk_rsrc((const char*)g.wu + (size_t)blockIdx.x * (TQ * 1024), TQ * 1024);
  static_for<33>([&](auto U) {
    constexpr int u = U.value;
    (void)&rsG; (void)&rsU; (void)&rsP; (void)&rsX; (void)&oc;
    if constexpr (u == 0) {
      dsw32<512>(smem_base + QH_OFF, tag);
      const float* rp = reinterpret_cast<const float*>(smem + QX_OFF + 1024) + lane;
      gstore2_sc1(u32x2{__float_as_uint((rp[0] + rp[64]) + (rp[128] + rp[192])), tag}, bm * 8192 + lane * 8, rsX);
    }
    if constexpr (u == CK) {
      const rsrc_t rsF = mk_rsrc(g.ws + WS_FH + (blockIdx.x & 7) * 32768 + (bm == 0 ? 0 : (bm - 1) * 2048), 4096);
      unsigned spins = 0, f0, f1;
      do {
        asm volatile("buffer_load_dword %0, %2, %3, 0 offen sc1\n\tbuffer_load_dword %1, %2, %3, %4 offen sc1\n\ts_waitcnt vmcnt(0)"
                     : "=&v"(f0), "=&v"(f1) : "v"((lane >> 2) * 128 + (lane & 3) * 4), "s"(rsF), "s"(bm ? 2048 : 0) : "memory");
      } while (!__all(f0 == tag && f1 == tag) && ++spins < (1u << 18));
      STAMPT(1, 2, 448);
    }
    if constexpr (u == UD) wait_vm<after(UC, u)>();
    if constexpr (u == 26) bload_lds_sc1<0>(smem_base + WO_H0 + 63 * 1024, voffO, rsO, (15 * 32u + 12u) << sh);
    if constexpr (u == 32) bload_lds_sc1<0>(smem_base + 63 * 1024, voffO, rsO, (31 * 32u + 12u) << sh);
    const unsigned par = ((blockIdx.x >> 3) & 1) << 30;
    if constexpr (u == DWO) touch8k(smem_base + QH_OFF, lane * 128u, rsP, 0);
#pragma unroll
    for (int j = 0; j < 2; ++j) {
      if (DW0 + QSD + (j * DWS) / 2 == u) touch8k(smem_base + QH_OFF, lane * 128u | (par ^ (1u << 30)), j ? rsU : rsG, 0);
      if (DW0 + (j * DWS) / 2 == u) touch8k(smem_base + QH_OFF, lane * 128u | par, j ? rsU : rsG, 0);
    }
    if constexpr (u == UC) gstore4_sc1(u32x4{~0u, ~0u, ~0u, ~0u}, lane * 16, mk_rsrc(g.ws + WS_STAT + (bm * 16 + bn) * 1024, 1024), 0);
    if constexpr ((u & 1) || u == 32) __builtin_amdgcn_s_barrier();
  });
  {
    const rsrc_t rsS = mk_rsrc(g.ws + WS_G2, 131072);
    const unsigned vc = cn * 512 + (lane >> 5) * 32768 + (lane & 31) * 16, vd = 98304 + d4::tile().bn * 1024 + lane * 16;
    u32x4 pm[12];
    unsigned spins = 0;
    for (;;) {
      asm volatile("buffer_load_dwordx4 %0, %12, %14, 0 offen sc1\n\tbuffer_load_dwordx4 %1, %12, %14, %15 offen sc1\n\t"
                   "buffer_load_dwordx4 %2, %12, %14, %16 offen sc1\n\tbuffer_load_dwordx4 %3, %12, %14, %17 offen sc1\n\t"
                   "buffer_load_dwordx4 %4, %12, %14, %18 offen sc1\n\tbuffer_load_dwordx4 %5, %12, %14, %19 offen sc1\n\t"
                   "buffer_load_dwordx4 %6, %12, %14, %20 offen sc1\n\tbuffer_load_dwordx4 %7, %12, %14, %21 offen sc1\n\t"
                   "buffer_load_dwordx4 %8, %13, %14, 0 offen sc1\n\tbuffer_load_dwordx4 %9, %13, %14, %15 offen sc1\n\t"
                   "buffer_load_dwordx4 %10, %13, %14, %16 offen sc1\n\tbuffer_load_dwordx4 %11, %13, %14, %17 offen sc1\n\ts_waitcnt vmcnt(0)"
                   : "=&v"(pm[0]), "=&v"(pm[1]), "=&v"(pm[2]), "=&v"(pm[3]), "=&v"(pm[4]), "=&v"(pm[5]), "=&v"(pm[6]), "=&v"(pm[7]),
                     "=&v"(pm[8]), "=&v"(pm[9]), "=&v"(pm[10]), "=&v"(pm[11])
                   : "v"(vc), "v"(vd), "s"(rsS), "s"(8192), "s"(16384), "s"(24576), "s"(65536), "s"(73728), "s"(81920), "s"(90112) : "memory");
      unsigned bad = 0;
#pragma unroll
      for (int i = 0; i < 12; ++i) bad |= (pm[i][1] ^ tag) | (pm[i][3] ^ tag);
      if (__all(bad == 0u) || ++spins > (1u << 16)) break;
    }
#pragma unroll
    for (int i = 0; i < 4; ++i) gstore4_sc1(u32x4{~0u, ~0u, ~0u, ~0u}, lane * 16, mk_rsrc(g.ws + WS_X + blockIdx.x * 16384 + 12288, 4096), i * 1024);
    f32x2 m[3];
#pragma unroll
    for (int v = 0; v < 3; ++v)
#pragma unroll
      for (int e = 0; e < 2; ++e)
        m[v][e] = (__uint_as_float(pm[v * 4][2 * e]) + __uint_as_float(pm[v * 4 + 1][2 * e])) + (__uint_as_float(pm[v * 4 + 2][2 * e]) + __uint_as_float(pm[v * 4 + 3][2 * e]));
    f32x2* tp = reinterpret_cast<f32x2*>(smem + QT_OFF);
    tp[(lane >> 5) * 64 + (lane & 31)] = rbf2(m[0]);
    if (lane < 32) tp[32 + lane] = rbf2(rbf2(m[1]) + 1.0f);
    tp[128 + lane] = rbf2(m[2]);
  }
  asm volatile("s_waitcnt lgkmcnt(0)" ::: "memory");
  return tag;
}

DI void qkv_mfma(char* smem, unsigned smem_base, int tid, int wid, int m0, int n0, bf16* O, unsigned* oflags, unsigned tag) {
  const int lane = tid & 63;
  const int wm = wid & 1, wn = wid >> 1;
  const int fr = lane & 15, fq = lane >> 4;
  unsigned rad[3][4];
  {
    const int q = (lane >> 2) & 3, p = lane & 3;
    const int r = 8 * fq + q;
    const int g = ((r >> 1) & 1) | (((r >> 3) & 1) << 1);
#pragma unroll
    for (int i = 0; i < 3; ++i) {
      rad[i][3] = smem_base + i * (3 * Q_SLOT) + lane * 16; rad[i][2] = rad[i][3] + 1024 + wm * 4096;
#pragma unroll
      for (int jj = 0; jj < 2; ++jj) rad[i][jj] = smem_base + i * (3 * Q_SLOT) + 9216 + r * 128 + (((wn * 2 + (jj ^ wm)) ^ g) * 32) + 8 * p;
      asm volatile("" : "+v"(rad[i][0]), "+v"(rad[i][1]), "+v"(rad[i][2]), "+v"(rad[i][3]));
    }
  }
  f32x4 acc[4][6], acch[2];
#pragma unroll
  for (int i = 0; i < 4; ++i)
#pragma unroll
    for (int j = 0; j < 6; ++j) { acc[i][j] = f32x4{0, 0, 0, 0}; asm volatile("" : "+v"(acc[i][j])); }
#pragma unroll
  for (int j = 0; j < 2; ++j) { acch[j] = f32x4{0, 0, 0, 0}; asm volatile("" : "+v"(acch[j])); }
  asm volatile("s_waitcnt lgkmcnt(0)" ::: "memory");
  __builtin_amdgcn_s_barrier();
  STAMPDECL;
  STAMPNW(2, 1);
  u32x4 a[2][5];
  u32x2 bl[2][6], bh[2][6];
  auto rdb = [&](auto PP, auto GG, auto RR) {
    constexpr int jj = RR.value >> 2, j = GG.value * 2 + jj, k = (RR.value >> 1) & 1, sl = (PP.value * 2 + k) % Q_NSL;
    constexpr int off = (sl % 3) * Q_SLOT + GG.value * 4096 + (RR.value & 1) * 512;
    if constexpr (RR.value & 1) dsr_tr<off>(bh[k][j], rad[sl / 3][jj]);
    else dsr_tr<off>(bl[k][j], rad[sl / 3][jj]);
  };
  auto gap = [&](auto PP, auto NN) {
    constexpr int n = NN.value, k = ~n & 1, sl = (PP.value * 2 + k) % Q_NSL;
    if constexpr (n < QY + 8) rdb(PP, ic<0>{}, ic<(n - QY) & 7>{});
    else if constexpr (n == 34 || n == 35) dsr128<(sl % 3) * Q_SLOT>(a[k][4], rad[sl / 3][3]);
    else if constexpr (n >= 36 && (n & 2)) dsr128<(sl % 3) * Q_SLOT + ((n - 36) >> 2) * 1024>(a[k][((n - 36) >> 2) & 3], rad[sl / 3][2]);
    else if constexpr (n >= 36) rdb(PP, ic<1>{}, ic<((n - 36) >> 2) * 2 + (n & 1)>{});
  };
  static_for<52 - QY>([&](auto N) { gap(ic<0>{}, ic<N.value + QY>{}); });
  static_for<16>([&](auto PP) {
    static_for<52>([&](auto N) {
      constexpr int n = N.value, m = n < 36 ? n : n - 4, k = (n ^ (n >> 1)) & 1;
      constexpr int i = (m & 16) ? 3 - ((m >> 2) & 3) : (m >> 2) & 3, j = (m >> 4) * 2 + (((m + 2) >> 2) & 1);
      (void)&acc; (void)&acch; (void)&a; (void)&bl; (void)&bh;
      if constexpr (n < 16 && n % 4 == 0) asm volatile("s_waitcnt lgkmcnt(%0)" ::"n"(n < 12 ? 12 : 8) : "memory");
      if constexpr (n == QY) {
        asm volatile("s_waitcnt lgkmcnt(0)" ::: "memory");
        __builtin_amdgcn_s_barrier();
      }
      constexpr int jb = n < 32 || n >= 36 ? j : (n < 34 ? 2 : 4);
      const u32x4 b = {bl[k][jb][0], bl[k][jb][1], bh[k][jb][0], bh[k][jb][1]};
      if constexpr (n < 32) MFMA_AV(acc[i][j], b, a[k][i]);
      else if constexpr (n < 34) MFMA_AV(acch[0], b, a[k][4]);
      else if constexpr (n < 36) MFMA_AV(acch[1], a[k][4], b);
      else MFMA_AV(acc[i][j], a[k][i], b);
      if constexpr (n < 8) rdb(PP, ic<2>{}, N);
      if constexpr (n >= QY && PP.value < 15) gap(ic<PP.value + 1>{}, N);
    });
  });
  asm volatile("s_waitcnt vmcnt(0) lgkmcnt(0)\n\ts_nop 15" ::: "memory");
  SCHED0();
  __builtin_amdgcn_s_setprio(0);
  STAMP(2, 2); STAMPFLUSH(2, 1);
#pragma unroll
  for (int i = 0; i < 4; ++i)
#pragma unroll
    for (int j = 0; j < 6; ++j) asm volatile("" : "+v"(acc[i][j]));
  asm volatile("" : "+v"(acch[0]), "+v"(acch[1]));
  auto pk2 = [](const f32x4 v) { return u32x2{pk_bf16(v[0], v[1]), pk_bf16(v[2], v[3])}; };
  auto cat = [](const u32x2 a, const u32x2 b) { return __builtin_bit_cast(bf16x8, u32x4{a[0], a[1], b[0], b[1]}); };
  u32x2 kp[5][2], vp[5][2];
  {
    char* xb = smem + QX_OFF + wn * 4096 + lane * 8;
#pragma unroll
    for (int f = 0; f < 2; ++f) { kp[4][f] = pk2(acc[3][2 + f]); vp[4][f] = pk2(acc[3][4 + f]); }
    *reinterpret_cast<u32x2*>(xb + wm * 512) = pk2(acch[0]);
    *reinterpret_cast<u32x2*>(xb + 1024 + wm * 512) = pk2(acch[1]);
    if (wm == 0) {
#pragma unroll
      for (int f = 0; f < 2; ++f) {
        *reinterpret_cast<u32x2*>(xb + 2048 + f * 512) = kp[4][f];
        *reinterpret_cast<u32x2*>(xb + 2048 + 1024 + f * 512) = vp[4][f];
      }
    }
    asm volatile("s_waitcnt lgkmcnt(0)" ::: "memory");
    __builtin_amdgcn_s_barrier();
    asm volatile("" ::: "memory");
#pragma unroll
    for (int f = 0; f < 2; ++f) {
      kp[0][f] = *reinterpret_cast<const u32x2*>(xb + wm * 2048 + (f ^ wm) * 512);
      vp[0][f] = *reinterpret_cast<const u32x2*>(xb + wm * 2048 + 1024 + (f ^ wm) * 512);
    }
  }
  STAMP(2, 3);
#pragma unroll
  for (int i = 0; i < 3; ++i)
#pragma unroll
    for (int f = 0; f < 2; ++f) { kp[1 + i][f] = pk2(acc[i][2 + f]); vp[1 + i][f] = pk2(acc[i][4 + f]); }
  const bool par = fr & 1;
  const bool cura = fq * 4 + (fr & 1) <= fr, curb = fq * 4 + 2 + (fr & 1) <= fr;
  const unsigned mh = par ? 0xffff0000u : 0x0000ffffu;
  const unsigned mca = cura ? mh : 0u, mpa = cura ? 0u : mh, mcb = curb ? mh : 0u, mpb = curb ? 0u : mh;
  const float cs = 0.17677669529663687f * 1.4426950408889634f;
  char* op = (char*)O + ((size_t)((m0 >> 6) + wm) * (D_DIM / 32) + (n0 >> 5) + wn) * 4096 + lane * 16;
  const f32x4 z4 = {0, 0, 0, 0};
#pragma unroll
  for (int ii = 0; ii < 4; ++ii) {
    const int i = 3 - ii;
    const bf16x8 qf = cat(pk2(acc[i][0]), pk2(acc[i][1]));
    const f32x4 sc = __builtin_amdgcn_mfma_f32_16x16x32_bf16(cat(kp[i + 1][0], kp[i + 1][1]), qf, z4, 0, 0, 0);
    f32x4 sp = __builtin_amdgcn_mfma_f32_16x16x32_bf16(cat(kp[i][0], kp[i][1]), qf, z4, 0, 0, 0);
    if (i == 0 && m0 == 0 && wm == 0) sp = f32x4{-5.656854249e9f, -5.656854249e9f, -5.656854249e9f, -5.656854249e9f};
    const float la = cura ? (par ? sc[1] : sc[0]) : (par ? sp[1] : sp[0]);
    const float lb = curb ? (par ? sc[3] : sc[2]) : (par ? sp[3] : sp[2]);
    float mx = fmaxf(la, lb);
    {
      const auto a = __builtin_amdgcn_permlane16_swap(__float_as_uint(mx), __float_as_uint(mx), false, false);
      mx = fmaxf(__uint_as_float(a[0]), __uint_as_float(a[1]));
      const auto b = __builtin_amdgcn_permlane32_swap(__float_as_uint(mx), __float_as_uint(mx), false, false);
      mx = fmaxf(__uint_as_float(b[0]), __uint_as_float(b[1]));
    }
    const float ea = __builtin_amdgcn_exp2f((la - mx) * cs), eb = __builtin_amdgcn_exp2f((lb - mx) * cs);
    const float inv = __builtin_amdgcn_rcpf(rowsum4(ea + eb));
    const unsigned wa = pk_bf16(ea, ea), wb = pk_bf16(eb, eb);
    const bf16x8 pf = __builtin_bit_cast(bf16x8, u32x4{wa & mpa, wb & mpb, wa & mca, wb & mcb});
    const f32x4 o0 = __builtin_amdgcn_mfma_f32_16x16x32_bf16(cat(vp[i][0], vp[i + 1][0]), pf, z4, 0, 0, 0) * inv;
    const f32x4 o1 = __builtin_amdgcn_mfma_f32_16x16x32_bf16(cat(vp[i][1], vp[i + 1][1]), pf, z4, 0, 0, 0) * inv;
    const u32x2 u0 = pk2(o0), u1 = pk2(o1);
    gst128(op + i * 1024, wm ? u32x4{u1[0], u1[1], u0[0], u0[1]} : u32x4{u0[0], u0[1], u1[0], u1[1]});
  }
  STAMP(2, 4);
  wait_vm<0>();
  asm volatile("buffer_store_dword %0, %1, %2, 0 offen sc1" ::"v"(tag), "v"(lane * (4096 * OFS)), "s"(mk_rsrc(oflags + ((m0 >> 1) + wm * 32 + (n0 >> 5) + wn) * OFS, (ORP - 1) * 4096 * OFS + 4)) : "memory");
  STAMP(2, 5);
}

DI void x2_store(const u32x4 (&x2p)[2], const char* ws, int lane, int w, int own) {
  const rsrc_t rsR = mk_rsrc(ws + WS_X2 + (((own >> 4) * 8 + (w & 3) * 2 + (w >> 2)) * 16 + (own & 15)) * 2048, 2048);
  gstore4_sc1(x2p[0], lane * 16, rsR, 0); gstore4_sc1(x2p[1], lane * 16, rsR, 1024);
}
DI void o_phase(char* smem, unsigned smem_base, int lane, int w, int cm, int cn, const bf16* O, const unsigned* oflags,
                const bf16* __restrict__ x, bf16* Y, float* stat, const char* ws, unsigned tag) {
  const int wm = w & 3, kh = w >> 2;
  const int fr = lane & 15, fq = lane >> 4;
  const int R = cm * 8 + wm * 2 + kh;
  u32x2 xv[4];
  {
    const rsrc_t rsX = mk_rsrc(x, S_LEN * D_DIM * 2);
    const unsigned vx = (unsigned)(((R * 16 + fr) * D_DIM + cn * 64 + fq * 4) * 2);
#pragma unroll
    for (int f = 0; f < 4; ++f) asm volatile("" : "=v"(xv[f]));
    bload2_v<0>(xv[0], vx, rsX, 0); bload2_v<32>(xv[1], vx, rsX, 0); bload2_v<64>(xv[2], vx, rsX, 0); bload2_v<96>(xv[3], vx, rsX, 0);
  }
  const unsigned hb = smem_base + (kh ? 0 : WO_H0);
  unsigned brd[4];
  {
    const int q = (lane >> 2) & 3, p = lane & 3;
    const int r = 8 * fq + q;
    const int g = ((r >> 1) & 1) | (((r >> 3) & 1) << 1);
#pragma unroll
    for (int f = 0; f < 4; ++f) brd[f] = hb + r * 128 + ((f ^ g) * 32) + 8 * p;
  }
  const rsrc_t rsA = mk_rsrc((const char*)O + (size_t)(cm * 2 + (wm >> 1)) * 131072 + kh * 65536, 65536);
  unsigned va0 = lane * 16 + ((wm & 1) * 2 + kh) * 1024, va1 = va0 ^ 1024;
  unsigned tsrc = smem_base + QT_OFF + fq * 16, xw = hb + lane * 16 + wm * 4096, xr = smem_base + (kh ? WO_H0 : 0) + lane * 16 + wm * 4096;
  char* yb = (char*)Y + ((size_t)(R >> 4) * 32 + cn * 2 + (fq & 1)) * 16384 + (R & 15) * 1024 + ((fq >> 1) * 16 + fr) * 16;
  asm volatile("" : "+v"(tsrc), "+v"(xw), "+v"(xr), "+v"(yb), "+v"(va0), "+v"(va1), "+v"(brd[0]), "+v"(brd[1]), "+v"(brd[2]), "+v"(brd[3]));
  f32x4 acc[2][4];
#pragma unroll
  for (int i = 0; i < 2; ++i)
#pragma unroll
    for (int f = 0; f < 4; ++f) { acc[i][f] = f32x4{0, 0, 0, 0}; asm volatile("" : "+v"(acc[i][f])); }
  if (w == POLLW) {
    const rsrc_t rsF = mk_rsrc(oflags + ((blockIdx.x & (ORP - 1)) * 1024 + cm * 64) * OFS, 256 * OFS);
    unsigned spins = 0, f;
    do {
      asm volatile("buffer_load_dword %0, %1, %2, 0 offen sc1\n\ts_waitcnt vmcnt(0)" : "=v"(f) : "v"(lane * (4 * OFS)), "s"(rsF) : "memory");
    } while (!__all(f == tag) && ++spins < (1u << 18));
  }
  __builtin_amdgcn_s_barrier();
  STAMPDECL;
  STAMPNW(3, 1);
  u32x4 a[16][2], gt[4], ct[4], st[4];
  u32x2 bl[2][4], bh[2][4];
  auto rdb = [&](auto S) {
    constexpr int s = S.value;
#pragma unroll
    for (int f = 0; f < 4; ++f) { dsr_tr<s * 4096>(bl[s & 1][f], brd[f]); dsr_tr<s * 4096 + 512>(bh[s & 1][f], brd[f]); }
  };
  auto lda = [&](auto S) {
    constexpr int s = S.value;
    asm volatile("" : "=v"(a[s][0]), "=v"(a[s][1]));
    bload_sc1<0>(a[s][0], va0, rsA, s * 4096);
    bload_sc1<0>(a[s][1], va1, rsA, s * 4096);
  };
  static_for<OD>([&](auto S) {
    lda(S);
    if constexpr (S.value == 0) rdb(ic<0>{});
    if constexpr (S.value == 1) rdb(ic<1>{});
  });
  static_for<16>([&](auto S) {
    constexpr int s = S.value;
    asm volatile("s_waitcnt vmcnt(%0) lgkmcnt(8)" ::"n"(2 * ((s + OD - 1 < 15 ? s + OD - 1 : 15) - s)) : "memory");
    SCHED0();
    u32x4 bb[4];
#pragma unroll
    for (int f = 0; f < 4; ++f) bb[f] = u32x4{bl[s & 1][f][0], bl[s & 1][f][1], bh[s & 1][f][0], bh[s & 1][f][1]};
    SCHED0();
    if constexpr (s + OD < 16) lda(ic<(s + OD < 16 ? s + OD : 0)>{});
    if constexpr (s == OMB) __builtin_amdgcn_s_barrier();
#pragma unroll
    for (int n = 0; n < 8; ++n) MFMA_AV(acc[1 - (n >> 2)][n & 3], bb[n & 3], a[s][1 - (n >> 2)]);
    if constexpr (s < 14) rdb(ic<(s < 14 ? s + 2 : 0)>{});
    if constexpr (s == 14) {
      static_for<4>([&](auto F) { dsr128<F.value * 64>(gt[F.value], tsrc); dsr128<256 + F.value * 64>(ct[F.value], tsrc); });
    }
    SCHED0();
  });
  STAMP(3, 2); STAMPFLUSH(3, 1);
  asm volatile("s_nop 7" ::: "memory");
  static_for<4>([&](auto F) { dsw128<F.value * 1024>(xw, __builtin_bit_cast(u32x4, acc[1][F.value])); });
  asm volatile("s_waitcnt lgkmcnt(0)" ::: "memory");
  __builtin_amdgcn_s_barrier();
  u32x4 pr[4];
  static_for<4>([&](auto F) { dsr128<F.value * 1024>(pr[F.value], xr); });
  static_for<4>([&](auto F) { dsr128<512 + F.value * 64>(st[F.value], tsrc); });
#pragma unroll
  for (int f = 0; f < 4; ++f) asm volatile("" : "+v"(acc[0][f]), "+v"(xv[f]), "+v"(gt[f]), "+v"(ct[f]));
  asm volatile("s_waitcnt lgkmcnt(4)" : "+v"(pr[0]), "+v"(pr[1]), "+v"(pr[2]), "+v"(pr[3])::"memory");
  f32x4 fin[4];
#pragma unroll
  for (int f = 0; f < 4; ++f) {
    const f32x4 xf = {bf_lo(xv[f][0]), bf_hi(xv[f][0]), bf_lo(xv[f][1]), bf_hi(xv[f][1])};
    fin[f] = xf + __builtin_bit_cast(f32x4, gt[f]) * (acc[0][f] + __builtin_bit_cast(f32x4, pr[f]));
  }
  const rsrc_t rsS = mk_rsrc((char*)stat + R * 2048, 2048);
  {
    const f32x4 s4 = (fin[0] + fin[1]) + (fin[2] + fin[3]);
    const float sm = rowsum4((s4[0] + s4[1]) + (s4[2] + s4[3]));
    const float mb = sm * (1.0f / 64.0f);
    f32x4 q4 = {0, 0, 0, 0};
#pragma unroll
    for (int f = 0; f < 4; ++f) { const f32x4 d = fin[f] - mb; q4 += d * d; }
    const float m2 = rowsum4((q4[0] + q4[1]) + (q4[2] + q4[3]));
    gstore2_sc1(u32x2{min(__float_as_uint(sm), ~1u), min(__float_as_uint(m2), ~1u)}, lane < 16 ? cn * 128 + lane * 8 : 1u << 30, rsS);
  }
  STAMP(3, 3);
  u32x4 x2p[2];
#pragma unroll
  for (int j = 0; j < 2; ++j) {
    x2p[j] = u32x4{pk_bf16(fin[2 * j][0], fin[2 * j][1]), pk_bf16(fin[2 * j][2], fin[2 * j][3]), pk_bf16(fin[2 * j + 1][0], fin[2 * j + 1][1]), pk_bf16(fin[2 * j + 1][2], fin[2 * j + 1][3])};
    asm volatile("" : "+v"(x2p[j]));
  }
  x2_store(x2p, ws, lane, w, cm * 16 + cn);
  u32x2 tv[4];
  {
    if (SSL) __builtin_amdgcn_s_sleep(SSL);
    unsigned spins = 0;
    for (;;) {
      asm volatile("buffer_load_dwordx2 %0, %4, %5, 0 offen sc1\n\tbuffer_load_dwordx2 %1, %4, %5, 0 offen offset:512 sc1\n\t"
                   "buffer_load_dwordx2 %2, %4, %5, 0 offen offset:1024 sc1\n\tbuffer_load_dwordx2 %3, %4, %5, 0 offen offset:1536 sc1\n\ts_waitcnt vmcnt(0) lgkmcnt(0)"
                   : "=&v"(tv[0]), "=&v"(tv[1]), "=&v"(tv[2]), "=&v"(tv[3]) : "v"(lane * 8), "s"(rsS) : "memory");
      const unsigned mx = max(max(max(tv[0][0], tv[0][1]), max(tv[1][0], tv[1][1])), max(max(tv[2][0], tv[2][1]), max(tv[3][0], tv[3][1])));
      if (!__any(mx == ~0u) || ++spins > (1u << 18)) break;
      if (SSI) __builtin_amdgcn_s_sleep(SSI);
    }
  }
  STAMP(3, 6);
  const float mean = rowsum4((__uint_as_float(tv[0][0]) + __uint_as_float(tv[1][0])) + (__uint_as_float(tv[2][0]) + __uint_as_float(tv[3][0]))) * (1.0f / D_DIM);
  float m2 = 0;
#pragma unroll
  for (int i = 0; i < 4; ++i) { const float d = __uint_as_float(tv[i][0]) * (1.0f / 64.0f) - mean; m2 += __uint_as_float(tv[i][1]) + 64.0f * d * d; }
  const float rstd = rsqrtf(rowsum4(m2) * (1.0f / D_DIM) + 1e-5f), rm = -mean * rstd;
  u32x2 pk[4];
#pragma unroll
  for (int f = 0; f < 4; ++f) {
    asm volatile("" : "+v"(st[f]));
    const f32x4 yv = (fin[f] * rstd + rm) * __builtin_bit_cast(f32x4, ct[f]) + __builtin_bit_cast(f32x4, st[f]);
    pk[f] = u32x2{pk_bf16(yv[0], yv[1]), pk_bf16(yv[2], yv[3])};
  }
#pragma unroll
  for (int j = 0; j < 2; ++j) {
    const auto s0 = __builtin_amdgcn_permlane16_swap(pk[j][0], pk[j + 2][0], false, false);
    const auto s1 = __builtin_amdgcn_permlane16_swap(pk[j][1], pk[j + 2][1], false, false);
    gst128(yb + j * 512, u32x4{s0[0], s1[0], s0[1], s1[1]});
  }
}

#define EPR 3
#define TPOS 62
#define GLX 12
#define GLP 0
#define GD4 4
#define GD3 3
struct Stg { rsrc_t rA, rB; unsigned vA, vB, sA, sB, dA, dB, e1, e3; };
#define G3_STAGE(SLOT, P)                                                                                  \
  do {                                                                                                     \
    if ((P) == 0) bload_lds_a<0>(m0w + (SLOT) * 32768, st.vA, st.rA, st.sA);                             \
    else if ((P) == 1) { bload_lds_a<1024>(m0w + (SLOT) * 32768, st.vA, st.rA, st.sA + st.e1); st.sA += st.dA; } \
    else if ((P) == 2) bload_lds_b<0>(m0w + (SLOT) * 32768 + 16384, st.vB, st.rB, st.sB);                    \
    else { bload_lds_b<0>(m0w + (SLOT) * 32768 + 16384 + 1024, st.vB, st.rB, st.sB + st.e3); st.sB += st.dB; } \
  } while (0)

#define DNF 24
namespace d4 {
constexpr int NF = DNF, NH = 56 - 2 * NF, N = NF + NH, NE = 12, FR = 32768;
constexpr int eg(int s) { return (s / 6) * 2 + (s & 1); }
constexpr int ef(int s) { return (s % 6) >> 1; }
constexpr int apos(int s) { return s < NE ? eg(s) * 3 + ef(s) : s + NE; }
constexpr int wpos(int s, int k) { return s < NE ? (eg(s) * 4 + k) * 7 + 4 + ef(s) : (s - NE) * 7 + k; }
struct WSrc { rsrc_t rs; unsigned v, f[3]; };
DI WSrc wsrc(const bf16* Wd, int j) {
  const Tile t = tile();
  const int lane = lane_id(), grp = t.bn * 2 + (j >> 1), ch = lane & 7, gg = ((lane >> 4) & 1) | ((j & 1) << 1);
  WSrc w;
  w.rs = mk_rsrc((const char*)Wd + grp * 128, F_DIM * D_DIM * 2 - grp * 128);
  w.v = (unsigned)(((j & 1) * 8 + (lane >> 3)) * 2048 + ((ch >> 1) ^ gg) * 32 + (ch & 1) * 16);
  w.f[0] = t.kh * (112 * FR); w.f[1] = t.kh * ((44 - NF) * 7 * FR); w.f[2] = 0;
  asm volatile("" : "+v"(w.v), "+s"(w.f[0]), "+s"(w.f[1]), "+s"(w.f[2]));
  return w;
}
constexpr int cls(int s) { return s < NE ? 0 : (s < NF ? 1 : 2); }
}

template <int FN>
DI void g3_body(char* smem, unsigned smem_base, int wm, int wn, int wid, int lane, int bm, int bn, Stg st,
                rsrc_t rsW, const bf16* Wd, char* ws) {
  const unsigned tmask = (wm < 2 ? 0x1111111u : 0x1100000u) << ((blockIdx.x >> 3) & 1);
  constexpr int NB = 2 * FN, NM = 8 * NB;
  const int fr = lane & 15, fq = lane >> 4;
  f32x4 acc[4][NB];
#pragma unroll
  for (int i = 0; i < 4; ++i)
#pragma unroll
    for (int j = 0; j < NB; ++j) { acc[i][j] = f32x4{0, 0, 0, 0}; asm volatile("" : "+v"(acc[i][j])); }
  unsigned aad[2], bad[2][FN];
  aad[0] = smem_base + wm * 4096 + lane * 16; aad[1] = aad[0] + 65536;
  {
    const int q = (lane >> 2) & 3, p = lane & 3;
    const int r = 8 * fq + q;
    const int sw = (r & 3) | (((r >> 3) & 1) << 2);
#pragma unroll
    for (int j = 0; j < FN; ++j) {
      const int gi = wn * 4 + j + (bn & 1);
      bad[0][j] = smem_base + 16384 + r * 256 + ((gi ^ sw) * 32) + 8 * p;
      bad[1][j] = bad[0][j] + 65536;
    }
  }
  u32x4 a[2][4];
  u32x2 bl[2][NB], bh[2][NB];
  const unsigned m0w = smem_base + wid * 2048;
  constexpr int GDS = FN == 4 ? GD4 : GD3;
  constexpr int L = NM - 16;
  auto rdb = [&](auto HH, auto GG, auto RR) {
    constexpr int j = GG.value * 2 + (RR.value >> 2), k = (RR.value >> 1) & 1, off = k * 32768 + (j / FN) * 8192 + (RR.value & 1) * 1024;
    if constexpr (RR.value & 1) dsr_tr<off>(bh[k][j], bad[HH.value][j % FN]);
    else dsr_tr<off>(bl[k][j], bad[HH.value][j % FN]);
  };
  auto row = [](int h, int n) constexpr { return ((n >> 4) ^ (FN == 4 ? h : 0)) & 1 ? 3 - ((n >> 2) & 3) : (n >> 2) & 3; };
  auto tail = [&](auto HH, auto NN) {
    constexpr int h = HH.value, n = NN.value;
    if constexpr (n & 2) dsr128<(~n & 1) * 32768 + row(h, n) * 1024>(a[~n & 1][row(h, n)], aad[h]);
    else rdb(HH, ic<FN - 2>{}, ic<(n >> 2) * 2 + (n & 1)>{});
  };
  unsigned vt = (lane & 31) * 128u + (lane >> 5) * 0x40000000u;
  const rsrc_t rsD = mk_rsrc((const char*)Wd + blockIdx.x * 28672, 28672);
  auto period = [&](auto HH, auto LAST, int u) {
    constexpr int h = HH.value;
    constexpr bool last = LAST.value && h == 1;
    constexpr int X = last ? GLX : 16;
    static_for<NM>([&](auto N) {
      constexpr int n = N.value, p = n >> 1, k = (n ^ p) & 1, i = row(h, n), j = (n >> 4) * 2 + (((p + 1) >> 1) & 1);
      (void)&acc; (void)&a; (void)&bl; (void)&bh; (void)&st; (void)&rsW; (void)&rsD; (void)&vt;
      if constexpr (last && FN == 3 && n == GLP) __builtin_amdgcn_s_setprio(EPR);
      if constexpr (n < X && n % 4 == 0) asm volatile("s_waitcnt lgkmcnt(%0)" ::"n"(n < 12 ? 12 : 8) : "memory");
      if constexpr (n == X) {
        asm volatile("s_waitcnt vmcnt(%0) lgkmcnt(0)" ::"n"(last ? 63 : 4) : "memory");
        __builtin_amdgcn_s_barrier();
      }
      if constexpr (n == L && !last) {
        wait_vm<4>();
        __builtin_amdgcn_s_barrier();
      }
      if constexpr (FN == 4 && n == TPOS && !LAST.value) {
        if (__builtin_expect((tmask >> (u + h)) & 1u, 0))
          touch8k(smem_base + QH_OFF, vt, u >= 20 ? rsD : rsW, u >= 20 ? (u - 20 + wm) * 4096u : (u + 8u) << 18);
      }
      if constexpr (LAST.value && h == 0 && n == 10) {
        const d4::WSrc w = d4::wsrc(Wd, wm);
        st.rA = st.rB = w.rs; st.vA = st.vB = w.v;
        st.sA = w.f[0] + (4 + wn * 14) * d4::FR; st.sB = st.sA + 28 * d4::FR; st.dA = st.dB = d4::FR;
        st.e3 = 7 * d4::FR; st.e1 = st.e3 - 1024;
      }
      MFMA_AV(acc[i][j], (u32x4{bl[k][j][0], bl[k][j][1], bh[k][j][0], bh[k][j][1]}), a[k][i]);
      if constexpr (n < 8) rdb(HH, ic<FN - 1>{}, ic<n & 7>{});
      if constexpr (last) {
        if constexpr (n > X && (n - X - 1) % GDS == 0 && (n - X - 1) / GDS < 4) G3_STAGE(2, (n - X - 1) / GDS);
      } else {
        if constexpr (n >= L) tail(ic<h ^ 1>{}, ic<n & 15>{});
        if constexpr (n >= 16 && n < L && (n & 15) < 8) rdb(ic<h ^ 1>{}, ic<(n >> 4) - 1>{}, ic<n & 7>{});
        constexpr int d = n >= L ? 4 * GDS + n - L - 1 : (n - 17 < 4 * GDS ? n - 17 : -1);
        if constexpr (d >= 0 && d % GDS == 0 && d / GDS < 8) G3_STAGE(2 * h + ((d / GDS >> 1) & 1), (d / GDS & 1) + (d / GDS < 4 ? 2 : 0));
      }
    });
  };
  static_for<8 * (FN - 2)>([&](auto RR) { rdb(ic<0>{}, ic<RR.value / 8>{}, ic<RR.value & 7>{}); });
#pragma unroll
  for (int j = 0; j < FN; ++j) asm volatile("" : "+v"(bad[1][j]));
  asm volatile("" : "+v"(aad[0]), "+v"(aad[1]), "+v"(vt), "+s"(st.sA), "+s"(st.sB) : "s"(rsD));
  wait_vm<4>();
  __builtin_amdgcn_s_barrier();
  STAMPDECL;
  STAMPNW(5, 1);
  static_for<16>([&](auto RR) { tail(ic<0>{}, RR); });
  for (int u = 0; u < 28; u += 4) {
    period(ic<0>{}, ic<0>{}, u);
    period(ic<1>{}, ic<0>{}, u);
  }
  const int J0 = FN == 4 ? (24 + bn) * 4 : (bn >> 2) * 12 + (bn & 3);
  const rsrc_t rsS = mk_rsrc(ws + WS_ACT + ((bm * 4 + wm) * (F_DIM / 32) + (J0 >> 1)) * 4096 + (J0 & 1) * 512, 20480);
  unsigned vs = (fq & 1) * 1024 + ((fq >> 1) * 16 + fr) * 16;
  asm volatile("" : "+v"(vs) : "s"(rsS));
  period(ic<0>{}, ic<1>{}, 28);
  period(ic<1>{}, ic<1>{}, 28);
  asm volatile("s_nop 15" ::: "memory");
#pragma unroll
  for (int j = 0; j < NB; ++j) asm volatile("" : "+v"(acc[0][j]), "+v"(acc[1][j]), "+v"(acc[2][j]), "+v"(acc[3][j]));
  SCHED0();
  STAMP(5, 2);
  STAMPT(3, 5, 256);
  STAMPFLUSH(5, 1);
  if constexpr (FN == 3 && GLP >= NM) __builtin_amdgcn_s_setprio(EPR);
  static_for<FN>([&](auto JJ) {
    constexpr int j = JJ.value;
    (void)&vs; (void)&rsS;
    u32x2 pk[4];
    {
      f32x2 e[8], gu[8];
#pragma unroll
      for (int g = 0; g < 8; ++g) e[g] = ((g & 1) ? hi2(acc[g >> 1][j]) : lo2(acc[g >> 1][j])) * -1.4426950408889634f;
      SCHED0();
#pragma unroll
      for (int g = 0; g < 8; ++g) e[g] = f32x2{__builtin_amdgcn_exp2f(e[g][0]), __builtin_amdgcn_exp2f(e[g][1])};
      SCHED0();
#pragma unroll
      for (int g = 0; g < 8; ++g) e[g] = e[g] + 1.0f;
      SCHED0();
#pragma unroll
      for (int g = 0; g < 8; ++g) e[g] = f32x2{__builtin_amdgcn_rcpf(e[g][0]), __builtin_amdgcn_rcpf(e[g][1])};
      SCHED0();
#pragma unroll
      for (int g = 0; g < 8; ++g) gu[g] = (g & 1) ? hi2(acc[g >> 1][j]) * hi2(acc[g >> 1][FN + j]) : lo2(acc[g >> 1][j]) * lo2(acc[g >> 1][FN + j]);
      SCHED0();
#pragma unroll
      for (int g = 0; g < 8; ++g) gu[g] = gu[g] * e[g];
      SCHED0();
#pragma unroll
      for (int g = 0; g < 8; ++g) pk[g >> 1][g & 1] = pk_bf16(gu[g][0], gu[g][1]);
    }
#pragma unroll
    for (int i = 0; i < 4; i += 2) {
      const auto s0 = __builtin_amdgcn_permlane16_swap(pk[i][0], pk[i + 1][0], false, false);
      const auto s1 = __builtin_amdgcn_permlane16_swap(pk[i][1], pk[i + 1][1], false, false);
      asm volatile("buffer_store_dwordx4 %0, %1, %2, %3 offen offset:%4 sc1\n\ts_nop 1" ::"v"(u32x4{s0[0], s1[0], s0[1], s1[1]}), "v"(vs), "s"(rsS),
                   "s"(FN == 4 ? (j >> 1) * 4096 : j * 8192), "i"(i * 1024 + (FN == 4 ? (j & 1) * 512 : 0)) : "memory");
    }
  });
  STAMP(5, 3);
  STAMPT(5, 5, 256);
}
DI unsigned flag_off(unsigned b, unsigned wid) {
  const unsigned xcd = b & 7, idx = b >> 3;
  return ((idx & 7) * 2 + ((wid >> 1) & 1)) * 512 + (xcd * 4 + (idx >> 3)) * 16 + (wid & 1) * 8 + (wid >> 2) * 4;
}
DI void act_flag(char* ws, int lane, int wid, unsigned tag) {
  asm volatile("buffer_store_dword %0, %1, %2, 0 offen sc1" ::"v"(tag), "v"(lane * 8192), "s"(mk_rsrc(ws + WS_FA + flag_off(blockIdx.x, wid), 7 * 8192 + 4)) : "memory");
}

#define DPS (N - 3)
#define DFS 0
#define DSL 4
#define DLA 4
#define DMX 3
#define LPR 0
namespace d4 {
struct Sched { int tA[N], tW[N], eA[N], eW[N], eB, cnt[N + 2], last[N], tL; };
constexpr Sched sched() {
  Sched q = {};
  unsigned fr = 7u << 6;
  int nA = 0, nW = 6, c = 0;
  auto take = [&]() { int e = 0; while (!((fr >> e) & 1)) ++e; fr &= ~(1u << e); return e; };
  for (int s = 0; s < 6; ++s) { q.tW[s] = -1; q.eW[s] = s; }
  for (int t = 0; t <= N; ++t) {
    q.cnt[t] = c;
    if (nA >= NE - 2 && !q.tL) { q.tL = t; c += 1; }
    if (t == 1) fr |= 1u << 9;
    if (t >= 2) fr |= (1u << q.eA[t - 2]) | (1u << q.eW[t - 2]);
    for (int m = 0; m < DMX && fr; ++m) {
      const bool a = nA <= nW;
      const int s = a ? nA : nW;
      if (s >= N || s > t + DLA) break;
      if (a) { q.tA[s] = t; q.eA[s] = take(); c += s < NF ? 4 : 2; ++nA; } else { q.tW[s] = t; q.eW[s] = take(); c += 4; ++nW; }
      q.last[s] = c;
    }
    if (t == N - 1) q.eB = take();
  }
  q.cnt[N + 1] = c;
  return q;
}
constexpr Sched Q = sched();
template <int M, int S, int W = 0>
DI void dma(unsigned m0b, unsigned voff, rsrc_t rs, unsigned so) {
  unsigned t;
  if constexpr (W)
    asm volatile("s_add_u32 m0, %1, %5\n\ts_add_u32 %0, %4, %6\n\tbuffer_load_dwordx4 %2, %3, %0 offen lds"
                 : "=&s"(t) : "s"(m0b), "v"(voff), "s"(rs), "s"(so), "n"(M), "n"(S) : "memory", "scc");
  else
    asm volatile("s_add_u32 m0, %1, %5\n\ts_add_u32 %0, %4, %6\n\tbuffer_load_dwordx4 %2, %3, %0 offen sc1 lds"
                 : "=&s"(t) : "s"(m0b), "v"(voff), "s"(rs), "s"(so), "n"(M), "n"(S) : "memory", "scc");
}
}
DI void d4_loader(char* smem, unsigned smem_base, int lane, int j, const bf16* Wd, char* ws, unsigned tag) {
  using namespace d4;
  wait_vm<0>();
  act_flag(ws, lane, 4 + j, tag);
  if (blockIdx.x == 0 && j == 3) asm volatile("buffer_store_dword %0, %1, %2, 0 offen sc1" ::"v"(tag), "v"(lane * 4), "s"(mk_rsrc(ws, 4)) : "memory");
  STAMPT(5, 6, 256);
  __builtin_amdgcn_s_setprio(LPR);
  const Tile t = tile();
  const rsrc_t rsA = mk_rsrc(ws + WS_ACT + t.bm * 917504, 917504);
  const WSrc w = wsrc(Wd, j);
  unsigned vA = lane * 16 + j * 1024;
  const unsigned mA = smem_base + j * 1024, mW = smem_base + j * 2048;
  unsigned sO[3] = {t.kh * (458752u + NE * 8192), t.kh * (458752u + (44 - NF) * 8192), t.kh * 458752u};
  unsigned sP[2] = {458752u - t.kh * (458752u - NE * 8192), 458752u - t.kh * (458752u - (44 - NF) * 8192)};
  asm volatile("" : "+v"(vA));
  asm volatile("" : "+s"(sO[0]), "+s"(sO[1]), "+s"(sO[2]), "+s"(sP[0]), "+s"(sP[1]));
  const rsrc_t rsF = mk_rsrc(ws + WS_FA + t.bn * 8192, 8192);
  if (j == 0) {
    if (DSL) __builtin_amdgcn_s_sleep(DSL);
    unsigned spins = 0, f;
    do {
      asm volatile("buffer_load_dword %0, %1, %2, %3 offen offset:4 sc1\n\ts_waitcnt vmcnt(0)" : "=v"(f) : "v"((lane & 31) * 8), "s"(rsF), "s"(t.bm * 512 + t.kh * 256) : "memory");
    } while (!__all(f == tag) && ++spins < (1u << 18));
    dsw32<768>(smem_base + QT_OFF, 1u);
  } else {
    unsigned n;
    do {
      asm volatile("s_sleep 1\n\tds_read_b32 %0, %1 offset:768\n\ts_waitcnt lgkmcnt(0)" : "=v"(n) : "v"(smem_base + QT_OFF) : "memory");
    } while (__builtin_amdgcn_readfirstlane(n) == 0u);
  }
  STAMPT(6, 0, 256);
  unsigned fl;
  const int cn = t.bn * 2 + ((lane >> 2) & 1);
  const unsigned vF = lane < 2 * (44 - NF) ? t.bm * 512 + t.kh * ((NF - NE) * 16) + lane * 8
                    : (lane < 56 ? flag_off(blockIdx.x ^ 1, 4 + (lane & 3)) : flag_off(((cn & 7) * 4 + (t.bm & 3)) * 8 + (t.bm >> 2) * 2 + (cn >> 3), t.kh * 2 + (lane & 1) + (lane & 2) * 2));
  static_for<N>([&](auto T) {
    constexpr int i = T.value;
    (void)&fl;
    if constexpr (i == Q.tL) { asm volatile("" : "=v"(fl)); bload1_sc1(fl, vF, rsF); }
    if constexpr (i == Q.tA[NE]) {
      asm volatile("s_waitcnt vmcnt(%1)" : "+v"(fl) : "n"(Q.cnt[i] - Q.cnt[Q.tL] - 1) : "memory");
      if (__builtin_expect(!__all(fl == tag), 0)) {
        unsigned spins = 0, f;
        do {
          asm volatile("buffer_load_dword %0, %1, %2, 0 offen sc1\n\ts_waitcnt vmcnt(0)" : "=v"(f) : "v"(vF), "s"(rsF) : "memory");
        } while (!__all(f == tag) && ++spins < (1u << 18));
      }
    }
    static_for<N>([&](auto S) {
      constexpr int s = S.value;
      if constexpr (Q.tA[s] == i) {
        static_for<2>([&](auto K) {
          constexpr int k = K.value, m = Q.eA[s] * 16384 + k * 8192;
          if constexpr (s < NF) dma<m, (apos(s) * 2 + k) * 4096>(mA, vA, rsA, sP[cls(s) & 1]);
          dma<m + 4096, (apos(s) * 2 + k) * 4096>(mA, vA, rsA, sO[cls(s)]);
        });
      }
      if constexpr (Q.tW[s] == i) {
        static_for<4>([&](auto K) {
          constexpr int k = K.value;
          dma<Q.eW[s] * 16384 + (k >> 1) * 8192 + (k & 1) * 1024, wpos(s, k) * FR, 1>(mW, w.v, w.rs, w.f[cls(s)]);
        });
      }
    });
    wait_vm<Q.cnt[i + 1] - Q.last[i]>();
    __builtin_amdgcn_s_barrier();
  });
}
DI bool missing(const u32x4 (&pv)[4]) {
  unsigned m = 0;
#pragma unroll
  for (int f = 0; f < 4; ++f) m = max(max(m, max(pv[f][0], pv[f][1])), max(pv[f][2], pv[f][3]));
  return __any(m == ~0u);
}
DI void d4_mfma(char* smem, unsigned smem_base, int lane, int w, bf16* out, char* ws, unsigned tag) {
  using namespace d4;
  const Tile t = tile();
  const int fr = lane & 15, fq = lane >> 4, wr = w & 1, wc = w >> 1;
  unsigned brd[3][4], ard[3], vL = lane * 16;
  {
    const int q = (lane >> 2) & 3, p = lane & 3, g = (q >> 1) | ((fq & 1) << 1);
#pragma unroll
    for (int i = 0; i < 3; ++i) {
#pragma unroll
      for (int f = 0; f < 4; ++f) {
        brd[i][f] = smem_base + i * 65536 + wc * 4096 + (fq & 1) * 2048 + (fq >> 1) * 1024 + q * 128 + ((f ^ g) * 32) + 8 * p;
        asm volatile("" : "+v"(brd[i][f]));
      }
      ard[i] = smem_base + i * 65536 + wr * 2048 + lane * 16;
      asm volatile("" : "+v"(ard[i]));
    }
  }
  asm volatile("" : "+v"(vL));
  f32x4 acc[4][4];
#pragma unroll
  for (int i = 0; i < 4; ++i)
#pragma unroll
    for (int f = 0; f < 4; ++f) { acc[i][f] = f32x4{0, 0, 0, 0}; asm volatile("" : "+v"(acc[i][f])); }
  const rsrc_t rsX = mk_rsrc(ws + xoff(blockIdx.x) + w * 4096, 4096), rsP = mk_rsrc(ws + xoff(blockIdx.x ^ 1) + w * 4096, 4096);
  const unsigned bo = Q.eB * 16384 + w * 4096;
  unsigned tw[4], tr = smem_base + bo + (lane >> 3) * 128 + (((lane & 7) ^ (lane >> 3)) * 16);
  const rsrc_t rsO = mk_rsrc((char*)out + ((t.bm * 2 + t.kh) * 64 + wr * 32) * (D_DIM * 2) + t.bn * 256 + wc * 128, 31 * D_DIM * 2 + 128);
  unsigned vO = (lane >> 3) * (D_DIM * 2) + (lane & 7) * 16;
#pragma unroll
  for (int f = 0; f < 4; ++f) tw[f] = smem_base + bo + fr * 128 + (((f * 2 + (fq >> 1)) ^ (fr & 7)) * 16) + (fq & 1) * 8;
  asm volatile("" : "+v"(tw[0]), "+v"(tw[1]), "+v"(tw[2]), "+v"(tw[3]), "+v"(tr), "+v"(vO) : "s"(rsO));
  __builtin_amdgcn_s_setprio(2);
  u32x4 gt[4], xq[4], ex, pv[4];
  f32x2 base[16];
  const rsrc_t rsR = mk_rsrc(ws + WS_X2 + (((t.bm * 2 + t.kh) * 4 + wr * 2) * 16 + t.bn * 2 + wc) * 2048, 32768 + 2048);
  unsigned k32 = 32768;
  asm volatile("" : "+s"(k32) : "s"(rsR));
  static_for<4>([&](auto F) { dsr128<1024 + F.value * 64>(gt[F.value], smem_base + QT_OFF + wc * 256 + fq * 16); });
  asm volatile("s_waitcnt vmcnt(8) lgkmcnt(0)" ::: "memory");
  STAMP(5, 4);
  __builtin_amdgcn_s_barrier();
  STAMPDECL;
  STAMPNW(6, 1);
  u32x4 a[2][2][4];
  u32x2 bl[2][2][4], bh[2][2][4];
  auto rd = [&](auto S, auto RR) {
    constexpr int s = S.value, per = s < NF ? 12 : 10, k = RR.value / per, r = RR.value % per;
    if constexpr (r < 8) {
      constexpr int e = Q.eW[s], off = (e & 3) * 16384 + k * 8192 + (r & 1) * 512;
      if constexpr (r & 1) dsr_tr<off>(bh[s & 1][k][r >> 1], brd[e >> 2][r >> 1]);
      else dsr_tr<off>(bl[s & 1][k][r >> 1], brd[e >> 2][r >> 1]);
    } else {
      constexpr int e = Q.eA[s], i = s < NF ? r - 8 : r - 6;
      dsr128<(e & 3) * 16384 + k * 8192 + (i >> 1) * 4096 + (i & 1) * 1024>(a[s & 1][k][i], ard[e >> 2]);
    }
  };
  static_for<24>([&](auto RR) { rd(ic<0>{}, RR); });
  static_for<N>([&](auto S) {
    constexpr int s = S.value, nm = s < NF ? 32 : 16, nr = s + 1 == N ? 0 : (s + 1 < NF ? 24 : 20), pg = s < NF ? 1 : 2;
    WAIT_LGKM0();
    u32x4 bb[2][4];
#pragma unroll
    for (int k = 0; k < 2; ++k)
#pragma unroll
      for (int f = 0; f < 4; ++f) bb[k][f] = u32x4{bl[s & 1][k][f][0], bl[s & 1][k][f][1], bh[s & 1][k][f][0], bh[s & 1][k][f][1]};
    SCHED0();
    static_for<nm>([&](auto M) {
      constexpr int n = M.value;
      (void)&base; (void)&xq; (void)&rsR; (void)&k32; (void)&rsX; (void)&vL; (void)&ex; (void)&pv; (void)&rsP;
      if constexpr (n == 2 && s + 1 < N) { __builtin_amdgcn_s_barrier(); asm volatile(".p2align 3"); }
      {
        constexpr int p = n >> 1, k = (n ^ p) & 1, f = s < NF ? p >> 2 : p >> 1, i = s < NF ? ((f & 1) ? 3 - (p & 3) : p & 3) : 2 + ((p ^ f) & 1);
        MFMA_AV(acc[i][f], bb[k][f], a[s & 1][k][i]);
      }
      static_for<pg>([&](auto G) {
        constexpr int r = (n - 2) * pg + G.value;
        if constexpr (n >= 2 && r < nr) rd(ic<(s + 1 < N ? s + 1 : 0)>{}, ic<(r >= 0 && r < nr ? r : 0)>{});
      });
      if constexpr (s == DFS && n == 28) { wait_vm<0>(); act_flag(ws, lane, w, tag); }
      if constexpr (s == NE + 1 && n >= 28) { asm volatile("" : "=v"(xq[n & 3])); bload_sc1<(n & 1) * 1024>(xq[n & 3], vL, rsR, (n & 2) ? k32 : 0u); }
      if constexpr (s == NF - 1 && n == 28) wait_vm<0>();
      if constexpr ((s == NF || s == NF + 1) && n >= 4 && n < 14) {
        constexpr int i = s - NF, p = (n - 4) / 5, h = (n - 4) % 5;
        if constexpr (h < 4) {
          asm volatile("v_cvt_pk_bf16_f32 %0, %1, %2\n\tv_min_u32 %0, -2, %0" : "=v"(ex[h]) : "v"(acc[i][2 * p + (h >> 1)][2 * (h & 1)]), "v"(acc[i][2 * p + (h >> 1)][2 * (h & 1) + 1]));
        } else {
          asm volatile("buffer_store_dwordx4 %0, %1, %2, 0 offen offset:%3 sc1" ::"v"(ex), "v"(vL), "s"(rsX), "i"(i * 2048 + p * 1024) : "memory");
        }
      }
      if constexpr (s == DPS && n >= 12) { asm volatile("" : "=v"(pv[n & 3])); bload_sc1<(n & 3) * 1024>(pv[n & 3], vL, rsP, 0); }
      if constexpr (s + 1 == N) base[n] = unpk_v(xq[n >> 2][n & 3]);
    });
    SCHED0();
  });
  asm volatile("s_nop 7\n\ts_waitcnt vmcnt(0) lgkmcnt(0)" : "+v"(gt[0]), "+v"(gt[1]), "+v"(gt[2]), "+v"(gt[3]), "+v"(pv[0]), "+v"(pv[1]), "+v"(pv[2]), "+v"(pv[3])::"memory");
  if (__builtin_expect(missing(pv), 0)) {
    unsigned spins = 0;
    do {
      asm volatile("buffer_load_dwordx4 %0, %4, %5, 0 offen sc1\n\tbuffer_load_dwordx4 %1, %4, %5, 0 offen offset:1024 sc1\n\t"
                   "buffer_load_dwordx4 %2, %4, %5, 0 offen offset:2048 sc1\n\tbuffer_load_dwordx4 %3, %4, %5, 0 offen offset:3072 sc1\n\t"
                   "s_waitcnt vmcnt(0)"
                   : "=&v"(pv[0]), "=&v"(pv[1]), "=&v"(pv[2]), "=&v"(pv[3]) : "v"(vL), "s"(rsP) : "memory");
    } while (missing(pv) && ++spins < (1u << 18));
  }
  STAMP(6, 2); STAMPFLUSH(6, 1);
  u32x4 ov[4];
  static_for<8>([&](auto E) {
    constexpr int e = E.value, i = e >> 2, f = e & 3;
    asm volatile("" : "+v"(acc[2 + i][f]));
    const unsigned p0 = pv[e >> 1][(e & 1) * 2], p1 = pv[e >> 1][(e & 1) * 2 + 1];
    const f32x4 v = f32x4{base[2 * e][0], base[2 * e][1], base[2 * e + 1][0], base[2 * e + 1][1]} + __builtin_bit_cast(f32x4, gt[f]) * (acc[2 + i][f] + f32x4{bf_lo(p0), bf_hi(p0), bf_lo(p1), bf_hi(p1)});
    asm volatile("ds_write_b64 %0, %1 offset:%2" ::"v"(tw[f]), "v"(u32x2{pk_bf16(v[0], v[1]), pk_bf16(v[2], v[3])}), "i"(i * 2048) : "memory");
    if constexpr (f == 3) { dsr128<i * 2048>(ov[2 * i], tr); dsr128<i * 2048 + 1024>(ov[2 * i + 1], tr); }
    if constexpr (e == 6) {
      asm volatile("s_waitcnt lgkmcnt(3)" : "+v"(ov[0]), "+v"(ov[1])::"memory");
      gstore4_sc1(ov[0], vO, rsO, 0); gstore4_sc1(ov[1], vO, rsO, 8 * D_DIM * 2);
    }
  });
  asm volatile("s_waitcnt lgkmcnt(0)" : "+v"(ov[2]), "+v"(ov[3])::"memory");
  gstore4_sc1(ov[2], vO, rsO, 16 * D_DIM * 2); gstore4_sc1(ov[3], vO, rsO, 24 * D_DIM * 2);
  STAMP(6, 3);
  STAMP(6, 4);
}

DI void g3_phase(char* smem, unsigned smem_base, int tid, int wid, const bf16* __restrict__ Wg, const bf16* __restrict__ Wu,
                 const bf16* __restrict__ Wd, bf16* out, char* ws, int own, unsigned tag) {
  const int lane = tid & 63;
  const int wm = wid & 3, wn = wid >> 2;
  const int xcd = blockIdx.x & 7, idx = blockIdx.x >> 3;
  const int bm = idx & 7, bn = xcd * 4 + (idx >> 3);
  const int n0 = bn * 112, n0a = n0 & ~31;
  unsigned* yflags = (unsigned*)(ws + WS_FY);
  const rsrc_t rsW = mk_rsrc((const char*)(wm & 1 ? Wu : Wg) + blockIdx.x * 4096, D_DIM * F_DIM * 2 - blockIdx.x * 4096);
  Stg st;
  st.rA = mk_rsrc(ws + WS_Y + (size_t)bm * 32 * 16384, 32 * 16384);
  st.rB = mk_rsrc(wn ? Wu : Wg, D_DIM * F_DIM * 2);
  st.vA = wid * 2048 + lane * 16;
  {
    const int r = wm * 8 + (lane >> 4);
    const int sw = (r & 3) | (((r >> 3) & 1) << 2);
    const int c16 = lane & 15;
    st.vB = (unsigned)(r * (F_DIM * 2) + (n0a + (((c16 >> 1) ^ sw) * 16) + (c16 & 1) * 8) * 2);
  }
  st.sA = 0; st.sB = 0; st.dA = 16384; st.dB = 229376; st.e1 = 0; st.e3 = 4 * F_DIM * 2;
  const unsigned m0w = smem_base + wid * 2048;
  asm volatile("" : "+s"(out), "+s"(Wd));
  G3_STAGE(0, 2); G3_STAGE(0, 3); G3_STAGE(1, 2); G3_STAGE(1, 3);
  wait_vm<4>();
  __builtin_amdgcn_s_barrier();
  STAMP(3, 4);
  if (wid == 0) asm volatile("buffer_store_dword %0, %1, %2, 0 offen sc1" ::"v"(tag), "v"(lane * (1024 * YFS)), "s"(mk_rsrc(yflags + own * YFS, 7 * 1024 * YFS + 4)) : "memory");
  G3_STAGE(2, 2); G3_STAGE(2, 3); G3_STAGE(3, 2); G3_STAGE(3, 3);
  if (wn == 0) {
    if (wm == 0) {
      const rsrc_t rsF = mk_rsrc(yflags + (xcd * 256 + bm * 32) * YFS, 128 * YFS);
      if (YSL) __builtin_amdgcn_s_sleep(YSL);
      unsigned spins = 0, f;
      do {
        asm volatile("buffer_load_dword %0, %1, %2, 0 offen sc1\n\ts_waitcnt vmcnt(0)" : "=v"(f) : "v"((lane & 31) * (4 * YFS)), "s"(rsF) : "memory");
      } while (!__all(f == tag) && ++spins < (1u << 18));
    }
    wait_vm<4>();
    __builtin_amdgcn_s_barrier();
    STAMP(5, 0);
    G3_STAGE(0, 0); G3_STAGE(0, 1); G3_STAGE(1, 0); G3_STAGE(1, 1);
    G3_STAGE(2, 0); G3_STAGE(2, 1); G3_STAGE(3, 0); G3_STAGE(3, 1);
    g3_body<4>(smem, smem_base, wm, wn, wid, lane, bm, bn, st, rsW, Wd, ws);
    d4_mfma(smem, smem_base, lane_id(), wm, out, ws, tag);
  } else {
    wait_vm<4>();
    __builtin_amdgcn_s_barrier();
    G3_STAGE(0, 0); G3_STAGE(0, 1); G3_STAGE(1, 0); G3_STAGE(1, 1);
    G3_STAGE(2, 0); G3_STAGE(2, 1); G3_STAGE(3, 0); G3_STAGE(3, 1);
    g3_body<3>(smem, smem_base, wm, wn, wid, lane, bm, bn, st, rsW, Wd, ws);
    d4_loader(smem, smem_base, lane_id(), wm, Wd, ws, tag);
  }
}

__global__ __launch_bounds__(512, 2) void block_kernel(const bf16* x_, const bf16* c_, const bf16* wmod_, char* ws_, const bf16* wq_,
                                                       const bf16* wo_, const bf16* wg_, const bf16* wu_, const bf16* __restrict__ wd, bf16* out) {
  __shared__ __attribute__((aligned(16))) char smem[Q_LDS];
  u64x8 ka;
  asm volatile("s_load_dwordx16 %0, %1, 0x0" : "=s"(ka) : "s"(__builtin_amdgcn_kernarg_segment_ptr()));
  STAMP(0, 0);
  const int tid = threadIdx.x;
  const int wid = __builtin_amdgcn_readfirstlane(tid >> 6);
  const unsigned smem_base = (unsigned)(uintptr_t)smem;
  const int xcd = blockIdx.x & 7, idx = blockIdx.x >> 3;
  {
    unsigned long long pc;
    asm volatile("s_getpc_b64 %0" : "=s"(pc));
    constexpr int CH = (CODESZ + 4095) / 4096 * 128;
    touch8k(smem_base + QH_OFF, tid - 448u < CH / 128 ? (tid - 448) * 128u : 1u << 30, mk_rsrc((const char*)(uintptr_t)(pc & ~255ull), CODESZ), idx * CH);
  }
  const int bm = (xcd >> 2) * 8 + (idx & 7), bn = (xcd & 3) * 4 + (idx >> 3);
  const int cm = (xcd >> 1) * 4 + (idx & 3), cn = __builtin_amdgcn_readfirstlane((xcd & 1) * 8 + (idx >> 2));
  const int lane = tid & 63;
  Pre p;
  {
    const bool gv = ((bm + bn) & 1) == 0;
    unsigned oa = ((bm >> 2) * 4 + (wid & 3)) * 786432 + (bm & 2) * 1024 + (bn ^ 1) * 128, ob = (bm * 128 + (bn >> 1) * 16 + (wid & 3) * 4) * 2048;
    unsigned va = (lane >> 3) * (8 * 12288) + (lane & 7) * 16, vb = lane * 16;
    asm volatile("" : "+s"(oa), "+s"(ob));
    asm volatile("" : "+v"(va), "+v"(vb));
    p.o1 = gv ? oa : ob; p.n1 = gv ? 63 * 12288 + 128 : 8192; p.vo = gv ? va : vb; p.st = gv ? 12288 : 1024;
    dsw32<0>(smem_base + QT_OFF, 0u);
    dsw32<4>(smem_base + QT_OFF, 0u);
    dsw32<768>(smem_base + QT_OFF, 0u);
    asm volatile("" : "+v"(p.vo));
    asm volatile("s_waitcnt lgkmcnt(0)" : "+s"(ka), "+s"(p.o1), "+s"(p.n1), "+s"(p.st));
  }
  asm volatile("s_cmp_lt_u32 %0, 4\n\ts_cbranch_scc1 1\n\ts_barrier" ::"s"(wid) : "scc", "memory");
  const Args g = {(const bf16*)ka[0], (const bf16*)ka[1], (const bf16*)ka[2], (const bf16*)ka[4], (const bf16*)ka[5], (const bf16*)ka[6], (const bf16*)ka[7], (char*)ka[3]};
  char* ws = g.ws;
  const bf16 *x = g.x, *wg = g.wg, *wu = g.wu;
  bf16* O = (bf16*)(ws + WS_O);
  bf16* Y = (bf16*)(ws + WS_Y);
  unsigned* oflags = (unsigned*)(ws + WS_FO);
  float* stat = (float*)(ws + WS_STAT);
  unsigned tag = 0;
  if (__builtin_expect(wid < 4, 1)) {
    __builtin_amdgcn_s_setprio(2);
    tag = front1(smem, smem_base, lane, wid, bm, bn, g, p);
    front_h(smem, smem_base, lane, wid, bm, bn, g, tag);
    qkv_mfma(smem, smem_base, tid, wid, bm * 128, bn * 64, O, oflags, tag);
  } else if (wid < 7) {
    tag = qkv_loader(smem, smem_base, lane, wid - 4, bm, bn, cn, g);
  } else {
    tag = qkv_spare(smem, smem_base, lane, bm, bn, cn, g);
  }
  STAMPT(2, 6, 320);
  STAMPT(3, 0, 448);
  o_phase(smem, smem_base, tid & 63, wid, cm, cn, O, oflags, x, Y, stat, ws, tag);
  g3_phase(smem, smem_base, tid, wid, wg, wu, wd, out, ws, cm * 16 + cn, tag);
}

extern "C" void kernel_launch(void* const* d_in, const int* in_sizes, int n_in, void* d_out, int out_size,
                              void* d_ws, size_t ws_size, hipStream_t stream) {
  const bf16* x = (const bf16*)d_in[0];
  const bf16* wqkv = (const bf16*)d_in[1];
  const bf16* wo = (const bf16*)d_in[2];
  const bf16* wg = (const bf16*)d_in[4];
  const bf16* wu = (const bf16*)d_in[5];
  const bf16* wd = (const bf16*)d_in[6];
  const bf16* c = (const bf16*)d_in[7];
  const bf16* wmod = (const bf16*)d_in[8];
  bf16* out = (bf16*)d_out;

  char* ws = (char*)d_ws;
  block_kernel<<<256, 512, 0, stream>>>(x, c, wmod, ws, wqkv, wo, wg, wu, wd, out);
}
```
